```python
import torch

def _softmax(x, axis=-1):
    return torch.softmax(x, dim=axis)
def _gelu(x):
    return torch.nn.functional.gelu(x, approximate="tanh")
def _silu(x):
    return torch.nn.functional.silu(x)
def _sigmoid(x):
    return torch.sigmoid(x)
def _layernorm(x, axis=-1, eps=1e-5):
    m = x.mean(dim=axis, keepdim=True)
    v = x.var(dim=axis, keepdim=True, unbiased=False)
    return (x - m) / torch.sqrt(v + eps)
def _scatter_add(tgt, idx, src, axis=0):
    return tgt.clone().index_add_(axis, idx, src)
def _segment_sum(src, seg, n):
    out = torch.zeros((n,) + src.shape[1:], dtype=src.dtype)
    return out.index_add_(0, seg, src)
def _pad(x, pw):
    flat = tuple(v for pair in reversed(pw) for v in pair)
    return torch.nn.functional.pad(x, flat)

kernel_name = 'sequential_preLN__spectral_fnet__swiglu_ffn__prompt'

def setup_inputs():

    _w = (lambda a: (lambda t: t.to(torch.bfloat16) if t.is_floating_point() else t)(torch.as_tensor(a)))
    def _A_setup():
        B, S, D = 1, 2048, 576
        ns = torch.arange(S)[:, None] * torch.arange(S)[None, :]
        nd = torch.arange(D)[:, None] * torch.arange(D)[None, :]
        Ws_re = torch.cos(-2.0 * torch.pi * ns / S).to(float)
        Ws_im = torch.sin(-2.0 * torch.pi * ns / S).to(float)
        Wd_re = torch.cos(-2.0 * torch.pi * nd / D).to(float)
        Wd_im = torch.sin(-2.0 * torch.pi * nd / D).to(float)
        return {
            "x": _w((lambda *s: torch.randn(*s, dtype=torch.bfloat16) * 0.05)(B, S, D)),
            "dft_seq_re": _w(Ws_re),
            "dft_seq_im": _w(Ws_im),
            "dft_hid_re": _w(Wd_re),
            "dft_hid_im": _w(Wd_im),
        }
    _A_in = _A_setup()

    def _B_setup():
        B, S, D, F = 1, 2048, 576, 4608
        return {
            "x": _w((lambda *s: torch.randn(*s, dtype=torch.bfloat16) * 0.05)(B, S, D)),
            "w_gate": _w((lambda *s: torch.randn(*s, dtype=torch.bfloat16) * 0.2)(D, F)),
            "w_up": _w((lambda *s: torch.randn(*s, dtype=torch.bfloat16) * 0.2)(D, F)),
            "w_down": _w((lambda *s: torch.randn(*s, dtype=torch.bfloat16) * 0.2)(F, D)),
        }
    _B_in = _B_setup()

    return {
        "A_x": _A_in["x"],
        "A_dft_seq_re": _A_in["dft_seq_re"],
        "A_dft_seq_im": _A_in["dft_seq_im"],
        "A_dft_hid_re": _A_in["dft_hid_re"],
        "A_dft_hid_im": _A_in["dft_hid_im"],
        "B_x": _B_in["x"],
        "B_w_gate": _B_in["w_gate"],
        "B_w_up": _B_in["w_up"],
        "B_w_down": _B_in["w_down"],
    }

def reference(A_x, A_dft_seq_re, A_dft_seq_im, A_dft_hid_re, A_dft_hid_im, B_x, B_w_gate, B_w_up, B_w_down):
    def _A_ref(x, dft_seq_re, dft_seq_im, dft_hid_re, dft_hid_im):
        a = torch.einsum("bsd,ed->bse", x, dft_hid_re)
        b = torch.einsum("bsd,ed->bse", x, dft_hid_im)
        y = torch.einsum("ts,bse->bte", dft_seq_re, a) - torch.einsum("ts,bse->bte", dft_seq_im, b)
        return y

    def _B_ref(x, w_gate, w_up, w_down):
        g = _silu(x @ w_gate)
        u = x @ w_up
        return (g * u) @ w_down

    x = A_x
    h = x + _A_ref(_layernorm(x, axis=-1), A_dft_seq_re, A_dft_seq_im, A_dft_hid_re, A_dft_hid_im)
    h2 = h + _B_ref(_layernorm(h, axis=-1), B_w_gate, B_w_up, B_w_down)
    return h2
```

```cpp
#include <hip/hip_runtime.h>
#include <hip/hip_bf16.h>
typedef unsigned short u16;
typedef short bf16x8 __attribute__((ext_vector_type(8)));
typedef float f32x4 __attribute__((ext_vector_type(4)));
constexpr int S=2048, D=576, F=4608;
__device__ inline float bf2f(u16 v){ return __uint_as_float(((unsigned)v)<<16); }
__device__ inline u16 f2bf(float f){ unsigned u=__float_as_uint(f); u += 0x7FFFu + ((u>>16)&1u); return (u16)(u>>16); }

template<typename T> __device__ inline float ld(const T* p, int i);
template<> __device__ inline float ld<u16>(const u16* p, int i){ return bf2f(p[i]); }
template<> __device__ inline float ld<float>(const float* p, int i){ return p[i]; }
template<typename T>
__global__ void k_ln(const T* in, u16* out){
  int row=blockIdx.x, lane=threadIdx.x; float v[9]; float s=0;
  for(int i=0;i<9;i++){ v[i]=ld<T>(in,row*D+lane+64*i); s+=v[i]; }
  for(int o=32;o;o>>=1) s+=__shfl_xor(s,o); float m=s/D; float q=0;
  for(int i=0;i<9;i++){ float d=v[i]-m; q+=d*d; }
  for(int o=32;o;o>>=1) q+=__shfl_xor(q,o); float r=1.0f/sqrtf(q/D+1e-5f);
  for(int i=0;i<9;i++) out[row*D+lane+64*i]=f2bf((v[i]-m)*r);
}
template<bool BT, int MODE>
__global__ void k_gemm(const u16* A, int lda, const u16* B, int ldb, const u16* B2, int K, void* C, int ldc, const void* R, float sign, int accum){
  int lane=threadIdx.x&63, w=threadIdx.x>>6; int n0=(blockIdx.x*4+w)*16, m0=blockIdx.y*16; int fr=lane&15,fq=lane>>4;
  f32x4 acc={0,0,0,0}, acc2={0,0,0,0};
  for(int k0=0;k0<K;k0+=32){
    bf16x8 a=*(const bf16x8*)(A+(size_t)(m0+fr)*lda+k0+fq*8); bf16x8 b,b2;
    if(BT){ b=*(const bf16x8*)(B+(size_t)(n0+fr)*ldb+k0+fq*8); }
    else { for(int j=0;j<8;j++) b[j]=B[(size_t)(k0+fq*8+j)*ldb+n0+fr]; if(MODE==2) for(int j=0;j<8;j++) b2[j]=B2[(size_t)(k0+fq*8+j)*ldb+n0+fr]; }
    acc=__builtin_amdgcn_mfma_f32_16x16x32_bf16(a,b,acc,0,0,0);
    if(MODE==2) acc2=__builtin_amdgcn_mfma_f32_16x16x32_bf16(a,b2,acc2,0,0,0);
  }
  for(int j=0;j<4;j++){ size_t r=m0+fq*4+j, c=n0+fr;
    if(MODE==0) ((u16*)C)[r*ldc+c]=f2bf(acc[j]);
    if(MODE==1){ float* Cf=(float*)C; float p = accum? Cf[r*ldc+c] : bf2f(((const u16*)R)[r*ldc+c]); Cf[r*ldc+c]=p+sign*acc[j]; }
    if(MODE==2){ float g=acc[j]; g=g/(1.0f+__expf(-g)); ((u16*)C)[r*ldc+c]=f2bf(g*acc2[j]); }
    if(MODE==3){ ((u16*)C)[r*ldc+c]=f2bf(((const float*)R)[r*ldc+c]+acc[j]); }
  }
}
extern "C" void kernel_launch(void* const* d_in, const int* in_sizes, int n_in, void* d_out, int out_size, void* d_ws, size_t ws_size, hipStream_t stream){
  const u16* x=(const u16*)d_in[0]; const u16* Cr=(const u16*)d_in[1]; const u16* Ci=(const u16*)d_in[2]; const u16* Wr=(const u16*)d_in[3]; const u16* Wi=(const u16*)d_in[4];
  const u16* wg=(const u16*)d_in[6]; const u16* wu=(const u16*)d_in[7]; const u16* wd=(const u16*)d_in[8];
  char* ws=(char*)d_ws;
  u16* xn=(u16*)(ws); u16* a=(u16*)(ws+(4<<20)); u16* b=(u16*)(ws+(8<<20)); float* h=(float*)(ws+(12<<20)); u16* hn=(u16*)(ws+(20<<20)); u16* act=(u16*)(ws+(24<<20));
  k_ln<u16><<<S,64,0,stream>>>(x,xn);
  k_gemm<true,0><<<dim3(S/64,D/16),256,0,stream>>>(Wr,D,xn,D,nullptr,D,a,S,nullptr,1.f,0);
  k_gemm<true,0><<<dim3(S/64,D/16),256,0,stream>>>(Wi,D,xn,D,nullptr,D,b,S,nullptr,1.f,0);
  k_gemm<true,1><<<dim3(D/64,S/16),256,0,stream>>>(Cr,S,a,S,nullptr,S,h,D,x,1.f,0);
  k_gemm<true,1><<<dim3(D/64,S/16),256,0,stream>>>(Ci,S,b,S,nullptr,S,h,D,x,-1.f,1);
  k_ln<float><<<S,64,0,stream>>>(h,hn);
  k_gemm<false,2><<<dim3(F/64,S/16),256,0,stream>>>(hn,D,wg,F,wu,D,act,F,nullptr,1.f,0);
  k_gemm<false,3><<<dim3(D/64,S/16),256,0,stream>>>(act,F,wd,D,nullptr,F,d_out,D,h,1.f,0);
}
```

```cpp
#include <hip/hip_runtime.h>
typedef unsigned short u16;
typedef unsigned u32x4 __attribute__((ext_vector_type(4)));
typedef unsigned u32x8 __attribute__((ext_vector_type(8)));
typedef unsigned u32x2 __attribute__((ext_vector_type(2)));
typedef float f32x4 __attribute__((ext_vector_type(4)));
typedef float f32x2 __attribute__((ext_vector_type(2)));
typedef short bf16x8 __attribute__((ext_vector_type(8)));
__device__ __forceinline__ u32x4 make_srd(const void* p, unsigned bytes) {
  const unsigned long long a = (unsigned long long)p;
  u32x4 r;
  r[0] = __builtin_amdgcn_readfirstlane((unsigned)a);
  r[1] = __builtin_amdgcn_readfirstlane((unsigned)(a >> 32));
  r[2] = bytes; r[3] = 0x00020000u;
  return r;
}
__device__ __forceinline__ u32x4 srd_of(unsigned lo, unsigned hi, unsigned bytes) { u32x4 r; r[0] = lo; r[1] = hi; r[2] = bytes; r[3] = 0x00020000u; return r; }
#define KARG_LOAD8(v, off) u32x8 v; asm volatile("s_load_dwordx8 %0, %1, " #off : "=s"(v) : "s"(__builtin_amdgcn_kernarg_segment_ptr()))
#define KARG_LOAD4(v, off) u32x4 v; asm volatile("s_load_dwordx4 %0, %1, " #off : "=s"(v) : "s"(__builtin_amdgcn_kernarg_segment_ptr()))
#define KARG_LOAD2(v, off) u32x2 v; asm volatile("s_load_dwordx2 %0, %1, " #off : "=s"(v) : "s"(__builtin_amdgcn_kernarg_segment_ptr()))
#define CODE_SRD(srd, B, E) u32x4 srd; { unsigned lo_, hi_, len_; asm volatile("s_getpc_b64 s[60:61]\n s_add_u32 s60, s60, " B "@rel32@lo+4\n s_addc_u32 s61, s61, " B "@rel32@hi+12\n s_and_b32 %2, s60, 127\n s_andn2_b32 %0, s60, 127\n s_mov_b32 %1, s61\n s_add_u32 %2, %2, " E "-" B : "=&s"(lo_), "=&s"(hi_), "=&s"(len_) :: "s60", "s61", "scc"); srd[0] = lo_; srd[1] = hi_; srd[2] = len_; srd[3] = 0x00020000u; }
#define CODE_LABEL(L) asm volatile(L ":"); __builtin_amdgcn_sched_barrier(0)

__device__ __forceinline__ float bf_lo(unsigned v) { return __builtin_bit_cast(float, v << 16); }
__device__ __forceinline__ float bf_hi(unsigned v) { return __builtin_bit_cast(float, v & 0xffff0000u); }
__device__ __forceinline__ unsigned pk_bf16(float a, float b) {
  typedef __bf16 bf2 __attribute__((ext_vector_type(2)));
  typedef float f2 __attribute__((ext_vector_type(2)));
  f2 v = {a, b};
  return __builtin_bit_cast(unsigned, __builtin_convertvector(v, bf2));
}
__device__ __forceinline__ void st_sc1(void* p, u32x4 v) { asm volatile("global_store_dwordx4 %0, %1, off sc1" :: "v"(p), "v"(v) : "memory"); }
__device__ __forceinline__ void st_sc01(void* p, u32x4 v) { asm volatile("global_store_dwordx4 %0, %1, off sc0 sc1" :: "v"(p), "v"(v) : "memory"); }
__device__ __forceinline__ void st_nt(void* p, u32x4 v) { asm volatile("global_store_dwordx4 %0, %1, off nt" :: "v"(p), "v"(v) : "memory"); }
__device__ __forceinline__ unsigned part_off(unsigned R, unsigned c) {
  const unsigned rt = R >> 7, rg = (R >> 6) & 1, rb = (R >> 4) & 3, n = R & 15, hf = c & 1;
  const bool m = c < 64;
  const unsigned db = (c >> 1) & 7;
  const unsigned ct = m ? c >> 4 : (c - 64) >> 1;
  const unsigned ch = m ? db >> 2 : rb >> 1;
  const unsigned idx = m ? 2 * ((rb + 2 * ch) & 3) + ((db >> 1) & 1) : 8;
  const unsigned g = (m ? (db & 1) : (rb & 1)) + 2 * hf;
  return ((((rt * 4 + ct) * 4 + rg + 2 * ch) * 9 + idx) * 64 + n + 16 * g) * 8;
}
__device__ __forceinline__ unsigned row_block(unsigned b) { return (b & 1) * 128 + (b >> 3) * 4 + ((b >> 1) & 3); }

__device__ __forceinline__ unsigned lane_id() { unsigned r; asm volatile("v_mbcnt_lo_u32_b32 %0, -1, 0\n v_mbcnt_hi_u32_b32 %0, -1, %0" : "=v"(r)); return r; }


__device__ __forceinline__ float sum_rows8(float v) {
  v += __builtin_bit_cast(float, __builtin_amdgcn_update_dpp(0, __builtin_bit_cast(int, v), 0x128, 0xf, 0xf, true));
  float a = v, b = v;
  asm volatile("s_nop 1\n v_permlane16_swap_b32 %0, %1" : "+v"(a), "+v"(b));
  a += b; b = a;
  asm volatile("s_nop 1\n v_permlane32_swap_b32 %0, %1" : "+v"(a), "+v"(b));
  return a + b;
}

__global__ __launch_bounds__(320) void k12_spectral(const u16* __restrict__ x, const u16* __restrict__ wre, const u16* __restrict__ wim, u16* __restrict__ abF, const u16* __restrict__ cr, const u16* __restrict__ ci, u16* __restrict__ part_, const u16* __restrict__ wg_, const u16* __restrict__ wu_, const u16* __restrict__ wd_, const u16* __restrict__ x_) {
  __shared__ __attribute__((aligned(16))) char smem[147456 + 64];
  CODE_LABEL("kc1b");
  unsigned cdm;
  { CODE_SRD(sc, "kc1b", "kc6e"); const unsigned vC = threadIdx.x < 64u ? (threadIdx.x * 32u + (blockIdx.x >> 3)) * 128u : 0x7fffffffu;
    asm volatile("buffer_load_dword %0, %1, %2, 0 offen" : "=&{v31}"(cdm) : "v"(vC), "s"(sc) : "memory"); }
  __builtin_amdgcn_sched_barrier(0);
  const unsigned tid = threadIdx.x, l = tid & 63;
  const unsigned w = __builtin_amdgcn_readfirstlane(tid >> 6);
  const unsigned b = blockIdx.x, xcd = b & 7, bi = b >> 3;
  const unsigned kq = xcd >> 1, part = kq >> 1, st = 16 * (kq & 1) + 8 * (xcd & 1) + (bi & 7), ct = bi >> 3;
  if (w >= 4) {
    unsigned dmy = cdm;
    const u32x4 srdE = make_srd((const char*)abF + (5u << 20) + 0xB90000u, 16384u + 64u);
    unsigned ep0;
    asm volatile("buffer_load_dword %0, %1, %2, 0 offen sc1" : "=&v"(ep0) : "v"(16384u), "s"(srdE) : "memory");
    const unsigned vC = (l * 32u + bi) * 128u;
    u32x4 srdPF = make_srd(kq < 2 ? cr : ci, 2048u * 2048u * 2u);
    const unsigned pfr = 1024u * (xcd & 1) + 32u * bi;
    const unsigned vPF = (pfr + l / 16u) * 4096u + (kq & 1) * 2048u + (l % 16u) * 128u;
    { const u32x4 srdZ = make_srd((const char*)abF + (5u << 20) + 0xB80000u, 4u * 256u * 64u);
      const unsigned vZ = (l < 4u && l != 1u) ? l * 16384u + b * 64u : 0x7fffffffu;
      asm volatile("buffer_store_dword %0, %1, %2, 0 offen" :: "v"(0u), "v"(vZ), "s"(srdZ) : "memory"); }
    asm volatile("s_barrier" ::: "memory");
    asm volatile("buffer_load_dword %0, %1, %2, %3 offen" : "+{v31}"(dmy) : "v"(vPF), "s"(srdPF), "s"(0u) : "memory");
    asm volatile("s_barrier" ::: "memory");
    asm volatile("buffer_load_dword %0, %1, %2, %3 offen" : "+{v31}"(dmy) : "v"(vPF), "s"(srdPF), "s"(16384u) : "memory");
    asm volatile("s_barrier" ::: "memory");
    asm volatile("buffer_load_dword %0, %1, %2, %3 offen" : "+{v31}"(dmy) : "v"(vPF), "s"(srdPF), "s"(32768u) : "memory");
    asm volatile("s_barrier" ::: "memory");
    asm volatile("buffer_load_dword %0, %1, %2, %3 offen" : "+{v31}"(dmy) : "v"(vPF), "s"(srdPF), "s"(49152u) : "memory");
    asm volatile("s_barrier" ::: "memory");
    asm volatile("buffer_load_dword %0, %1, %2, %3 offen" : "+{v31}"(dmy) : "v"(vPF), "s"(srdPF), "s"(65536u) : "memory");
    asm volatile("s_barrier" ::: "memory");
    asm volatile("buffer_load_dword %0, %1, %2, %3 offen" : "+{v31}"(dmy) : "v"(vPF), "s"(srdPF), "s"(81920u) : "memory");
    asm volatile("s_barrier" ::: "memory");
    { CODE_SRD(sc, "kc2b", "kc4b"); asm volatile("buffer_load_dword %0, %1, %2, 0 offen" : "+{v31}"(dmy) : "v"(vC), "s"(sc) : "memory"); }
    asm volatile("buffer_load_dword %0, %1, %2, %3 offen" : "+{v31}"(dmy) : "v"(vPF), "s"(srdPF), "s"(98304u) : "memory");
    asm volatile("s_barrier" ::: "memory");
    asm volatile("buffer_load_dword %0, %1, %2, %3 offen" : "+{v31}"(dmy) : "v"(vPF), "s"(srdPF), "s"(114688u) : "memory");
    asm volatile("s_barrier" ::: "memory");
    asm volatile("s_barrier" ::: "memory");
    asm volatile("s_waitcnt vmcnt(0)" : "+{v31}"(dmy), "+v"(ep0) :: "memory");
    const unsigned ep1 = ep0 + 1u;
    asm volatile("ds_write_b32 %0, %1" :: "v"((unsigned)(size_t)smem + 147456u), "v"(ep1) : "memory");
    unsigned wl = w, bl = b;
    asm volatile("" : "+s"(wl), "+s"(bl));
    {

  KARG_LOAD8(ka, 0x18); KARG_LOAD4(kb, 0x38); KARG_LOAD4(kc, 0x48);
  __builtin_amdgcn_sched_barrier(0);
  unsigned l = lane_id();
  const unsigned w = wl;
  const unsigned b = bl, xcd = b & 7, bi = b >> 3;
  const unsigned rt = 8 * (xcd & 1) + (bi & 7), kq = xcd >> 1, ct = bi >> 3;
  const unsigned rg = w & 1, ch = w >> 1;
  const unsigned lds = (unsigned)(size_t)smem;

  const unsigned soffA = (ct * 128u + kq * 32u) * 9216u + w * 4608u + rt * 18432u, soffAw = soffA - 16u * 18432u, lim = 16u - rt;
  const unsigned m0A = lds + w * 4608u, m0A2 = m0A + 4096u, m0B = lds + 18432u + w * 4096u;
  const unsigned vA = l * 16u, vAh = l * 16u + 4096u;
  const unsigned r8 = l >> 3;
  const unsigned rowD = 128u * rt + 32u * w + r8;
  const unsigned vB0 = rowD * 4096u + 16u * ((l & 7) ^ (r8 >> 1)), vB1 = (rowD + 8) * 4096u + 16u * ((l & 7) ^ (4 | (r8 >> 1))) - 1024u;
  const unsigned vB2 = vB0 + 16u * 4096u - 2048u, vB3 = vB1 + 16u * 4096u - 2048u;
  const unsigned soffB = (kq & 1) * 2048u + rt * 128u, soffBw = soffB - 2048u;
  asm volatile("s_waitcnt lgkmcnt(0)" : "+s"(ka), "+s"(kb), "+s"(kc) : "v"(vA), "v"(vAh), "v"(vB0), "v"(vB1), "v"(vB2), "v"(vB3), "s"(soffA), "s"(m0A), "s"(m0A2), "s"(m0B), "s"(soffB));
    {
    const unsigned vC = (l * 32u + bi) * 128u;
    u32x4 srdG = srd_of(kb[0], kb[1], 576u * 4608u * 2u), srdU = srd_of(kb[2], kb[3], 576u * 4608u * 2u);
    const unsigned vPF = (b * 162u + l) * 128u, vP2 = l < 34u ? vPF : 0x7fffffffu;
    const unsigned long long rmb = (((unsigned long long)ka[7] << 32) | ka[6]) + (33u << 20);
    const unsigned rm_d = b >= 144u, rm_m = b >= 72u, rb = b - (rm_d ? 144u : (rm_m ? 72u : 0u)), on = b < 216u;
    u32x4 srdRM = srd_of((unsigned)rmb, (unsigned)(rmb >> 32), on ? 1728u * 1024u : 0u);
    u32x4 srdRS = srd_of(rm_d ? kc[0] : (rm_m ? kb[2] : kb[0]), rm_d ? kc[1] : (rm_m ? kb[3] : kb[1]), on ? 4608u * 576u * 2u : 0u);
    const unsigned r8 = l >> 3, c8 = l & 7;
    const unsigned vRS = __umul24(r8, rm_d ? 1152u : 9216u) + 16u * c8;
    const unsigned sRS = rm_d ? rb * 73728u + 1024u : rb * 73728u + 8192u, sRSd = rm_d ? 9216u : 128u;
    const unsigned rkq = rb < 64u ? rb >> 4 : (rb - 64u) >> 1, rv = rb < 64u ? rb & 15u : 16u + (rb & 1u);
    const unsigned sRD = rm_d ? 1152u * 1024u + (rkq * 72u + rv) * 2048u : ((rb >> 3) * 4u + 2u * ((rb >> 2) & 1u) + rm_m) * 1024u + (rb & 3u) * 256u, sRDd = rm_d ? 256u : 147456u;
    const unsigned rx = rm_d ? 0u : (rb & 1u) * 4u, pc = (c8 >> 1) * 36864u + (l & 1u) * 16u;
    const unsigned vRD0 = pc + (r8 ^ rx) * 32u, vRD1 = pc + (r8 ^ (rm_d ? 4u : rx)) * 32u;

    const unsigned vFs = l == 0u ? b * 64u : 0x7fffffffu, vFp = ((xcd & 6u) + ((l >> 3) & 1u) + 8u * ((bi & 24u) + (l & 7u))) * 64u;
    const unsigned uq = (rt + 1u + ((l & 3u) == 3u ? 2u : (l & 3u))) & 15u, vFq = ((xcd & 6u) + (uq >> 3) + 8u * ((bi & 24u) + (uq & 7u))) * 64u;
    asm volatile("s_waitcnt lgkmcnt(0)\n s_barrier" :: "v"(vFs), "v"(vFp), "v"(vFq), "v"(vRS), "v"(vRD0), "v"(vRD1), "v"(vPF), "v"(vP2), "s"(srdRM), "s"(srdRS), "s"(sRS), "s"(sRD) : "memory");
    {
      unsigned f0, f1, f2, cnt; unsigned long long fm;
      asm volatile("buffer_store_dword %[one], %[vS], %[sd], 0 offen sc1\n s_barrier\n"
"buffer_load_dword %[f0], %[vP], %[sd], 0 offen sc1\n s_sleep 3\n"
"buffer_load_dword %[f1], %[vP], %[sd], 0 offen sc1\n s_sleep 3\n"
"buffer_load_dword %[f2], %[vP], %[sd], 0 offen sc1\n"
"s_movk_i32 %[cnt], 0x1000\n"
"Lq%=:\n"
"s_waitcnt vmcnt(2)\n v_cmp_ne_u32 %[fm], %[one], %[f0]\n s_cmp_eq_u64 %[fm], 0\n s_cbranch_scc1 Lr%=\n"
"buffer_load_dword %[f0], %[vP], %[sd], 0 offen sc1\n"
"s_waitcnt vmcnt(2)\n v_cmp_ne_u32 %[fm], %[one], %[f1]\n s_cmp_eq_u64 %[fm], 0\n s_cbranch_scc1 Lr%=\n"
"buffer_load_dword %[f1], %[vP], %[sd], 0 offen sc1\n"
"s_waitcnt vmcnt(2)\n v_cmp_ne_u32 %[fm], %[one], %[f2]\n s_cmp_eq_u64 %[fm], 0\n s_cbranch_scc1 Lr%=\n"
"buffer_load_dword %[f2], %[vP], %[sd], 0 offen sc1\n"
"s_sub_u32 %[cnt], %[cnt], 1\n s_cbranch_scc0 Lq%=\n"
"Lr%=:\n s_barrier\n s_waitcnt vmcnt(0)"
                   : [f0]"=&v"(f0), [f1]"=&v"(f1), [f2]"=&v"(f2), [cnt]"=&s"(cnt), [fm]"=&s"(fm)
                   : [one]"v"(ep1), [vS]"v"(vFs), [vP]"v"(vFq), [sd]"s"(srdE) : "memory", "scc");
    }
    for (unsigned it = 0; it < 8192u; ++it) {
      unsigned fv;
      asm volatile("buffer_load_dword %0, %1, %2, 0 offen sc1\n s_waitcnt vmcnt(0)" : "=&v"(fv) : "v"(vFp), "s"(srdE) : "memory");
      if (__builtin_amdgcn_ballot_w64(fv != ep1) == 0ull) break;
    }
    u32x4 rd0;
    asm volatile("buffer_load_dwordx4 %0, %1, %2, %3 offen" : "=&v"(rd0) : "v"(vRS), "s"(srdRS), "s"(sRS + 0u * sRSd) : "memory");
    u32x4 rd1;
    asm volatile("buffer_load_dwordx4 %0, %1, %2, %3 offen" : "=&v"(rd1) : "v"(vRS), "s"(srdRS), "s"(sRS + 1u * sRSd) : "memory");
    asm volatile("s_barrier" ::: "memory");
    u32x4 rd2;
    asm volatile("buffer_load_dwordx4 %0, %1, %2, %3 offen" : "=&v"(rd2) : "v"(vRS), "s"(srdRS), "s"(sRS + 2u * sRSd) : "memory");
    u32x4 rd3;
    asm volatile("buffer_load_dwordx4 %0, %1, %2, %3 offen" : "=&v"(rd3) : "v"(vRS), "s"(srdRS), "s"(sRS + 3u * sRSd) : "memory");
    asm volatile("buffer_load_dword %0, %1, %2, %3 offen" : "+{v31}"(dmy) : "v"(vPF), "s"(srdG), "s"(0u) : "memory");
    asm volatile("s_barrier" ::: "memory");
    u32x4 rd4;
    asm volatile("buffer_load_dwordx4 %0, %1, %2, %3 offen" : "=&v"(rd4) : "v"(vRS), "s"(srdRS), "s"(sRS + 4u * sRSd) : "memory");
    u32x4 rd5;
    asm volatile("buffer_load_dwordx4 %0, %1, %2, %3 offen" : "=&v"(rd5) : "v"(vRS), "s"(srdRS), "s"(sRS + 5u * sRSd) : "memory");
    asm volatile("s_barrier" ::: "memory");
    u32x4 rd6;
    asm volatile("buffer_load_dwordx4 %0, %1, %2, %3 offen" : "=&v"(rd6) : "v"(vRS), "s"(srdRS), "s"(sRS + 6u * sRSd) : "memory");
    u32x4 rd7;
    asm volatile("buffer_load_dwordx4 %0, %1, %2, %3 offen" : "=&v"(rd7) : "v"(vRS), "s"(srdRS), "s"(sRS + 7u * sRSd) : "memory");
    asm volatile("buffer_load_dword %0, %1, %2, %3 offen" : "+{v31}"(dmy) : "v"(vPF), "s"(srdU), "s"(0u) : "memory");
    asm volatile("s_barrier" ::: "memory");
    asm volatile("s_barrier" ::: "memory");
    asm volatile("buffer_load_dword %0, %1, %2, %3 offen" : "+{v31}"(dmy) : "v"(vPF), "s"(srdG), "s"(8192u) : "memory");
    asm volatile("s_barrier" ::: "memory");
    asm volatile("s_barrier" ::: "memory");
    asm volatile("buffer_load_dword %0, %1, %2, %3 offen" : "+{v31}"(dmy) : "v"(vPF), "s"(srdU), "s"(8192u) : "memory");
    asm volatile("s_barrier" ::: "memory");
    asm volatile("s_barrier" ::: "memory");
    asm volatile("buffer_load_dword %0, %1, %2, %3 offen" : "+{v31}"(dmy) : "v"(vP2), "s"(srdG), "s"(16384u) : "memory");
    asm volatile("s_barrier" ::: "memory");
    asm volatile("s_barrier" ::: "memory");
    asm volatile("buffer_load_dword %0, %1, %2, %3 offen" : "+{v31}"(dmy) : "v"(vP2), "s"(srdU), "s"(16384u) : "memory");
    asm volatile("s_barrier" ::: "memory");
    { CODE_SRD(sc, "kc3b", "kc4e"); asm volatile("buffer_load_dword %0, %1, %2, 0 offen" : "+{v31}"(dmy) : "v"(vC), "s"(sc) : "memory"); }
    asm volatile("s_barrier" ::: "memory");
    asm volatile("s_barrier" ::: "memory");
    asm volatile("s_barrier" ::: "memory");
    if (b == 0u) {
      for (unsigned it = 0; it < 8192u; ++it) {
        unsigned f0, f1, f2, f3;
        asm volatile("buffer_load_dword %0, %4, %5, 0 offen sc1\n buffer_load_dword %1, %4, %5, %6 offen sc1\n buffer_load_dword %2, %4, %5, %7 offen sc1\n buffer_load_dword %3, %4, %5, %8 offen sc1\n s_waitcnt vmcnt(0)"
                     : "=&v"(f0), "=&v"(f1), "=&v"(f2), "=&v"(f3) : "v"(l * 64u), "s"(srdE), "s"(4096u), "s"(8192u), "s"(12288u) : "memory");
        if (__builtin_amdgcn_ballot_w64(f0 != ep1 || f1 != ep1 || f2 != ep1 || f3 != ep1) == 0ull) break;
      }
      asm volatile("buffer_store_dword %0, %1, %2, 0 offen sc1" :: "v"(ep1), "v"(l == 0u ? 16384u : 0x7fffffffu), "s"(srdE) : "memory");
    }
    asm volatile("s_waitcnt vmcnt(14)" : "+v"(rd0) :: "memory");
    asm volatile("buffer_store_dwordx4 %0, %1, %2, %3 offen sc1" :: "v"(rd0), "v"(vRD0), "s"(srdRM), "s"(sRD + 0u * sRDd) : "memory");
    asm volatile("s_waitcnt vmcnt(14)" : "+v"(rd1) :: "memory");
    asm volatile("buffer_store_dwordx4 %0, %1, %2, %3 offen sc1" :: "v"(rd1), "v"(vRD1), "s"(srdRM), "s"(sRD + 1u * sRDd) : "memory");
    asm volatile("s_waitcnt vmcnt(14)" : "+v"(rd2) :: "memory");
    asm volatile("buffer_store_dwordx4 %0, %1, %2, %3 offen sc1" :: "v"(rd2), "v"(vRD0), "s"(srdRM), "s"(sRD + 2u * sRDd) : "memory");
    asm volatile("s_waitcnt vmcnt(14)" : "+v"(rd3) :: "memory");
    asm volatile("buffer_store_dwordx4 %0, %1, %2, %3 offen sc1" :: "v"(rd3), "v"(vRD1), "s"(srdRM), "s"(sRD + 3u * sRDd) : "memory");
    asm volatile("s_waitcnt vmcnt(13)" : "+v"(rd4) :: "memory");
    asm volatile("buffer_store_dwordx4 %0, %1, %2, %3 offen sc1" :: "v"(rd4), "v"(vRD0), "s"(srdRM), "s"(sRD + 4u * sRDd) : "memory");
    asm volatile("s_waitcnt vmcnt(13)" : "+v"(rd5) :: "memory");
    asm volatile("buffer_store_dwordx4 %0, %1, %2, %3 offen sc1" :: "v"(rd5), "v"(vRD1), "s"(srdRM), "s"(sRD + 5u * sRDd) : "memory");
    asm volatile("s_waitcnt vmcnt(13)" : "+v"(rd6) :: "memory");
    asm volatile("buffer_store_dwordx4 %0, %1, %2, %3 offen sc1" :: "v"(rd6), "v"(vRD0), "s"(srdRM), "s"(sRD + 6u * sRDd) : "memory");
    asm volatile("s_waitcnt vmcnt(13)" : "+v"(rd7) :: "memory");
    asm volatile("buffer_store_dwordx4 %0, %1, %2, %3 offen sc1" :: "v"(rd7), "v"(vRD1), "s"(srdRM), "s"(sRD + 7u * sRDd) : "memory");
    asm volatile("" :: "{v31}"(dmy));
    }
    }
    return;
  }
  const unsigned lds = (unsigned)(size_t)smem;
  u32x4 srdX = make_srd(x, 2048u * 576u * 2u);
  u32x4 srdW = make_srd(part ? wim : wre, 576u * 576u * 2u);
  u32x4 srdO = make_srd(abF, 4096u * 576u * 2u);
  const unsigned m0X = lds + 2048u * w, m0W = lds + 8192u + 4608u * w;
  const unsigned r8 = l >> 3, c8 = l & 7;
  const unsigned p0 = 2 * w, p1 = 2 * w + 1;
  const unsigned vXd0 = (64u * st + 8u * p0 + r8) * 1152u + 16u * (c8 ^ (((r8 >> 1) & 1) | ((p0 & 3) << 1)));
  const unsigned vXd1 = (64u * st + 8u * p1 + r8) * 1152u + 16u * (c8 ^ (((r8 >> 1) & 1) | ((p1 & 3) << 1))) - 1024u;
#define WROW(i) (36u * w + 8u * (i) + r8)
#define WCOL(R) ((R) + 128u * ct + ((R) >> 7) * (384u - 112u * ct))
#define WADR(i) (WCOL(WROW(i)) * 1152u + 16u * (c8 ^ ((WROW(i) >> 1) & 7)))
  const unsigned vW0 = WADR(0), vW1 = WADR(1) - 1024u, vW2 = WADR(2) - 2048u, vW3 = WADR(3) - 3072u, vW4 = WADR(4);
#undef WROW
#undef WCOL
#undef WADR
  const unsigned g = l >> 4, n = l & 15;
  const unsigned c0 = 16u * (g ^ (n >> 1)), c1 = c0 ^ 64u;
  const unsigned ra = lds + (8u * (n >> 2) + (n & 3)) * 128u;
  const unsigned vA0 = ra + c0, vA1 = ra + c1;
  const unsigned rbb = lds + 8192u + (32u * w + n) * 128u;
  const unsigned vB0 = rbb + c0, vB1 = rbb + c1;
  const unsigned r8b = lds + 8192u + (128u + n) * 128u, on = lds + 26624u;
  const unsigned mk0 = w == 0 ? ~0u : 0u, mk2 = w == 2 ? ~0u : 0u, mk1 = ~(mk0 | mk2);
#define XA(s, bb) (((r8b + (s ? c1 : c0)) & mk0) | (((s ? vA1 : vA0) + 4096u * ((bb) >> 1) + 512u * ((bb) & 1)) & mk2) | (on & mk1))
  const unsigned vX00 = XA(0, 0), vX01 = XA(0, 1), vX02 = XA(0, 2), vX03 = XA(0, 3), vX10 = XA(1, 0), vX11 = XA(1, 1), vX12 = XA(1, 2), vX13 = XA(1, 3);
#undef XA
  const unsigned vOn = on + (l % 5u) * 26880u;
  const unsigned vEw = lds + w * 4096u + l * 16u, vEr = lds + 4096u + l * 16u, vEg = lds + 8192u + (20u * g) * 16u;
  const unsigned vS = l * 16u + 2u * w * 1024u, vSX = w == 0 ? l * 16u + 8192u : 0x7fffffffu;
  const unsigned soffO = __builtin_amdgcn_readfirstlane((ct * 128u + 64u * part + 2u * st) * 9216u);
  const float inv = 1.0f / 576.0f, eps = 1e-5f, sgn = part ? -1.0f : 1.0f;

  asm volatile(
"s_movk_i32 s60, 0\n"
"s_add_u32 m0, %[m0X], 0\n"
"s_nop 0\n"
"buffer_load_dwordx4 %[vXd0], %[srdX], s60 offen offset:0 lds\n"
"buffer_load_dwordx4 %[vXd1], %[srdX], s60 offen offset:1024 lds\n"
"s_add_u32 m0, %[m0W], 0\n"
"s_nop 0\n"
"buffer_load_dwordx4 %[vW0], %[srdW], s60 offen offset:0 lds\n"
"buffer_load_dwordx4 %[vW1], %[srdW], s60 offen offset:1024 lds\n"
"buffer_load_dwordx4 %[vW2], %[srdW], s60 offen offset:2048 lds\n"
"buffer_load_dwordx4 %[vW3], %[srdW], s60 offen offset:3072 lds\n"
"s_add_u32 m0, %[m0W], 4096\n"
"s_mov_b32 exec_hi, 0\n"
"buffer_load_dwordx4 %[vW4], %[srdW], s60 offen lds\n"
"s_mov_b32 exec_hi, -1\n"
"s_movk_i32 s60, 128\n"
"s_add_u32 m0, %[m0X], 26880\n"
"s_nop 0\n"
"buffer_load_dwordx4 %[vXd0], %[srdX], s60 offen offset:0 lds\n"
"buffer_load_dwordx4 %[vXd1], %[srdX], s60 offen offset:1024 lds\n"
"s_add_u32 m0, %[m0W], 26880\n"
"s_nop 0\n"
"buffer_load_dwordx4 %[vW0], %[srdW], s60 offen offset:0 lds\n"
"buffer_load_dwordx4 %[vW1], %[srdW], s60 offen offset:1024 lds\n"
"buffer_load_dwordx4 %[vW2], %[srdW], s60 offen offset:2048 lds\n"
"buffer_load_dwordx4 %[vW3], %[srdW], s60 offen offset:3072 lds\n"
"s_add_u32 m0, %[m0W], 30976\n"
"s_mov_b32 exec_hi, 0\n"
"buffer_load_dwordx4 %[vW4], %[srdW], s60 offen lds\n"
"s_mov_b32 exec_hi, -1\n"
"s_movk_i32 s60, 256\n"
"s_add_u32 m0, %[m0X], 53760\n"
"s_nop 0\n"
"buffer_load_dwordx4 %[vXd0], %[srdX], s60 offen offset:0 lds\n"
"buffer_load_dwordx4 %[vXd1], %[srdX], s60 offen offset:1024 lds\n"
"s_add_u32 m0, %[m0W], 53760\n"
"s_nop 0\n"
"buffer_load_dwordx4 %[vW0], %[srdW], s60 offen offset:0 lds\n"
"buffer_load_dwordx4 %[vW1], %[srdW], s60 offen offset:1024 lds\n"
"buffer_load_dwordx4 %[vW2], %[srdW], s60 offen offset:2048 lds\n"
"buffer_load_dwordx4 %[vW3], %[srdW], s60 offen offset:3072 lds\n"
"s_add_u32 m0, %[m0W], 57856\n"
"s_mov_b32 exec_hi, 0\n"
"buffer_load_dwordx4 %[vW4], %[srdW], s60 offen lds\n"
"s_mov_b32 exec_hi, -1\n"
"v_mov_b32 v92, 0x3f803f80\n"
"v_mov_b32 v93, 0x3f803f80\n"
"v_mov_b32 v94, 0x3f803f80\n"
"v_mov_b32 v95, 0x3f803f80\n"
"v_add_u32 v96, 0, %[vA0]\n"
"v_add_u32 v102, 0, %[vB0]\n"
"v_add_u32 v108, 0, %[vX00]\n"
"v_add_u32 v111, 0, %[vX01]\n"
"v_add_u32 v114, 0, %[vX02]\n"
"v_add_u32 v117, 0, %[vX03]\n"
"v_add_u32 v99, 0, %[vA1]\n"
"v_add_u32 v105, 0, %[vB1]\n"
"v_add_u32 v120, 0, %[vX10]\n"
"v_add_u32 v123, 0, %[vX11]\n"
"v_add_u32 v126, 0, %[vX12]\n"
"v_add_u32 v129, 0, %[vX13]\n"
"v_add_u32 v97, 53760, %[vA0]\n"
"v_add_u32 v103, 53760, %[vB0]\n"
"v_add_u32 v109, 53760, %[vX00]\n"
"v_add_u32 v112, 53760, %[vX01]\n"
"v_add_u32 v115, 53760, %[vX02]\n"
"v_add_u32 v118, 53760, %[vX03]\n"
"v_add_u32 v100, 53760, %[vA1]\n"
"v_add_u32 v106, 53760, %[vB1]\n"
"v_add_u32 v121, 53760, %[vX10]\n"
"v_add_u32 v124, 53760, %[vX11]\n"
"v_add_u32 v127, 53760, %[vX12]\n"
"v_add_u32 v130, 53760, %[vX13]\n"
"v_add_u32 v98, 107520, %[vA0]\n"
"v_add_u32 v104, 107520, %[vB0]\n"
"v_add_u32 v110, 107520, %[vX00]\n"
"v_add_u32 v113, 107520, %[vX01]\n"
"v_add_u32 v116, 107520, %[vX02]\n"
"v_add_u32 v119, 107520, %[vX03]\n"
"v_add_u32 v101, 107520, %[vA1]\n"
"v_add_u32 v107, 107520, %[vB1]\n"
"v_add_u32 v122, 107520, %[vX10]\n"
"v_add_u32 v125, 107520, %[vX11]\n"
"v_add_u32 v128, 107520, %[vX12]\n"
"v_add_u32 v131, 107520, %[vX13]\n"
"ds_write_b128 %[vOn], v[92:95]\n"
"s_waitcnt vmcnt(14)\n"
"s_waitcnt lgkmcnt(0)\n"
"s_barrier\n"
"ds_read_b128 a[0:3], v96 offset:0\n"
"ds_read_b128 a[32:35], v102 offset:0\n"
"ds_read_b128 a[36:39], v102 offset:2048\n"
"ds_read_b128 a[48:51], v108 offset:0\n"
"ds_read_b128 a[4:7], v96 offset:512\n"
"ds_read_b128 a[52:55], v111 offset:0\n"
"ds_read_b128 a[8:11], v96 offset:4096\n"
"ds_read_b128 a[56:59], v114 offset:0\n"
"ds_read_b128 a[12:15], v96 offset:4608\n"
"ds_read_b128 a[60:63], v117 offset:0\n"
"s_waitcnt lgkmcnt(4)\n"
"v_mfma_f32_16x16x32_bf16 v[32:35], a[0:3], a[32:35], 0\n"
"s_movk_i32 s60, 384\n"
"s_add_u32 m0, %[m0X], 80640\n"
"s_nop 0\n"
"buffer_load_dwordx4 %[vXd0], %[srdX], s60 offen offset:0 lds\n"
"v_mfma_f32_16x16x32_bf16 v[36:39], a[0:3], a[36:39], 0\n"
"ds_read_b128 a[16:19], v99 offset:0\n"
"v_mfma_f32_16x16x32_bf16 v[40:43], a[0:3], a[48:51], 0\n"
"ds_read_b128 a[40:43], v105 offset:0\n"
"v_mfma_f32_16x16x32_bf16 v[80:83], v[92:95], a[32:35], 0\n"
"ds_read_b128 a[44:47], v105 offset:2048\n"
"v_mfma_f32_16x16x32_bf16 v[44:47], a[4:7], a[32:35], 0\n"
"ds_read_b128 a[64:67], v120 offset:0\n"
"v_mfma_f32_16x16x32_bf16 v[48:51], a[4:7], a[36:39], 0\n"
"ds_read_b128 a[20:23], v99 offset:512\n"
"v_mfma_f32_16x16x32_bf16 v[52:55], a[4:7], a[52:55], 0\n"
"ds_read_b128 a[68:71], v123 offset:0\n"
"v_mfma_f32_16x16x32_bf16 v[84:87], v[92:95], a[36:39], 0\n"
"ds_read_b128 a[24:27], v99 offset:4096\n"
"s_waitcnt lgkmcnt(9)\n"
"v_mfma_f32_16x16x32_bf16 v[56:59], a[8:11], a[32:35], 0\n"
"ds_read_b128 a[72:75], v126 offset:0\n"
"v_mfma_f32_16x16x32_bf16 v[60:63], a[8:11], a[36:39], 0\n"
"ds_read_b128 a[28:31], v99 offset:4608\n"
"v_mfma_f32_16x16x32_bf16 v[64:67], a[8:11], a[56:59], 0\n"
"ds_read_b128 a[76:79], v129 offset:0\n"
"v_mfma_f32_16x16x32_bf16 v[88:91], v[92:95], a[48:51], 0\n"
"buffer_load_dwordx4 %[vXd1], %[srdX], s60 offen offset:1024 lds\n"
"s_waitcnt lgkmcnt(10)\n"
"v_mfma_f32_16x16x32_bf16 v[68:71], a[12:15], a[32:35], 0\n"
"s_add_u32 m0, %[m0W], 80640\n"
"s_nop 0\n"
"buffer_load_dwordx4 %[vW0], %[srdW], s60 offen offset:0 lds\n"
"v_mfma_f32_16x16x32_bf16 v[72:75], a[12:15], a[36:39], 0\n"
"buffer_load_dwordx4 %[vW1], %[srdW], s60 offen offset:1024 lds\n"
"v_mfma_f32_16x16x32_bf16 v[76:79], a[12:15], a[60:63], 0\n"
"buffer_load_dwordx4 %[vW2], %[srdW], s60 offen offset:2048 lds\n"
"s_waitcnt lgkmcnt(4)\n"
"v_mfma_f32_16x16x32_bf16 v[32:35], a[16:19], a[40:43], v[32:35]\n"
"buffer_load_dwordx4 %[vW3], %[srdW], s60 offen offset:3072 lds\n"
"v_mfma_f32_16x16x32_bf16 v[36:39], a[16:19], a[44:47], v[36:39]\n"
"s_add_u32 m0, %[m0W], 84736\n"
"s_mov_b32 exec_hi, 0\n"
"buffer_load_dwordx4 %[vW4], %[srdW], s60 offen lds\n"
"s_mov_b32 exec_hi, -1\n"
"v_mfma_f32_16x16x32_bf16 v[40:43], a[16:19], a[64:67], v[40:43]\n"
"s_waitcnt vmcnt(14)\n"
"s_barrier\n"
"ds_read_b128 a[0:3], v96 offset:26880\n"
"v_mfma_f32_16x16x32_bf16 v[80:83], v[92:95], a[40:43], v[80:83]\n"
"ds_read_b128 a[32:35], v102 offset:26880\n"
"v_mfma_f32_16x16x32_bf16 v[44:47], a[20:23], a[40:43], v[44:47]\n"
"ds_read_b128 a[36:39], v102 offset:28928\n"
"v_mfma_f32_16x16x32_bf16 v[48:51], a[20:23], a[44:47], v[48:51]\n"
"ds_read_b128 a[48:51], v108 offset:26880\n"
"v_mfma_f32_16x16x32_bf16 v[52:55], a[20:23], a[68:71], v[52:55]\n"
"ds_read_b128 a[4:7], v96 offset:27392\n"
"v_mfma_f32_16x16x32_bf16 v[84:87], v[92:95], a[44:47], v[84:87]\n"
"ds_read_b128 a[52:55], v111 offset:26880\n"
"s_waitcnt lgkmcnt(8)\n"
"v_mfma_f32_16x16x32_bf16 v[56:59], a[24:27], a[40:43], v[56:59]\n"
"ds_read_b128 a[8:11], v96 offset:30976\n"
"v_mfma_f32_16x16x32_bf16 v[60:63], a[24:27], a[44:47], v[60:63]\n"
"ds_read_b128 a[56:59], v114 offset:26880\n"
"v_mfma_f32_16x16x32_bf16 v[64:67], a[24:27], a[72:75], v[64:67]\n"
"ds_read_b128 a[12:15], v96 offset:31488\n"
"v_mfma_f32_16x16x32_bf16 v[88:91], v[92:95], a[64:67], v[88:91]\n"
"ds_read_b128 a[60:63], v117 offset:26880\n"
"s_waitcnt lgkmcnt(10)\n"
"v_mfma_f32_16x16x32_bf16 v[68:71], a[28:31], a[40:43], v[68:71]\n"
"s_movk_i32 s60, 512\n"
"s_add_u32 m0, %[m0X], 107520\n"
"s_nop 0\n"
"buffer_load_dwordx4 %[vXd0], %[srdX], s60 offen offset:0 lds\n"
"v_mfma_f32_16x16x32_bf16 v[72:75], a[28:31], a[44:47], v[72:75]\n"
"buffer_load_dwordx4 %[vXd1], %[srdX], s60 offen offset:1024 lds\n"
"v_mfma_f32_16x16x32_bf16 v[76:79], a[28:31], a[76:79], v[76:79]\n"
"s_add_u32 m0, %[m0W], 107520\n"
"s_nop 0\n"
"buffer_load_dwordx4 %[vW0], %[srdW], s60 offen offset:0 lds\n"
"s_waitcnt lgkmcnt(4)\n"
"v_mfma_f32_16x16x32_bf16 v[32:35], a[0:3], a[32:35], v[32:35]\n"
"buffer_load_dwordx4 %[vW1], %[srdW], s60 offen offset:1024 lds\n"
"v_mfma_f32_16x16x32_bf16 v[36:39], a[0:3], a[36:39], v[36:39]\n"
"ds_read_b128 a[16:19], v99 offset:26880\n"
"v_mfma_f32_16x16x32_bf16 v[40:43], a[0:3], a[48:51], v[40:43]\n"
"ds_read_b128 a[40:43], v105 offset:26880\n"
"v_mfma_f32_16x16x32_bf16 v[80:83], v[92:95], a[32:35], v[80:83]\n"
"ds_read_b128 a[44:47], v105 offset:28928\n"
"v_mfma_f32_16x16x32_bf16 v[44:47], a[4:7], a[32:35], v[44:47]\n"
"ds_read_b128 a[64:67], v120 offset:26880\n"
"v_mfma_f32_16x16x32_bf16 v[48:51], a[4:7], a[36:39], v[48:51]\n"
"ds_read_b128 a[20:23], v99 offset:27392\n"
"v_mfma_f32_16x16x32_bf16 v[52:55], a[4:7], a[52:55], v[52:55]\n"
"ds_read_b128 a[68:71], v123 offset:26880\n"
"v_mfma_f32_16x16x32_bf16 v[84:87], v[92:95], a[36:39], v[84:87]\n"
"ds_read_b128 a[24:27], v99 offset:30976\n"
"s_waitcnt lgkmcnt(9)\n"
"v_mfma_f32_16x16x32_bf16 v[56:59], a[8:11], a[32:35], v[56:59]\n"
"ds_read_b128 a[72:75], v126 offset:26880\n"
"v_mfma_f32_16x16x32_bf16 v[60:63], a[8:11], a[36:39], v[60:63]\n"
"ds_read_b128 a[28:31], v99 offset:31488\n"
"v_mfma_f32_16x16x32_bf16 v[64:67], a[8:11], a[56:59], v[64:67]\n"
"ds_read_b128 a[76:79], v129 offset:26880\n"
"v_mfma_f32_16x16x32_bf16 v[88:91], v[92:95], a[48:51], v[88:91]\n"
"buffer_load_dwordx4 %[vW2], %[srdW], s60 offen offset:2048 lds\n"
"s_waitcnt lgkmcnt(10)\n"
"v_mfma_f32_16x16x32_bf16 v[68:71], a[12:15], a[32:35], v[68:71]\n"
"buffer_load_dwordx4 %[vW3], %[srdW], s60 offen offset:3072 lds\n"
"v_mfma_f32_16x16x32_bf16 v[72:75], a[12:15], a[36:39], v[72:75]\n"
"s_add_u32 m0, %[m0W], 111616\n"
"s_mov_b32 exec_hi, 0\n"
"buffer_load_dwordx4 %[vW4], %[srdW], s60 offen lds\n"
"s_mov_b32 exec_hi, -1\n"
"v_mfma_f32_16x16x32_bf16 v[76:79], a[12:15], a[60:63], v[76:79]\n"
"s_waitcnt lgkmcnt(4)\n"
"v_mfma_f32_16x16x32_bf16 v[32:35], a[16:19], a[40:43], v[32:35]\n"
"v_mfma_f32_16x16x32_bf16 v[36:39], a[16:19], a[44:47], v[36:39]\n"
"v_mfma_f32_16x16x32_bf16 v[40:43], a[16:19], a[64:67], v[40:43]\n"
"s_waitcnt vmcnt(14)\n"
"s_barrier\n"
"ds_read_b128 a[0:3], v97 offset:0\n"
"v_mfma_f32_16x16x32_bf16 v[80:83], v[92:95], a[40:43], v[80:83]\n"
"ds_read_b128 a[32:35], v103 offset:0\n"
"v_mfma_f32_16x16x32_bf16 v[44:47], a[20:23], a[40:43], v[44:47]\n"
"ds_read_b128 a[36:39], v103 offset:2048\n"
"v_mfma_f32_16x16x32_bf16 v[48:51], a[20:23], a[44:47], v[48:51]\n"
"ds_read_b128 a[48:51], v109 offset:0\n"
"v_mfma_f32_16x16x32_bf16 v[52:55], a[20:23], a[68:71], v[52:55]\n"
"ds_read_b128 a[4:7], v97 offset:512\n"
"v_mfma_f32_16x16x32_bf16 v[84:87], v[92:95], a[44:47], v[84:87]\n"
"ds_read_b128 a[52:55], v112 offset:0\n"
"s_waitcnt lgkmcnt(8)\n"
"v_mfma_f32_16x16x32_bf16 v[56:59], a[24:27], a[40:43], v[56:59]\n"
"ds_read_b128 a[8:11], v97 offset:4096\n"
"v_mfma_f32_16x16x32_bf16 v[60:63], a[24:27], a[44:47], v[60:63]\n"
"ds_read_b128 a[56:59], v115 offset:0\n"
"v_mfma_f32_16x16x32_bf16 v[64:67], a[24:27], a[72:75], v[64:67]\n"
"ds_read_b128 a[12:15], v97 offset:4608\n"
"v_mfma_f32_16x16x32_bf16 v[88:91], v[92:95], a[64:67], v[88:91]\n"
"ds_read_b128 a[60:63], v118 offset:0\n"
"s_waitcnt lgkmcnt(10)\n"
"v_mfma_f32_16x16x32_bf16 v[68:71], a[28:31], a[40:43], v[68:71]\n"
"s_movk_i32 s60, 640\n"
"s_add_u32 m0, %[m0X], 0\n"
"s_nop 0\n"
"buffer_load_dwordx4 %[vXd0], %[srdX], s60 offen offset:0 lds\n"
"v_mfma_f32_16x16x32_bf16 v[72:75], a[28:31], a[44:47], v[72:75]\n"
"buffer_load_dwordx4 %[vXd1], %[srdX], s60 offen offset:1024 lds\n"
"v_mfma_f32_16x16x32_bf16 v[76:79], a[28:31], a[76:79], v[76:79]\n"
"s_add_u32 m0, %[m0W], 0\n"
"s_nop 0\n"
"buffer_load_dwordx4 %[vW0], %[srdW], s60 offen offset:0 lds\n"
"s_waitcnt lgkmcnt(4)\n"
"v_mfma_f32_16x16x32_bf16 v[32:35], a[0:3], a[32:35], v[32:35]\n"
"buffer_load_dwordx4 %[vW1], %[srdW], s60 offen offset:1024 lds\n"
"v_mfma_f32_16x16x32_bf16 v[36:39], a[0:3], a[36:39], v[36:39]\n"
"ds_read_b128 a[16:19], v100 offset:0\n"
"v_mfma_f32_16x16x32_bf16 v[40:43], a[0:3], a[48:51], v[40:43]\n"
"ds_read_b128 a[40:43], v106 offset:0\n"
"v_mfma_f32_16x16x32_bf16 v[80:83], v[92:95], a[32:35], v[80:83]\n"
"ds_read_b128 a[44:47], v106 offset:2048\n"
"v_mfma_f32_16x16x32_bf16 v[44:47], a[4:7], a[32:35], v[44:47]\n"
"ds_read_b128 a[64:67], v121 offset:0\n"
"v_mfma_f32_16x16x32_bf16 v[48:51], a[4:7], a[36:39], v[48:51]\n"
"ds_read_b128 a[20:23], v100 offset:512\n"
"v_mfma_f32_16x16x32_bf16 v[52:55], a[4:7], a[52:55], v[52:55]\n"
"ds_read_b128 a[68:71], v124 offset:0\n"
"v_mfma_f32_16x16x32_bf16 v[84:87], v[92:95], a[36:39], v[84:87]\n"
"ds_read_b128 a[24:27], v100 offset:4096\n"
"s_waitcnt lgkmcnt(9)\n"
"v_mfma_f32_16x16x32_bf16 v[56:59], a[8:11], a[32:35], v[56:59]\n"
"ds_read_b128 a[72:75], v127 offset:0\n"
"v_mfma_f32_16x16x32_bf16 v[60:63], a[8:11], a[36:39], v[60:63]\n"
"ds_read_b128 a[28:31], v100 offset:4608\n"
"v_mfma_f32_16x16x32_bf16 v[64:67], a[8:11], a[56:59], v[64:67]\n"
"ds_read_b128 a[76:79], v130 offset:0\n"
"v_mfma_f32_16x16x32_bf16 v[88:91], v[92:95], a[48:51], v[88:91]\n"
"buffer_load_dwordx4 %[vW2], %[srdW], s60 offen offset:2048 lds\n"
"s_waitcnt lgkmcnt(10)\n"
"v_mfma_f32_16x16x32_bf16 v[68:71], a[12:15], a[32:35], v[68:71]\n"
"buffer_load_dwordx4 %[vW3], %[srdW], s60 offen offset:3072 lds\n"
"v_mfma_f32_16x16x32_bf16 v[72:75], a[12:15], a[36:39], v[72:75]\n"
"s_add_u32 m0, %[m0W], 4096\n"
"s_mov_b32 exec_hi, 0\n"
"buffer_load_dwordx4 %[vW4], %[srdW], s60 offen lds\n"
"s_mov_b32 exec_hi, -1\n"
"v_mfma_f32_16x16x32_bf16 v[76:79], a[12:15], a[60:63], v[76:79]\n"
"s_waitcnt lgkmcnt(4)\n"
"v_mfma_f32_16x16x32_bf16 v[32:35], a[16:19], a[40:43], v[32:35]\n"
"v_mfma_f32_16x16x32_bf16 v[36:39], a[16:19], a[44:47], v[36:39]\n"
"v_mfma_f32_16x16x32_bf16 v[40:43], a[16:19], a[64:67], v[40:43]\n"
"s_waitcnt vmcnt(14)\n"
"s_barrier\n"
"ds_read_b128 a[0:3], v97 offset:26880\n"
"v_mfma_f32_16x16x32_bf16 v[80:83], v[92:95], a[40:43], v[80:83]\n"
"ds_read_b128 a[32:35], v103 offset:26880\n"
"v_mfma_f32_16x16x32_bf16 v[44:47], a[20:23], a[40:43], v[44:47]\n"
"ds_read_b128 a[36:39], v103 offset:28928\n"
"v_mfma_f32_16x16x32_bf16 v[48:51], a[20:23], a[44:47], v[48:51]\n"
"ds_read_b128 a[48:51], v109 offset:26880\n"
"v_mfma_f32_16x16x32_bf16 v[52:55], a[20:23], a[68:71], v[52:55]\n"
"ds_read_b128 a[4:7], v97 offset:27392\n"
"v_mfma_f32_16x16x32_bf16 v[84:87], v[92:95], a[44:47], v[84:87]\n"
"ds_read_b128 a[52:55], v112 offset:26880\n"
"s_waitcnt lgkmcnt(8)\n"
"v_mfma_f32_16x16x32_bf16 v[56:59], a[24:27], a[40:43], v[56:59]\n"
"ds_read_b128 a[8:11], v97 offset:30976\n"
"v_mfma_f32_16x16x32_bf16 v[60:63], a[24:27], a[44:47], v[60:63]\n"
"ds_read_b128 a[56:59], v115 offset:26880\n"
"v_mfma_f32_16x16x32_bf16 v[64:67], a[24:27], a[72:75], v[64:67]\n"
"ds_read_b128 a[12:15], v97 offset:31488\n"
"v_mfma_f32_16x16x32_bf16 v[88:91], v[92:95], a[64:67], v[88:91]\n"
"ds_read_b128 a[60:63], v118 offset:26880\n"
"s_waitcnt lgkmcnt(10)\n"
"v_mfma_f32_16x16x32_bf16 v[68:71], a[28:31], a[40:43], v[68:71]\n"
"s_movk_i32 s60, 768\n"
"s_add_u32 m0, %[m0X], 26880\n"
"s_nop 0\n"
"buffer_load_dwordx4 %[vXd0], %[srdX], s60 offen offset:0 lds\n"
"v_mfma_f32_16x16x32_bf16 v[72:75], a[28:31], a[44:47], v[72:75]\n"
"buffer_load_dwordx4 %[vXd1], %[srdX], s60 offen offset:1024 lds\n"
"v_mfma_f32_16x16x32_bf16 v[76:79], a[28:31], a[76:79], v[76:79]\n"
"s_add_u32 m0, %[m0W], 26880\n"
"s_nop 0\n"
"buffer_load_dwordx4 %[vW0], %[srdW], s60 offen offset:0 lds\n"
"s_waitcnt lgkmcnt(4)\n"
"v_mfma_f32_16x16x32_bf16 v[32:35], a[0:3], a[32:35], v[32:35]\n"
"buffer_load_dwordx4 %[vW1], %[srdW], s60 offen offset:1024 lds\n"
"v_mfma_f32_16x16x32_bf16 v[36:39], a[0:3], a[36:39], v[36:39]\n"
"ds_read_b128 a[16:19], v100 offset:26880\n"
"v_mfma_f32_16x16x32_bf16 v[40:43], a[0:3], a[48:51], v[40:43]\n"
"ds_read_b128 a[40:43], v106 offset:26880\n"
"v_mfma_f32_16x16x32_bf16 v[80:83], v[92:95], a[32:35], v[80:83]\n"
"ds_read_b128 a[44:47], v106 offset:28928\n"
"v_mfma_f32_16x16x32_bf16 v[44:47], a[4:7], a[32:35], v[44:47]\n"
"ds_read_b128 a[64:67], v121 offset:26880\n"
"v_mfma_f32_16x16x32_bf16 v[48:51], a[4:7], a[36:39], v[48:51]\n"
"ds_read_b128 a[20:23], v100 offset:27392\n"
"v_mfma_f32_16x16x32_bf16 v[52:55], a[4:7], a[52:55], v[52:55]\n"
"ds_read_b128 a[68:71], v124 offset:26880\n"
"v_mfma_f32_16x16x32_bf16 v[84:87], v[92:95], a[36:39], v[84:87]\n"
"ds_read_b128 a[24:27], v100 offset:30976\n"
"s_waitcnt lgkmcnt(9)\n"
"v_mfma_f32_16x16x32_bf16 v[56:59], a[8:11], a[32:35], v[56:59]\n"
"ds_read_b128 a[72:75], v127 offset:26880\n"
"v_mfma_f32_16x16x32_bf16 v[60:63], a[8:11], a[36:39], v[60:63]\n"
"ds_read_b128 a[28:31], v100 offset:31488\n"
"v_mfma_f32_16x16x32_bf16 v[64:67], a[8:11], a[56:59], v[64:67]\n"
"ds_read_b128 a[76:79], v130 offset:26880\n"
"v_mfma_f32_16x16x32_bf16 v[88:91], v[92:95], a[48:51], v[88:91]\n"
"buffer_load_dwordx4 %[vW2], %[srdW], s60 offen offset:2048 lds\n"
"s_waitcnt lgkmcnt(10)\n"
"v_mfma_f32_16x16x32_bf16 v[68:71], a[12:15], a[32:35], v[68:71]\n"
"buffer_load_dwordx4 %[vW3], %[srdW], s60 offen offset:3072 lds\n"
"v_mfma_f32_16x16x32_bf16 v[72:75], a[12:15], a[36:39], v[72:75]\n"
"s_add_u32 m0, %[m0W], 30976\n"
"s_mov_b32 exec_hi, 0\n"
"buffer_load_dwordx4 %[vW4], %[srdW], s60 offen lds\n"
"s_mov_b32 exec_hi, -1\n"
"v_mfma_f32_16x16x32_bf16 v[76:79], a[12:15], a[60:63], v[76:79]\n"
"s_waitcnt lgkmcnt(4)\n"
"v_mfma_f32_16x16x32_bf16 v[32:35], a[16:19], a[40:43], v[32:35]\n"
"v_mfma_f32_16x16x32_bf16 v[36:39], a[16:19], a[44:47], v[36:39]\n"
"v_mfma_f32_16x16x32_bf16 v[40:43], a[16:19], a[64:67], v[40:43]\n"
"s_waitcnt vmcnt(14)\n"
"s_barrier\n"
"ds_read_b128 a[0:3], v98 offset:0\n"
"v_mfma_f32_16x16x32_bf16 v[80:83], v[92:95], a[40:43], v[80:83]\n"
"ds_read_b128 a[32:35], v104 offset:0\n"
"v_mfma_f32_16x16x32_bf16 v[44:47], a[20:23], a[40:43], v[44:47]\n"
"ds_read_b128 a[36:39], v104 offset:2048\n"
"v_mfma_f32_16x16x32_bf16 v[48:51], a[20:23], a[44:47], v[48:51]\n"
"ds_read_b128 a[48:51], v110 offset:0\n"
"v_mfma_f32_16x16x32_bf16 v[52:55], a[20:23], a[68:71], v[52:55]\n"
"ds_read_b128 a[4:7], v98 offset:512\n"
"v_mfma_f32_16x16x32_bf16 v[84:87], v[92:95], a[44:47], v[84:87]\n"
"ds_read_b128 a[52:55], v113 offset:0\n"
"s_waitcnt lgkmcnt(8)\n"
"v_mfma_f32_16x16x32_bf16 v[56:59], a[24:27], a[40:43], v[56:59]\n"
"ds_read_b128 a[8:11], v98 offset:4096\n"
"v_mfma_f32_16x16x32_bf16 v[60:63], a[24:27], a[44:47], v[60:63]\n"
"ds_read_b128 a[56:59], v116 offset:0\n"
"v_mfma_f32_16x16x32_bf16 v[64:67], a[24:27], a[72:75], v[64:67]\n"
"ds_read_b128 a[12:15], v98 offset:4608\n"
"v_mfma_f32_16x16x32_bf16 v[88:91], v[92:95], a[64:67], v[88:91]\n"
"ds_read_b128 a[60:63], v119 offset:0\n"
"s_waitcnt lgkmcnt(10)\n"
"v_mfma_f32_16x16x32_bf16 v[68:71], a[28:31], a[40:43], v[68:71]\n"
"s_movk_i32 s60, 896\n"
"s_add_u32 m0, %[m0X], 53760\n"
"s_nop 0\n"
"buffer_load_dwordx4 %[vXd0], %[srdX], s60 offen offset:0 lds\n"
"v_mfma_f32_16x16x32_bf16 v[72:75], a[28:31], a[44:47], v[72:75]\n"
"buffer_load_dwordx4 %[vXd1], %[srdX], s60 offen offset:1024 lds\n"
"v_mfma_f32_16x16x32_bf16 v[76:79], a[28:31], a[76:79], v[76:79]\n"
"s_add_u32 m0, %[m0W], 53760\n"
"s_nop 0\n"
"buffer_load_dwordx4 %[vW0], %[srdW], s60 offen offset:0 lds\n"
"s_waitcnt lgkmcnt(4)\n"
"v_mfma_f32_16x16x32_bf16 v[32:35], a[0:3], a[32:35], v[32:35]\n"
"buffer_load_dwordx4 %[vW1], %[srdW], s60 offen offset:1024 lds\n"
"v_mfma_f32_16x16x32_bf16 v[36:39], a[0:3], a[36:39], v[36:39]\n"
"ds_read_b128 a[16:19], v101 offset:0\n"
"v_mfma_f32_16x16x32_bf16 v[40:43], a[0:3], a[48:51], v[40:43]\n"
"ds_read_b128 a[40:43], v107 offset:0\n"
"v_mfma_f32_16x16x32_bf16 v[80:83], v[92:95], a[32:35], v[80:83]\n"
"ds_read_b128 a[44:47], v107 offset:2048\n"
"v_mfma_f32_16x16x32_bf16 v[44:47], a[4:7], a[32:35], v[44:47]\n"
"ds_read_b128 a[64:67], v122 offset:0\n"
"v_mfma_f32_16x16x32_bf16 v[48:51], a[4:7], a[36:39], v[48:51]\n"
"ds_read_b128 a[20:23], v101 offset:512\n"
"v_mfma_f32_16x16x32_bf16 v[52:55], a[4:7], a[52:55], v[52:55]\n"
"ds_read_b128 a[68:71], v125 offset:0\n"
"v_mfma_f32_16x16x32_bf16 v[84:87], v[92:95], a[36:39], v[84:87]\n"
"ds_read_b128 a[24:27], v101 offset:4096\n"
"s_waitcnt lgkmcnt(9)\n"
"v_mfma_f32_16x16x32_bf16 v[56:59], a[8:11], a[32:35], v[56:59]\n"
"ds_read_b128 a[72:75], v128 offset:0\n"
"v_mfma_f32_16x16x32_bf16 v[60:63], a[8:11], a[36:39], v[60:63]\n"
"ds_read_b128 a[28:31], v101 offset:4608\n"
"v_mfma_f32_16x16x32_bf16 v[64:67], a[8:11], a[56:59], v[64:67]\n"
"ds_read_b128 a[76:79], v131 offset:0\n"
"v_mfma_f32_16x16x32_bf16 v[88:91], v[92:95], a[48:51], v[88:91]\n"
"buffer_load_dwordx4 %[vW2], %[srdW], s60 offen offset:2048 lds\n"
"s_waitcnt lgkmcnt(10)\n"
"v_mfma_f32_16x16x32_bf16 v[68:71], a[12:15], a[32:35], v[68:71]\n"
"buffer_load_dwordx4 %[vW3], %[srdW], s60 offen offset:3072 lds\n"
"v_mfma_f32_16x16x32_bf16 v[72:75], a[12:15], a[36:39], v[72:75]\n"
"s_add_u32 m0, %[m0W], 57856\n"
"s_mov_b32 exec_hi, 0\n"
"buffer_load_dwordx4 %[vW4], %[srdW], s60 offen lds\n"
"s_mov_b32 exec_hi, -1\n"
"v_mfma_f32_16x16x32_bf16 v[76:79], a[12:15], a[60:63], v[76:79]\n"
"s_waitcnt lgkmcnt(4)\n"
"v_mfma_f32_16x16x32_bf16 v[32:35], a[16:19], a[40:43], v[32:35]\n"
"v_mfma_f32_16x16x32_bf16 v[36:39], a[16:19], a[44:47], v[36:39]\n"
"v_mfma_f32_16x16x32_bf16 v[40:43], a[16:19], a[64:67], v[40:43]\n"
"s_waitcnt vmcnt(14)\n"
"s_barrier\n"
"ds_read_b128 a[0:3], v96 offset:0\n"
"v_mfma_f32_16x16x32_bf16 v[80:83], v[92:95], a[40:43], v[80:83]\n"
"ds_read_b128 a[32:35], v102 offset:0\n"
"v_mfma_f32_16x16x32_bf16 v[44:47], a[20:23], a[40:43], v[44:47]\n"
"ds_read_b128 a[36:39], v102 offset:2048\n"
"v_mfma_f32_16x16x32_bf16 v[48:51], a[20:23], a[44:47], v[48:51]\n"
"ds_read_b128 a[48:51], v108 offset:0\n"
"v_mfma_f32_16x16x32_bf16 v[52:55], a[20:23], a[68:71], v[52:55]\n"
"ds_read_b128 a[4:7], v96 offset:512\n"
"v_mfma_f32_16x16x32_bf16 v[84:87], v[92:95], a[44:47], v[84:87]\n"
"ds_read_b128 a[52:55], v111 offset:0\n"
"s_waitcnt lgkmcnt(8)\n"
"v_mfma_f32_16x16x32_bf16 v[56:59], a[24:27], a[40:43], v[56:59]\n"
"ds_read_b128 a[8:11], v96 offset:4096\n"
"v_mfma_f32_16x16x32_bf16 v[60:63], a[24:27], a[44:47], v[60:63]\n"
"ds_read_b128 a[56:59], v114 offset:0\n"
"v_mfma_f32_16x16x32_bf16 v[64:67], a[24:27], a[72:75], v[64:67]\n"
"ds_read_b128 a[12:15], v96 offset:4608\n"
"v_mfma_f32_16x16x32_bf16 v[88:91], v[92:95], a[64:67], v[88:91]\n"
"ds_read_b128 a[60:63], v117 offset:0\n"
"s_waitcnt lgkmcnt(10)\n"
"v_mfma_f32_16x16x32_bf16 v[68:71], a[28:31], a[40:43], v[68:71]\n"
"s_movk_i32 s60, 1024\n"
"s_add_u32 m0, %[m0X], 80640\n"
"s_nop 0\n"
"buffer_load_dwordx4 %[vXd0], %[srdX], s60 offen offset:0 lds\n"
"v_mfma_f32_16x16x32_bf16 v[72:75], a[28:31], a[44:47], v[72:75]\n"
"buffer_load_dwordx4 %[vXd1], %[srdX], s60 offen offset:1024 lds\n"
"v_mfma_f32_16x16x32_bf16 v[76:79], a[28:31], a[76:79], v[76:79]\n"
"s_add_u32 m0, %[m0W], 80640\n"
"s_nop 0\n"
"buffer_load_dwordx4 %[vW0], %[srdW], s60 offen offset:0 lds\n"
"s_waitcnt lgkmcnt(4)\n"
"v_mfma_f32_16x16x32_bf16 v[32:35], a[0:3], a[32:35], v[32:35]\n"
"buffer_load_dwordx4 %[vW1], %[srdW], s60 offen offset:1024 lds\n"
"v_mfma_f32_16x16x32_bf16 v[36:39], a[0:3], a[36:39], v[36:39]\n"
"ds_read_b128 a[16:19], v99 offset:0\n"
"v_mfma_f32_16x16x32_bf16 v[40:43], a[0:3], a[48:51], v[40:43]\n"
"ds_read_b128 a[40:43], v105 offset:0\n"
"v_mfma_f32_16x16x32_bf16 v[80:83], v[92:95], a[32:35], v[80:83]\n"
"ds_read_b128 a[44:47], v105 offset:2048\n"
"v_mfma_f32_16x16x32_bf16 v[44:47], a[4:7], a[32:35], v[44:47]\n"
"ds_read_b128 a[64:67], v120 offset:0\n"
"v_mfma_f32_16x16x32_bf16 v[48:51], a[4:7], a[36:39], v[48:51]\n"
"ds_read_b128 a[20:23], v99 offset:512\n"
"v_mfma_f32_16x16x32_bf16 v[52:55], a[4:7], a[52:55], v[52:55]\n"
"ds_read_b128 a[68:71], v123 offset:0\n"
"v_mfma_f32_16x16x32_bf16 v[84:87], v[92:95], a[36:39], v[84:87]\n"
"ds_read_b128 a[24:27], v99 offset:4096\n"
"s_waitcnt lgkmcnt(9)\n"
"v_mfma_f32_16x16x32_bf16 v[56:59], a[8:11], a[32:35], v[56:59]\n"
"ds_read_b128 a[72:75], v126 offset:0\n"
"v_mfma_f32_16x16x32_bf16 v[60:63], a[8:11], a[36:39], v[60:63]\n"
"ds_read_b128 a[28:31], v99 offset:4608\n"
"v_mfma_f32_16x16x32_bf16 v[64:67], a[8:11], a[56:59], v[64:67]\n"
"ds_read_b128 a[76:79], v129 offset:0\n"
"v_mfma_f32_16x16x32_bf16 v[88:91], v[92:95], a[48:51], v[88:91]\n"
"buffer_load_dwordx4 %[vW2], %[srdW], s60 offen offset:2048 lds\n"
"s_waitcnt lgkmcnt(10)\n"
"v_mfma_f32_16x16x32_bf16 v[68:71], a[12:15], a[32:35], v[68:71]\n"
"buffer_load_dwordx4 %[vW3], %[srdW], s60 offen offset:3072 lds\n"
"v_mfma_f32_16x16x32_bf16 v[72:75], a[12:15], a[36:39], v[72:75]\n"
"s_add_u32 m0, %[m0W], 84736\n"
"s_mov_b32 exec_hi, 0\n"
"buffer_load_dwordx4 %[vW4], %[srdW], s60 offen lds\n"
"s_mov_b32 exec_hi, -1\n"
"v_mfma_f32_16x16x32_bf16 v[76:79], a[12:15], a[60:63], v[76:79]\n"
"s_waitcnt lgkmcnt(4)\n"
"v_mfma_f32_16x16x32_bf16 v[32:35], a[16:19], a[40:43], v[32:35]\n"
"v_mfma_f32_16x16x32_bf16 v[36:39], a[16:19], a[44:47], v[36:39]\n"
"v_mfma_f32_16x16x32_bf16 v[40:43], a[16:19], a[64:67], v[40:43]\n"
"s_waitcnt vmcnt(14)\n"
"s_barrier\n"
"ds_read_b128 a[0:3], v96 offset:26880\n"
"v_mfma_f32_16x16x32_bf16 v[80:83], v[92:95], a[40:43], v[80:83]\n"
"ds_read_b128 a[32:35], v102 offset:26880\n"
"v_mfma_f32_16x16x32_bf16 v[44:47], a[20:23], a[40:43], v[44:47]\n"
"ds_read_b128 a[36:39], v102 offset:28928\n"
"v_mfma_f32_16x16x32_bf16 v[48:51], a[20:23], a[44:47], v[48:51]\n"
"ds_read_b128 a[48:51], v108 offset:26880\n"
"v_mfma_f32_16x16x32_bf16 v[52:55], a[20:23], a[68:71], v[52:55]\n"
"ds_read_b128 a[4:7], v96 offset:27392\n"
"v_mfma_f32_16x16x32_bf16 v[84:87], v[92:95], a[44:47], v[84:87]\n"
"ds_read_b128 a[52:55], v111 offset:26880\n"
"s_waitcnt lgkmcnt(8)\n"
"v_mfma_f32_16x16x32_bf16 v[56:59], a[24:27], a[40:43], v[56:59]\n"
"ds_read_b128 a[8:11], v96 offset:30976\n"
"v_mfma_f32_16x16x32_bf16 v[60:63], a[24:27], a[44:47], v[60:63]\n"
"ds_read_b128 a[56:59], v114 offset:26880\n"
"v_mfma_f32_16x16x32_bf16 v[64:67], a[24:27], a[72:75], v[64:67]\n"
"ds_read_b128 a[12:15], v96 offset:31488\n"
"v_mfma_f32_16x16x32_bf16 v[88:91], v[92:95], a[64:67], v[88:91]\n"
"ds_read_b128 a[60:63], v117 offset:26880\n"
"s_waitcnt lgkmcnt(10)\n"
"v_mfma_f32_16x16x32_bf16 v[68:71], a[28:31], a[40:43], v[68:71]\n"
"v_mfma_f32_16x16x32_bf16 v[72:75], a[28:31], a[44:47], v[72:75]\n"
"v_mfma_f32_16x16x32_bf16 v[76:79], a[28:31], a[76:79], v[76:79]\n"
"s_waitcnt lgkmcnt(4)\n"
"v_mfma_f32_16x16x32_bf16 v[32:35], a[0:3], a[32:35], v[32:35]\n"
"v_mfma_f32_16x16x32_bf16 v[36:39], a[0:3], a[36:39], v[36:39]\n"
"ds_read_b128 a[16:19], v99 offset:26880\n"
"v_mfma_f32_16x16x32_bf16 v[40:43], a[0:3], a[48:51], v[40:43]\n"
"ds_read_b128 a[40:43], v105 offset:26880\n"
"v_mfma_f32_16x16x32_bf16 v[80:83], v[92:95], a[32:35], v[80:83]\n"
"ds_read_b128 a[44:47], v105 offset:28928\n"
"v_mfma_f32_16x16x32_bf16 v[44:47], a[4:7], a[32:35], v[44:47]\n"
"ds_read_b128 a[64:67], v120 offset:26880\n"
"v_mfma_f32_16x16x32_bf16 v[48:51], a[4:7], a[36:39], v[48:51]\n"
"ds_read_b128 a[20:23], v99 offset:27392\n"
"v_mfma_f32_16x16x32_bf16 v[52:55], a[4:7], a[52:55], v[52:55]\n"
"ds_read_b128 a[68:71], v123 offset:26880\n"
"v_mfma_f32_16x16x32_bf16 v[84:87], v[92:95], a[36:39], v[84:87]\n"
"ds_read_b128 a[24:27], v99 offset:30976\n"
"s_waitcnt lgkmcnt(9)\n"
"v_mfma_f32_16x16x32_bf16 v[56:59], a[8:11], a[32:35], v[56:59]\n"
"ds_read_b128 a[72:75], v126 offset:26880\n"
"v_mfma_f32_16x16x32_bf16 v[60:63], a[8:11], a[36:39], v[60:63]\n"
"ds_read_b128 a[28:31], v99 offset:31488\n"
"v_mfma_f32_16x16x32_bf16 v[64:67], a[8:11], a[56:59], v[64:67]\n"
"ds_read_b128 a[76:79], v129 offset:26880\n"
"v_mfma_f32_16x16x32_bf16 v[88:91], v[92:95], a[48:51], v[88:91]\n"
"s_waitcnt lgkmcnt(10)\n"
"v_mfma_f32_16x16x32_bf16 v[68:71], a[12:15], a[32:35], v[68:71]\n"
"v_mfma_f32_16x16x32_bf16 v[72:75], a[12:15], a[36:39], v[72:75]\n"
"v_mfma_f32_16x16x32_bf16 v[76:79], a[12:15], a[60:63], v[76:79]\n"
"s_waitcnt lgkmcnt(4)\n"
"v_mfma_f32_16x16x32_bf16 v[32:35], a[16:19], a[40:43], v[32:35]\n"
"v_mfma_f32_16x16x32_bf16 v[36:39], a[16:19], a[44:47], v[36:39]\n"
"v_mfma_f32_16x16x32_bf16 v[40:43], a[16:19], a[64:67], v[40:43]\n"
"s_waitcnt vmcnt(7)\n"
"s_barrier\n"
"ds_read_b128 a[0:3], v97 offset:0\n"
"v_mfma_f32_16x16x32_bf16 v[80:83], v[92:95], a[40:43], v[80:83]\n"
"ds_read_b128 a[32:35], v103 offset:0\n"
"v_mfma_f32_16x16x32_bf16 v[44:47], a[20:23], a[40:43], v[44:47]\n"
"ds_read_b128 a[36:39], v103 offset:2048\n"
"v_mfma_f32_16x16x32_bf16 v[48:51], a[20:23], a[44:47], v[48:51]\n"
"ds_read_b128 a[48:51], v109 offset:0\n"
"v_mfma_f32_16x16x32_bf16 v[52:55], a[20:23], a[68:71], v[52:55]\n"
"ds_read_b128 a[4:7], v97 offset:512\n"
"v_mfma_f32_16x16x32_bf16 v[84:87], v[92:95], a[44:47], v[84:87]\n"
"ds_read_b128 a[52:55], v112 offset:0\n"
"s_waitcnt lgkmcnt(8)\n"
"v_mfma_f32_16x16x32_bf16 v[56:59], a[24:27], a[40:43], v[56:59]\n"
"ds_read_b128 a[8:11], v97 offset:4096\n"
"v_mfma_f32_16x16x32_bf16 v[60:63], a[24:27], a[44:47], v[60:63]\n"
"ds_read_b128 a[56:59], v115 offset:0\n"
"v_mfma_f32_16x16x32_bf16 v[64:67], a[24:27], a[72:75], v[64:67]\n"
"ds_read_b128 a[12:15], v97 offset:4608\n"
"v_mfma_f32_16x16x32_bf16 v[88:91], v[92:95], a[64:67], v[88:91]\n"
"ds_read_b128 a[60:63], v118 offset:0\n"
"s_waitcnt lgkmcnt(10)\n"
"v_mfma_f32_16x16x32_bf16 v[68:71], a[28:31], a[40:43], v[68:71]\n"
"v_mfma_f32_16x16x32_bf16 v[72:75], a[28:31], a[44:47], v[72:75]\n"
"v_mfma_f32_16x16x32_bf16 v[76:79], a[28:31], a[76:79], v[76:79]\n"
"s_waitcnt lgkmcnt(4)\n"
"v_mfma_f32_16x16x32_bf16 v[32:35], a[0:3], a[32:35], v[32:35]\n"
"v_mfma_f32_16x16x32_bf16 v[36:39], a[0:3], a[36:39], v[36:39]\n"
"ds_read_b128 a[16:19], v100 offset:0\n"
"v_mfma_f32_16x16x32_bf16 v[40:43], a[0:3], a[48:51], v[40:43]\n"
"ds_read_b128 a[40:43], v106 offset:0\n"
"v_mfma_f32_16x16x32_bf16 v[80:83], v[92:95], a[32:35], v[80:83]\n"
"ds_read_b128 a[44:47], v106 offset:2048\n"
"v_mfma_f32_16x16x32_bf16 v[44:47], a[4:7], a[32:35], v[44:47]\n"
"ds_read_b128 a[64:67], v121 offset:0\n"
"v_mfma_f32_16x16x32_bf16 v[48:51], a[4:7], a[36:39], v[48:51]\n"
"ds_read_b128 a[20:23], v100 offset:512\n"
"v_mfma_f32_16x16x32_bf16 v[52:55], a[4:7], a[52:55], v[52:55]\n"
"ds_read_b128 a[68:71], v124 offset:0\n"
"v_mfma_f32_16x16x32_bf16 v[84:87], v[92:95], a[36:39], v[84:87]\n"
"ds_read_b128 a[24:27], v100 offset:4096\n"
"s_waitcnt lgkmcnt(9)\n"
"v_mfma_f32_16x16x32_bf16 v[56:59], a[8:11], a[32:35], v[56:59]\n"
"ds_read_b128 a[72:75], v127 offset:0\n"
"v_mfma_f32_16x16x32_bf16 v[60:63], a[8:11], a[36:39], v[60:63]\n"
"ds_read_b128 a[28:31], v100 offset:4608\n"
"v_mfma_f32_16x16x32_bf16 v[64:67], a[8:11], a[56:59], v[64:67]\n"
"ds_read_b128 a[76:79], v130 offset:0\n"
"v_mfma_f32_16x16x32_bf16 v[88:91], v[92:95], a[48:51], v[88:91]\n"
"s_waitcnt lgkmcnt(10)\n"
"v_mfma_f32_16x16x32_bf16 v[68:71], a[12:15], a[32:35], v[68:71]\n"
"v_mfma_f32_16x16x32_bf16 v[72:75], a[12:15], a[36:39], v[72:75]\n"
"v_mfma_f32_16x16x32_bf16 v[76:79], a[12:15], a[60:63], v[76:79]\n"
"s_waitcnt lgkmcnt(4)\n"
"v_mfma_f32_16x16x32_bf16 v[32:35], a[16:19], a[40:43], v[32:35]\n"
"v_mfma_f32_16x16x32_bf16 v[36:39], a[16:19], a[44:47], v[36:39]\n"
"v_mfma_f32_16x16x32_bf16 v[40:43], a[16:19], a[64:67], v[40:43]\n"
"s_waitcnt vmcnt(0)\n"
"s_barrier\n"
"ds_read_b128 a[0:3], v97 offset:26880\n"
"v_mfma_f32_16x16x32_bf16 v[80:83], v[92:95], a[40:43], v[80:83]\n"
"ds_read_b128 a[32:35], v103 offset:26880\n"
"v_mfma_f32_16x16x32_bf16 v[44:47], a[20:23], a[40:43], v[44:47]\n"
"ds_read_b128 a[36:39], v103 offset:28928\n"
"v_mfma_f32_16x16x32_bf16 v[48:51], a[20:23], a[44:47], v[48:51]\n"
"ds_read_b128 a[48:51], v109 offset:26880\n"
"v_mfma_f32_16x16x32_bf16 v[52:55], a[20:23], a[68:71], v[52:55]\n"
"ds_read_b128 a[4:7], v97 offset:27392\n"
"v_mfma_f32_16x16x32_bf16 v[84:87], v[92:95], a[44:47], v[84:87]\n"
"ds_read_b128 a[52:55], v112 offset:26880\n"
"s_waitcnt lgkmcnt(8)\n"
"v_mfma_f32_16x16x32_bf16 v[56:59], a[24:27], a[40:43], v[56:59]\n"
"ds_read_b128 a[8:11], v97 offset:30976\n"
"v_mfma_f32_16x16x32_bf16 v[60:63], a[24:27], a[44:47], v[60:63]\n"
"ds_read_b128 a[56:59], v115 offset:26880\n"
"v_mfma_f32_16x16x32_bf16 v[64:67], a[24:27], a[72:75], v[64:67]\n"
"ds_read_b128 a[12:15], v97 offset:31488\n"
"v_mfma_f32_16x16x32_bf16 v[88:91], v[92:95], a[64:67], v[88:91]\n"
"ds_read_b128 a[60:63], v118 offset:26880\n"
"s_waitcnt lgkmcnt(10)\n"
"v_mfma_f32_16x16x32_bf16 v[68:71], a[28:31], a[40:43], v[68:71]\n"
"v_mfma_f32_16x16x32_bf16 v[72:75], a[28:31], a[44:47], v[72:75]\n"
"v_mfma_f32_16x16x32_bf16 v[76:79], a[28:31], a[76:79], v[76:79]\n"
"s_waitcnt lgkmcnt(4)\n"
"v_mfma_f32_16x16x32_bf16 v[32:35], a[0:3], a[32:35], v[32:35]\n"
"v_mfma_f32_16x16x32_bf16 v[36:39], a[0:3], a[36:39], v[36:39]\n"
"ds_read_b128 a[16:19], v100 offset:26880\n"
"v_mfma_f32_16x16x32_bf16 v[40:43], a[0:3], a[48:51], v[40:43]\n"
"ds_read_b128 a[40:43], v106 offset:26880\n"
"v_mfma_f32_16x16x32_bf16 v[80:83], v[92:95], a[32:35], v[80:83]\n"
"ds_read_b128 a[44:47], v106 offset:28928\n"
"v_mfma_f32_16x16x32_bf16 v[44:47], a[4:7], a[32:35], v[44:47]\n"
"ds_read_b128 a[64:67], v121 offset:26880\n"
"v_mfma_f32_16x16x32_bf16 v[48:51], a[4:7], a[36:39], v[48:51]\n"
"ds_read_b128 a[20:23], v100 offset:27392\n"
"v_mfma_f32_16x16x32_bf16 v[52:55], a[4:7], a[52:55], v[52:55]\n"
"ds_read_b128 a[68:71], v124 offset:26880\n"
"v_mfma_f32_16x16x32_bf16 v[84:87], v[92:95], a[36:39], v[84:87]\n"
"ds_read_b128 a[24:27], v100 offset:30976\n"
"s_waitcnt lgkmcnt(9)\n"
"v_mfma_f32_16x16x32_bf16 v[56:59], a[8:11], a[32:35], v[56:59]\n"
"ds_read_b128 a[72:75], v127 offset:26880\n"
"v_mfma_f32_16x16x32_bf16 v[60:63], a[8:11], a[36:39], v[60:63]\n"
"ds_read_b128 a[28:31], v100 offset:31488\n"
"v_mfma_f32_16x16x32_bf16 v[64:67], a[8:11], a[56:59], v[64:67]\n"
"ds_read_b128 a[76:79], v130 offset:26880\n"
"v_mfma_f32_16x16x32_bf16 v[88:91], v[92:95], a[48:51], v[88:91]\n"
"s_waitcnt lgkmcnt(10)\n"
"v_mfma_f32_16x16x32_bf16 v[68:71], a[12:15], a[32:35], v[68:71]\n"
"v_mfma_f32_16x16x32_bf16 v[72:75], a[12:15], a[36:39], v[72:75]\n"
"v_mfma_f32_16x16x32_bf16 v[76:79], a[12:15], a[60:63], v[76:79]\n"
"s_waitcnt lgkmcnt(4)\n"
"v_mfma_f32_16x16x32_bf16 v[32:35], a[16:19], a[40:43], v[32:35]\n"
"v_mfma_f32_16x16x32_bf16 v[36:39], a[16:19], a[44:47], v[36:39]\n"
"v_mfma_f32_16x16x32_bf16 v[40:43], a[16:19], a[64:67], v[40:43]\n"
"v_mfma_f32_16x16x32_bf16 v[80:83], v[92:95], a[40:43], v[80:83]\n"
"v_mfma_f32_16x16x32_bf16 v[44:47], a[20:23], a[40:43], v[44:47]\n"
"v_mfma_f32_16x16x32_bf16 v[48:51], a[20:23], a[44:47], v[48:51]\n"
"v_mfma_f32_16x16x32_bf16 v[52:55], a[20:23], a[68:71], v[52:55]\n"
"v_mfma_f32_16x16x32_bf16 v[84:87], v[92:95], a[44:47], v[84:87]\n"
"s_waitcnt lgkmcnt(2)\n"
"v_mfma_f32_16x16x32_bf16 v[56:59], a[24:27], a[40:43], v[56:59]\n"
"v_mfma_f32_16x16x32_bf16 v[60:63], a[24:27], a[44:47], v[60:63]\n"
"v_mfma_f32_16x16x32_bf16 v[64:67], a[24:27], a[72:75], v[64:67]\n"
"v_mfma_f32_16x16x32_bf16 v[88:91], v[92:95], a[64:67], v[88:91]\n"
"s_waitcnt lgkmcnt(0)\n"
"v_mfma_f32_16x16x32_bf16 v[68:71], a[28:31], a[40:43], v[68:71]\n"
"v_mfma_f32_16x16x32_bf16 v[72:75], a[28:31], a[44:47], v[72:75]\n"
"v_mfma_f32_16x16x32_bf16 v[76:79], a[28:31], a[76:79], v[76:79]\n"
"s_nop 7\n"
"ds_write_b128 %[vEw], v[40:43] offset:0\n"
"ds_write_b128 %[vEw], v[52:55] offset:1024\n"
"ds_write_b128 %[vEw], v[64:67] offset:2048\n"
"ds_write_b128 %[vEw], v[76:79] offset:3072\n"
"s_waitcnt lgkmcnt(0)\n"
"s_barrier\n"
"ds_read_b128 v[100:103], %[vEr] offset:0\n"
"ds_read_b32 v116, %[vEg] offset:0\n"
"ds_read_b32 v117, %[vEg] offset:20\n"
"ds_read_b32 v118, %[vEg] offset:40\n"
"ds_read_b32 v119, %[vEg] offset:60\n"
"ds_read_b128 v[104:107], %[vEr] offset:1024\n"
"ds_read_b32 v120, %[vEg] offset:1024\n"
"ds_read_b32 v121, %[vEg] offset:1044\n"
"ds_read_b32 v122, %[vEg] offset:1064\n"
"ds_read_b32 v123, %[vEg] offset:1084\n"
"ds_read_b128 v[108:111], %[vEr] offset:2048\n"
"ds_read_b32 v124, %[vEg] offset:2048\n"
"ds_read_b32 v125, %[vEg] offset:2068\n"
"ds_read_b32 v126, %[vEg] offset:2088\n"
"ds_read_b32 v127, %[vEg] offset:2108\n"
"ds_read_b128 v[112:115], %[vEr] offset:3072\n"
"ds_read_b32 v128, %[vEg] offset:3072\n"
"ds_read_b32 v129, %[vEg] offset:3092\n"
"ds_read_b32 v130, %[vEg] offset:3112\n"
"ds_read_b32 v131, %[vEg] offset:3132\n"
"v_mov_b32 v92, %[inv]\n"
"v_mov_b32 v93, %[inv]\n"
"v_mov_b32 v94, %[eps]\n"
"v_mov_b32 v95, %[eps]\n"
"v_mov_b32 v96, %[sgn]\n"
"v_mov_b32 v97, %[sgn]\n"
"s_waitcnt lgkmcnt(15)\n"
"v_pk_mul_f32 v[100:101], v[100:101], v[92:93]\n"
"v_pk_fma_f32 v[132:133], v[100:101], v[100:101], v[94:95] neg_lo:[1,0,0] neg_hi:[1,0,0]\n"
"v_pk_fma_f32 v[116:117], v[116:117], v[92:93], v[132:133]\n"
"v_max_f32 v116, v116, v94\n"
"v_max_f32 v117, v117, v94\n"
"v_rsq_f32 v116, v116\n"
"v_rsq_f32 v117, v117\n"
"s_nop 0\n"
"v_pk_mul_f32 v[116:117], v[116:117], v[96:97]\n"
"v_pk_mul_f32 v[100:101], v[100:101], v[116:117] neg_lo:[1,0] neg_hi:[1,0]\n"
"v_pk_mul_f32 v[102:103], v[102:103], v[92:93]\n"
"v_pk_fma_f32 v[134:135], v[102:103], v[102:103], v[94:95] neg_lo:[1,0,0] neg_hi:[1,0,0]\n"
"v_pk_fma_f32 v[118:119], v[118:119], v[92:93], v[134:135]\n"
"v_max_f32 v118, v118, v94\n"
"v_max_f32 v119, v119, v94\n"
"v_rsq_f32 v118, v118\n"
"v_rsq_f32 v119, v119\n"
"s_nop 0\n"
"v_pk_mul_f32 v[118:119], v[118:119], v[96:97]\n"
"v_pk_mul_f32 v[102:103], v[102:103], v[118:119] neg_lo:[1,0] neg_hi:[1,0]\n"
"s_waitcnt lgkmcnt(10)\n"
"v_pk_mul_f32 v[104:105], v[104:105], v[92:93]\n"
"v_pk_fma_f32 v[132:133], v[104:105], v[104:105], v[94:95] neg_lo:[1,0,0] neg_hi:[1,0,0]\n"
"v_pk_fma_f32 v[120:121], v[120:121], v[92:93], v[132:133]\n"
"v_max_f32 v120, v120, v94\n"
"v_max_f32 v121, v121, v94\n"
"v_rsq_f32 v120, v120\n"
"v_rsq_f32 v121, v121\n"
"s_nop 0\n"
"v_pk_mul_f32 v[120:121], v[120:121], v[96:97]\n"
"v_pk_mul_f32 v[104:105], v[104:105], v[120:121] neg_lo:[1,0] neg_hi:[1,0]\n"
"v_pk_mul_f32 v[106:107], v[106:107], v[92:93]\n"
"v_pk_fma_f32 v[134:135], v[106:107], v[106:107], v[94:95] neg_lo:[1,0,0] neg_hi:[1,0,0]\n"
"v_pk_fma_f32 v[122:123], v[122:123], v[92:93], v[134:135]\n"
"v_max_f32 v122, v122, v94\n"
"v_max_f32 v123, v123, v94\n"
"v_rsq_f32 v122, v122\n"
"v_rsq_f32 v123, v123\n"
"s_nop 0\n"
"v_pk_mul_f32 v[122:123], v[122:123], v[96:97]\n"
"v_pk_mul_f32 v[106:107], v[106:107], v[122:123] neg_lo:[1,0] neg_hi:[1,0]\n"
"s_waitcnt lgkmcnt(5)\n"
"v_pk_mul_f32 v[108:109], v[108:109], v[92:93]\n"
"v_pk_fma_f32 v[132:133], v[108:109], v[108:109], v[94:95] neg_lo:[1,0,0] neg_hi:[1,0,0]\n"
"v_pk_fma_f32 v[124:125], v[124:125], v[92:93], v[132:133]\n"
"v_max_f32 v124, v124, v94\n"
"v_max_f32 v125, v125, v94\n"
"v_rsq_f32 v124, v124\n"
"v_rsq_f32 v125, v125\n"
"s_nop 0\n"
"v_pk_mul_f32 v[124:125], v[124:125], v[96:97]\n"
"v_pk_mul_f32 v[108:109], v[108:109], v[124:125] neg_lo:[1,0] neg_hi:[1,0]\n"
"v_pk_mul_f32 v[110:111], v[110:111], v[92:93]\n"
"v_pk_fma_f32 v[134:135], v[110:111], v[110:111], v[94:95] neg_lo:[1,0,0] neg_hi:[1,0,0]\n"
"v_pk_fma_f32 v[126:127], v[126:127], v[92:93], v[134:135]\n"
"v_max_f32 v126, v126, v94\n"
"v_max_f32 v127, v127, v94\n"
"v_rsq_f32 v126, v126\n"
"v_rsq_f32 v127, v127\n"
"s_nop 0\n"
"v_pk_mul_f32 v[126:127], v[126:127], v[96:97]\n"
"v_pk_mul_f32 v[110:111], v[110:111], v[126:127] neg_lo:[1,0] neg_hi:[1,0]\n"
"s_waitcnt lgkmcnt(0)\n"
"v_pk_mul_f32 v[112:113], v[112:113], v[92:93]\n"
"v_pk_fma_f32 v[132:133], v[112:113], v[112:113], v[94:95] neg_lo:[1,0,0] neg_hi:[1,0,0]\n"
"v_pk_fma_f32 v[128:129], v[128:129], v[92:93], v[132:133]\n"
"v_max_f32 v128, v128, v94\n"
"v_max_f32 v129, v129, v94\n"
"v_rsq_f32 v128, v128\n"
"v_rsq_f32 v129, v129\n"
"s_nop 0\n"
"v_pk_mul_f32 v[128:129], v[128:129], v[96:97]\n"
"v_pk_mul_f32 v[112:113], v[112:113], v[128:129] neg_lo:[1,0] neg_hi:[1,0]\n"
"v_pk_mul_f32 v[114:115], v[114:115], v[92:93]\n"
"v_pk_fma_f32 v[134:135], v[114:115], v[114:115], v[94:95] neg_lo:[1,0,0] neg_hi:[1,0,0]\n"
"v_pk_fma_f32 v[130:131], v[130:131], v[92:93], v[134:135]\n"
"v_max_f32 v130, v130, v94\n"
"v_max_f32 v131, v131, v94\n"
"v_rsq_f32 v130, v130\n"
"v_rsq_f32 v131, v131\n"
"s_nop 0\n"
"v_pk_mul_f32 v[130:131], v[130:131], v[96:97]\n"
"v_pk_mul_f32 v[114:115], v[114:115], v[130:131] neg_lo:[1,0] neg_hi:[1,0]\n"
"v_pk_mul_f32 v[132:133], v[100:101], v[80:81]\n"
"v_pk_fma_f32 v[32:33], v[32:33], v[116:117], v[132:133]\n"
"v_pk_mul_f32 v[134:135], v[102:103], v[80:81]\n"
"v_pk_fma_f32 v[34:35], v[34:35], v[118:119], v[134:135]\n"
"v_cvt_pk_bf16_f32 v136, v32, v33\n"
"v_cvt_pk_bf16_f32 v137, v34, v35\n"
"v_pk_mul_f32 v[132:133], v[104:105], v[80:81]\n"
"v_pk_fma_f32 v[44:45], v[44:45], v[120:121], v[132:133]\n"
"v_pk_mul_f32 v[134:135], v[106:107], v[80:81]\n"
"v_pk_fma_f32 v[46:47], v[46:47], v[122:123], v[134:135]\n"
"v_cvt_pk_bf16_f32 v138, v44, v45\n"
"v_cvt_pk_bf16_f32 v139, v46, v47\n"
"s_add_u32 s60, %[soffO], 0\n"
"buffer_store_dwordx4 v[136:139], %[vS], %[srdO], s60 offen sc1\n"
"v_pk_mul_f32 v[132:133], v[100:101], v[84:85]\n"
"v_pk_fma_f32 v[36:37], v[36:37], v[116:117], v[132:133]\n"
"v_pk_mul_f32 v[134:135], v[102:103], v[84:85]\n"
"v_pk_fma_f32 v[38:39], v[38:39], v[118:119], v[134:135]\n"
"v_cvt_pk_bf16_f32 v140, v36, v37\n"
"v_cvt_pk_bf16_f32 v141, v38, v39\n"
"v_pk_mul_f32 v[132:133], v[104:105], v[84:85]\n"
"v_pk_fma_f32 v[48:49], v[48:49], v[120:121], v[132:133]\n"
"v_pk_mul_f32 v[134:135], v[106:107], v[84:85]\n"
"v_pk_fma_f32 v[50:51], v[50:51], v[122:123], v[134:135]\n"
"v_cvt_pk_bf16_f32 v142, v48, v49\n"
"v_cvt_pk_bf16_f32 v143, v50, v51\n"
"s_add_u32 s60, %[soffO], 1024\n"
"buffer_store_dwordx4 v[140:143], %[vS], %[srdO], s60 offen sc1\n"
"v_pk_mul_f32 v[132:133], v[100:101], v[88:89]\n"
"v_pk_fma_f32 v[40:41], v[40:41], v[116:117], v[132:133]\n"
"v_pk_mul_f32 v[134:135], v[102:103], v[88:89]\n"
"v_pk_fma_f32 v[42:43], v[42:43], v[118:119], v[134:135]\n"
"v_cvt_pk_bf16_f32 v144, v40, v41\n"
"v_cvt_pk_bf16_f32 v145, v42, v43\n"
"v_pk_mul_f32 v[132:133], v[104:105], v[88:89]\n"
"v_pk_fma_f32 v[52:53], v[52:53], v[120:121], v[132:133]\n"
"v_pk_mul_f32 v[134:135], v[106:107], v[88:89]\n"
"v_pk_fma_f32 v[54:55], v[54:55], v[122:123], v[134:135]\n"
"v_cvt_pk_bf16_f32 v146, v52, v53\n"
"v_cvt_pk_bf16_f32 v147, v54, v55\n"
"s_add_u32 s60, %[soffO], 0\n"
"buffer_store_dwordx4 v[144:147], %[vSX], %[srdO], s60 offen sc1\n"
"v_pk_mul_f32 v[132:133], v[108:109], v[80:81]\n"
"v_pk_fma_f32 v[56:57], v[56:57], v[124:125], v[132:133]\n"
"v_pk_mul_f32 v[134:135], v[110:111], v[80:81]\n"
"v_pk_fma_f32 v[58:59], v[58:59], v[126:127], v[134:135]\n"
"v_cvt_pk_bf16_f32 v148, v56, v57\n"
"v_cvt_pk_bf16_f32 v149, v58, v59\n"
"v_pk_mul_f32 v[132:133], v[112:113], v[80:81]\n"
"v_pk_fma_f32 v[68:69], v[68:69], v[128:129], v[132:133]\n"
"v_pk_mul_f32 v[134:135], v[114:115], v[80:81]\n"
"v_pk_fma_f32 v[70:71], v[70:71], v[130:131], v[134:135]\n"
"v_cvt_pk_bf16_f32 v150, v68, v69\n"
"v_cvt_pk_bf16_f32 v151, v70, v71\n"
"s_add_u32 s60, %[soffO], 9216\n"
"buffer_store_dwordx4 v[148:151], %[vS], %[srdO], s60 offen sc1\n"
"v_pk_mul_f32 v[132:133], v[108:109], v[84:85]\n"
"v_pk_fma_f32 v[60:61], v[60:61], v[124:125], v[132:133]\n"
"v_pk_mul_f32 v[134:135], v[110:111], v[84:85]\n"
"v_pk_fma_f32 v[62:63], v[62:63], v[126:127], v[134:135]\n"
"v_cvt_pk_bf16_f32 v136, v60, v61\n"
"v_cvt_pk_bf16_f32 v137, v62, v63\n"
"v_pk_mul_f32 v[132:133], v[112:113], v[84:85]\n"
"v_pk_fma_f32 v[72:73], v[72:73], v[128:129], v[132:133]\n"
"v_pk_mul_f32 v[134:135], v[114:115], v[84:85]\n"
"v_pk_fma_f32 v[74:75], v[74:75], v[130:131], v[134:135]\n"
"v_cvt_pk_bf16_f32 v138, v72, v73\n"
"v_cvt_pk_bf16_f32 v139, v74, v75\n"
"s_add_u32 s60, %[soffO], 10240\n"
"buffer_store_dwordx4 v[136:139], %[vS], %[srdO], s60 offen sc1\n"
"v_pk_mul_f32 v[132:133], v[108:109], v[88:89]\n"
"v_pk_fma_f32 v[64:65], v[64:65], v[124:125], v[132:133]\n"
"v_pk_mul_f32 v[134:135], v[110:111], v[88:89]\n"
"v_pk_fma_f32 v[66:67], v[66:67], v[126:127], v[134:135]\n"
"v_cvt_pk_bf16_f32 v140, v64, v65\n"
"v_cvt_pk_bf16_f32 v141, v66, v67\n"
"v_pk_mul_f32 v[132:133], v[112:113], v[88:89]\n"
"v_pk_fma_f32 v[76:77], v[76:77], v[128:129], v[132:133]\n"
"v_pk_mul_f32 v[134:135], v[114:115], v[88:89]\n"
"v_pk_fma_f32 v[78:79], v[78:79], v[130:131], v[134:135]\n"
"v_cvt_pk_bf16_f32 v142, v76, v77\n"
"v_cvt_pk_bf16_f32 v143, v78, v79\n"
"s_add_u32 s60, %[soffO], 9216\n"
"buffer_store_dwordx4 v[140:143], %[vSX], %[srdO], s60 offen sc1\n"
    :: [srdX]"s"(srdX), [srdW]"s"(srdW), [srdO]"s"(srdO), [m0X]"s"(m0X), [m0W]"s"(m0W), [soffO]"s"(soffO),
       [inv]"s"(inv), [eps]"s"(eps), [sgn]"s"(sgn),
       [vXd0]"v"(vXd0), [vXd1]"v"(vXd1), [vW0]"v"(vW0), [vW1]"v"(vW1), [vW2]"v"(vW2), [vW3]"v"(vW3), [vW4]"v"(vW4),
       [vA0]"v"(vA0), [vA1]"v"(vA1), [vB0]"v"(vB0), [vB1]"v"(vB1),
       [vX00]"v"(vX00), [vX01]"v"(vX01), [vX02]"v"(vX02), [vX03]"v"(vX03), [vX10]"v"(vX10), [vX11]"v"(vX11), [vX12]"v"(vX12), [vX13]"v"(vX13),
       [vOn]"v"(vOn), [vEw]"v"(vEw), [vEr]"v"(vEr), [vEg]"v"(vEg), [vS]"v"(vS), [vSX]"v"(vSX), [cdm]"{v31}"(cdm)
    : "memory", "s60", "s61", "s62", "s63",  "scc", "v32","v33","v34","v35","v36","v37","v38","v39","v40","v41","v42","v43","v44","v45","v46","v47","v48","v49","v50","v51","v52","v53","v54","v55","v56","v57","v58","v59","v60","v61","v62","v63","v64","v65","v66","v67","v68","v69","v70","v71","v72","v73","v74","v75","v76","v77","v78","v79","v80","v81","v82","v83","v84","v85","v86","v87","v88","v89","v90","v91","v92","v93","v94","v95","v96","v97","v98","v99","v100","v101","v102","v103","v104","v105","v106","v107","v108","v109","v110","v111","v112","v113","v114","v115","v116","v117","v118","v119","v120","v121","v122","v123","v124","v125","v126","v127","v128","v129","v130","v131","v132","v133","v134","v135","v136","v137","v138","v139","v140","v141","v142","v143","v144","v145","v146","v147","v148","v149","v150","v151", "a0","a1","a2","a3","a4","a5","a6","a7","a8","a9","a10","a11","a12","a13","a14","a15","a16","a17","a18","a19","a20","a21","a22","a23","a24","a25","a26","a27","a28","a29","a30","a31","a32","a33","a34","a35","a36","a37","a38","a39","a40","a41","a42","a43","a44","a45","a46","a47","a48","a49","a50","a51","a52","a53","a54","a55","a56","a57","a58","a59","a60","a61","a62","a63","a64","a65","a66","a67","a68","a69","a70","a71","a72","a73","a74","a75","a76","a77","a78","a79");
  asm volatile("kc1e:");
  asm volatile("s_waitcnt vmcnt(0)\n s_barrier" ::: "memory");
  unsigned wl = w, bl = b;
  asm volatile("" : "+s"(wl), "+s"(bl));
  {

  CODE_LABEL("kc2b");
  KARG_LOAD8(ka, 0x18); KARG_LOAD4(kb, 0x38); KARG_LOAD4(kc, 0x48);
  __builtin_amdgcn_sched_barrier(0);
  unsigned l = lane_id();
  const unsigned w = wl;
  const unsigned b = bl, xcd = b & 7, bi = b >> 3;
  const unsigned rt = 8 * (xcd & 1) + (bi & 7), kq = xcd >> 1, ct = bi >> 3;
  const unsigned rg = w & 1, ch = w >> 1;
  const unsigned lds = (unsigned)(size_t)smem;

  const unsigned soffA = (ct * 128u + kq * 32u) * 9216u + w * 4608u + rt * 18432u, soffAw = soffA - 16u * 18432u, lim = 16u - rt;
  const unsigned m0A = lds + w * 4608u, m0A2 = m0A + 4096u, m0B = lds + 18432u + w * 4096u;
  const unsigned vA = l * 16u, vAh = l * 16u + 4096u;
  const unsigned r8 = l >> 3;
  const unsigned rowD = 128u * rt + 32u * w + r8;
  const unsigned vB0 = rowD * 4096u + 16u * ((l & 7) ^ (r8 >> 1)), vB1 = (rowD + 8) * 4096u + 16u * ((l & 7) ^ (4 | (r8 >> 1))) - 1024u;
  const unsigned vB2 = vB0 + 16u * 4096u - 2048u, vB3 = vB1 + 16u * 4096u - 2048u;
  const unsigned soffB = (kq & 1) * 2048u + rt * 128u, soffBw = soffB - 2048u;
  asm volatile("s_waitcnt lgkmcnt(0)" : "+s"(ka), "+s"(kb), "+s"(kc) : "v"(vA), "v"(vAh), "v"(vB0), "v"(vB1), "v"(vB2), "v"(vB3), "s"(soffA), "s"(m0A), "s"(m0A2), "s"(m0B), "s"(soffB));
  u32x4 srdA = srd_of(ka[0], ka[1], 4096u * 576u * 2u);
  u32x4 srdB = srd_of(kq < 2 ? ka[2] : ka[4], kq < 2 ? ka[3] : ka[5], 2048u * 2048u * 2u);
  asm volatile(
"s_cmp_le_u32 %[lim], 0\n"
"s_cselect_b32 s61, %[soffAw], %[soffA]\n"
"s_add_u32 s61, s61, 0\n"
"s_add_u32 m0, %[m0A], 0\n"
"s_nop 0\n"
"buffer_load_dwordx4 %[vA], %[srdA], s61 offen offset:0 sc1 lds\n"
"buffer_load_dwordx4 %[vA], %[srdA], s61 offen offset:1024 sc1 lds\n"
"buffer_load_dwordx4 %[vA], %[srdA], s61 offen offset:2048 sc1 lds\n"
"buffer_load_dwordx4 %[vA], %[srdA], s61 offen offset:3072 sc1 lds\n"
"s_add_u32 m0, %[m0A2], 0\n"
"s_mov_b32 exec_hi, 0\n"
"buffer_load_dwordx4 %[vAh], %[srdA], s61 offen sc1 lds\n"
"s_mov_b32 exec_hi, -1\n"
"s_cmp_le_u32 %[lim], 0\n"
"s_cselect_b32 s60, %[soffBw], %[soffB]\n"
"s_add_u32 s60, s60, 0\n"
"s_add_u32 m0, %[m0B], 0\n"
"s_nop 0\n"
"buffer_load_dwordx4 %[vB0], %[srdB], s60 offen offset:0 lds\n"
"buffer_load_dwordx4 %[vB1], %[srdB], s60 offen offset:1024 lds\n"
"buffer_load_dwordx4 %[vB2], %[srdB], s60 offen offset:2048 lds\n"
"buffer_load_dwordx4 %[vB3], %[srdB], s60 offen offset:3072 lds\n"
"s_cmp_le_u32 %[lim], 1\n"
"s_cselect_b32 s60, %[soffBw], %[soffB]\n"
"s_add_u32 s60, s60, 128\n"
"s_add_u32 m0, %[m0B], 34816\n"
"s_nop 0\n"
"buffer_load_dwordx4 %[vB0], %[srdB], s60 offen offset:0 lds\n"
"buffer_load_dwordx4 %[vB1], %[srdB], s60 offen offset:1024 lds\n"
"buffer_load_dwordx4 %[vB2], %[srdB], s60 offen offset:2048 lds\n"
"buffer_load_dwordx4 %[vB3], %[srdB], s60 offen offset:3072 lds\n"
    : "+v"(l) : [lim]"s"(lim), [soffAw]"s"(soffAw), [soffA]"s"(soffA), [m0A]"s"(m0A), [vA]"v"(vA), [srdA]"s"(srdA), [m0A2]"s"(m0A2), [vAh]"v"(vAh), [soffBw]"s"(soffBw), [soffB]"s"(soffB), [m0B]"s"(m0B), [vB0]"v"(vB0), [srdB]"s"(srdB), [vB1]"v"(vB1), [vB2]"v"(vB2), [vB3]"v"(vB3)
    : "memory", "scc", "s60", "s61", "s62", "s63");
  const unsigned g = l >> 4, n = l & 15;
  u32x4 srdP = srd_of(ka[6], ka[7], 4u * 2048u * 576u * 2u);
  const unsigned vP = l * 16u, soffP = (((kq * 16 + rt) * 4 + ct) * 4 + w) * 9216u;
  const unsigned vTA0 = lds + ch * 4096u + l * 16u, vTA1 = vTA0 + 2u * 34816u;
  const unsigned vT80 = lds + 8192u + l * 16u, vT81 = vT80 + 2u * 34816u;
  const unsigned tb = lds + 18432u + (64u * rg + n) * 128u;
  const unsigned ra = ch * 4096u, rb2 = ch ? 0u - 4096u : 0u;
  const unsigned c0 = 16u * (g ^ (n >> 1)), c1 = c0 ^ 64u;
  const unsigned vTBa00 = tb + ra + c0, vTBa10 = tb + ra + c1, vTBb00 = tb + rb2 + c0, vTBb10 = tb + rb2 + c1;
  const unsigned vTBa01 = vTBa00 + 2u * 34816u, vTBa11 = vTBa10 + 2u * 34816u, vTBb01 = vTBb00 + 2u * 34816u, vTBb11 = vTBb10 + 2u * 34816u;

  asm volatile(
"s_waitcnt vmcnt(4)\n"
"s_barrier\n"
"ds_read_b128 a[0:3], %[vTBa00] offset:0\n"
"ds_read_b128 v[112:115], %[vTA0] offset:0\n"
"ds_read_b128 a[4:7], %[vTBa00] offset:2048\n"
"ds_read_b128 a[8:11], %[vTBb00] offset:4096\n"
"ds_read_b128 a[12:15], %[vTBb00] offset:6144\n"
"ds_read_b128 v[116:119], %[vTA0] offset:1024\n"
"s_waitcnt lgkmcnt(1)\n"
"v_mfma_f32_16x16x32_bf16 v[32:35], v[112:115], a[0:3], 0\n"
"ds_read_b128 v[120:123], %[vTA0] offset:2048\n"
"v_mfma_f32_16x16x32_bf16 v[36:39], v[112:115], a[4:7], 0\n"
"s_cmp_le_u32 %[lim], 2\n"
"s_cselect_b32 s60, %[soffBw], %[soffB]\n"
"s_add_u32 s60, s60, 256\n"
"s_add_u32 m0, %[m0B], 69632\n"
"s_nop 0\n"
"buffer_load_dwordx4 %[vB0], %[srdB], s60 offen offset:0 lds\n"
"v_mfma_f32_16x16x32_bf16 v[40:43], v[112:115], a[8:11], 0\n"
"ds_read_b128 a[16:19], %[vTBa10] offset:0\n"
"v_mfma_f32_16x16x32_bf16 v[44:47], v[112:115], a[12:15], 0\n"
"ds_read_b128 a[20:23], %[vTBa10] offset:2048\n"
"s_waitcnt lgkmcnt(3)\n"
"v_mfma_f32_16x16x32_bf16 v[48:51], v[116:119], a[0:3], 0\n"
"ds_read_b128 a[24:27], %[vTBb10] offset:4096\n"
"v_mfma_f32_16x16x32_bf16 v[52:55], v[116:119], a[4:7], 0\n"
"ds_read_b128 a[28:31], %[vTBb10] offset:6144\n"
"v_mfma_f32_16x16x32_bf16 v[56:59], v[116:119], a[8:11], 0\n"
"ds_read_b128 v[124:127], %[vTA0] offset:3072\n"
"v_mfma_f32_16x16x32_bf16 v[60:63], v[116:119], a[12:15], 0\n"
"buffer_load_dwordx4 %[vB1], %[srdB], s60 offen offset:1024 lds\n"
"s_waitcnt lgkmcnt(5)\n"
"v_mfma_f32_16x16x32_bf16 v[64:67], v[120:123], a[0:3], 0\n"
"ds_read_b128 v[128:131], %[vT80] offset:0\n"
"v_mfma_f32_16x16x32_bf16 v[68:71], v[120:123], a[4:7], 0\n"
"v_mfma_f32_16x16x32_bf16 v[72:75], v[120:123], a[8:11], 0\n"
"v_mfma_f32_16x16x32_bf16 v[76:79], v[120:123], a[12:15], 0\n"
"s_waitcnt lgkmcnt(1)\n"
"v_mfma_f32_16x16x32_bf16 v[80:83], v[124:127], a[0:3], 0\n"
"ds_read_b128 v[112:115], %[vTA0] offset:9216\n"
"v_mfma_f32_16x16x32_bf16 v[84:87], v[124:127], a[4:7], 0\n"
"buffer_load_dwordx4 %[vB2], %[srdB], s60 offen offset:2048 lds\n"
"v_mfma_f32_16x16x32_bf16 v[88:91], v[124:127], a[8:11], 0\n"
"v_mfma_f32_16x16x32_bf16 v[92:95], v[124:127], a[12:15], 0\n"
"s_waitcnt lgkmcnt(1)\n"
"v_mfma_f32_16x16x32_bf16 v[96:99], v[128:131], a[0:3], 0\n"
"ds_read_b128 v[116:119], %[vTA0] offset:10240\n"
"v_mfma_f32_16x16x32_bf16 v[100:103], v[128:131], a[4:7], 0\n"
"s_waitcnt lgkmcnt(1)\n"
"v_mfma_f32_16x16x32_bf16 v[32:35], v[112:115], a[16:19], v[32:35]\n"
"ds_read_b128 v[120:123], %[vTA0] offset:11264\n"
"v_mfma_f32_16x16x32_bf16 v[36:39], v[112:115], a[20:23], v[36:39]\n"
"buffer_load_dwordx4 %[vB3], %[srdB], s60 offen offset:3072 lds\n"
"v_mfma_f32_16x16x32_bf16 v[40:43], v[112:115], a[24:27], v[40:43]\n"
"v_mfma_f32_16x16x32_bf16 v[44:47], v[112:115], a[28:31], v[44:47]\n"
"s_waitcnt lgkmcnt(1)\n"
"v_mfma_f32_16x16x32_bf16 v[48:51], v[116:119], a[16:19], v[48:51]\n"
"ds_read_b128 v[124:127], %[vTA0] offset:12288\n"
"v_mfma_f32_16x16x32_bf16 v[52:55], v[116:119], a[20:23], v[52:55]\n"
"v_mfma_f32_16x16x32_bf16 v[56:59], v[116:119], a[24:27], v[56:59]\n"
"v_mfma_f32_16x16x32_bf16 v[60:63], v[116:119], a[28:31], v[60:63]\n"
"s_waitcnt lgkmcnt(1)\n"
"v_mfma_f32_16x16x32_bf16 v[64:67], v[120:123], a[16:19], v[64:67]\n"
"ds_read_b128 v[128:131], %[vT80] offset:9216\n"
"v_mfma_f32_16x16x32_bf16 v[68:71], v[120:123], a[20:23], v[68:71]\n"
"s_barrier\n"
"s_cmp_le_u32 %[lim], 1\n"
"s_cselect_b32 s61, %[soffAw], %[soffA]\n"
"s_add_u32 s61, s61, 18432\n"
"s_add_u32 m0, %[m0A], 34816\n"
"s_nop 0\n"
"buffer_load_dwordx4 %[vA], %[srdA], s61 offen offset:0 sc1 lds\n"
"buffer_load_dwordx4 %[vA], %[srdA], s61 offen offset:1024 sc1 lds\n"
"buffer_load_dwordx4 %[vA], %[srdA], s61 offen offset:2048 sc1 lds\n"
"buffer_load_dwordx4 %[vA], %[srdA], s61 offen offset:3072 sc1 lds\n"
"s_add_u32 m0, %[m0A2], 34816\n"
"s_mov_b32 exec_hi, 0\n"
"buffer_load_dwordx4 %[vAh], %[srdA], s61 offen sc1 lds\n"
"s_mov_b32 exec_hi, -1\n"
"s_cmp_le_u32 %[lim], 2\n"
"s_cselect_b32 s61, %[soffAw], %[soffA]\n"
"s_add_u32 s61, s61, 36864\n"
"s_add_u32 m0, %[m0A], 69632\n"
"s_nop 0\n"
"buffer_load_dwordx4 %[vA], %[srdA], s61 offen offset:0 sc1 lds\n"
"buffer_load_dwordx4 %[vA], %[srdA], s61 offen offset:1024 sc1 lds\n"
"buffer_load_dwordx4 %[vA], %[srdA], s61 offen offset:2048 sc1 lds\n"
"buffer_load_dwordx4 %[vA], %[srdA], s61 offen offset:3072 sc1 lds\n"
"s_add_u32 m0, %[m0A2], 69632\n"
"s_mov_b32 exec_hi, 0\n"
"buffer_load_dwordx4 %[vAh], %[srdA], s61 offen sc1 lds\n"
"s_mov_b32 exec_hi, -1\n"
"s_waitcnt vmcnt(5)\n"
"s_barrier\n"
"ds_read_b128 a[0:3], %[vTBa00] offset:34816\n"
"v_mfma_f32_16x16x32_bf16 v[72:75], v[120:123], a[24:27], v[72:75]\n"
"ds_read_b128 a[4:7], %[vTBa00] offset:36864\n"
"v_mfma_f32_16x16x32_bf16 v[76:79], v[120:123], a[28:31], v[76:79]\n"
"ds_read_b128 a[8:11], %[vTBb00] offset:38912\n"
"s_waitcnt lgkmcnt(4)\n"
"v_mfma_f32_16x16x32_bf16 v[80:83], v[124:127], a[16:19], v[80:83]\n"
"ds_read_b128 a[12:15], %[vTBb00] offset:40960\n"
"v_mfma_f32_16x16x32_bf16 v[84:87], v[124:127], a[20:23], v[84:87]\n"
"ds_read_b128 v[112:115], %[vTA0] offset:34816\n"
"v_mfma_f32_16x16x32_bf16 v[88:91], v[124:127], a[24:27], v[88:91]\n"
"s_cmp_le_u32 %[lim], 3\n"
"s_cselect_b32 s61, %[soffAw], %[soffA]\n"
"s_add_u32 s61, s61, 55296\n"
"s_add_u32 m0, %[m0A], 104448\n"
"s_nop 0\n"
"buffer_load_dwordx4 %[vA], %[srdA], s61 offen offset:0 sc1 lds\n"
"v_mfma_f32_16x16x32_bf16 v[92:95], v[124:127], a[28:31], v[92:95]\n"
"buffer_load_dwordx4 %[vA], %[srdA], s61 offen offset:1024 sc1 lds\n"
"s_waitcnt lgkmcnt(5)\n"
"v_mfma_f32_16x16x32_bf16 v[96:99], v[128:131], a[16:19], v[96:99]\n"
"ds_read_b128 v[116:119], %[vTA0] offset:35840\n"
"v_mfma_f32_16x16x32_bf16 v[100:103], v[128:131], a[20:23], v[100:103]\n"
"s_waitcnt lgkmcnt(1)\n"
"v_mfma_f32_16x16x32_bf16 v[32:35], v[112:115], a[0:3], v[32:35]\n"
"ds_read_b128 v[120:123], %[vTA0] offset:36864\n"
"v_mfma_f32_16x16x32_bf16 v[36:39], v[112:115], a[4:7], v[36:39]\n"
"buffer_load_dwordx4 %[vA], %[srdA], s61 offen offset:2048 sc1 lds\n"
"v_mfma_f32_16x16x32_bf16 v[40:43], v[112:115], a[8:11], v[40:43]\n"
"ds_read_b128 a[16:19], %[vTBa10] offset:34816\n"
"v_mfma_f32_16x16x32_bf16 v[44:47], v[112:115], a[12:15], v[44:47]\n"
"ds_read_b128 a[20:23], %[vTBa10] offset:36864\n"
"s_waitcnt lgkmcnt(3)\n"
"v_mfma_f32_16x16x32_bf16 v[48:51], v[116:119], a[0:3], v[48:51]\n"
"ds_read_b128 a[24:27], %[vTBb10] offset:38912\n"
"v_mfma_f32_16x16x32_bf16 v[52:55], v[116:119], a[4:7], v[52:55]\n"
"ds_read_b128 a[28:31], %[vTBb10] offset:40960\n"
"v_mfma_f32_16x16x32_bf16 v[56:59], v[116:119], a[8:11], v[56:59]\n"
"ds_read_b128 v[124:127], %[vTA0] offset:37888\n"
"v_mfma_f32_16x16x32_bf16 v[60:63], v[116:119], a[12:15], v[60:63]\n"
"buffer_load_dwordx4 %[vA], %[srdA], s61 offen offset:3072 sc1 lds\n"
"s_waitcnt lgkmcnt(5)\n"
"v_mfma_f32_16x16x32_bf16 v[64:67], v[120:123], a[0:3], v[64:67]\n"
"ds_read_b128 v[128:131], %[vT80] offset:34816\n"
"v_mfma_f32_16x16x32_bf16 v[68:71], v[120:123], a[4:7], v[68:71]\n"
"s_add_u32 m0, %[m0A2], 104448\n"
"s_mov_b32 exec_hi, 0\n"
"buffer_load_dwordx4 %[vAh], %[srdA], s61 offen sc1 lds\n"
"s_mov_b32 exec_hi, -1\n"
"v_mfma_f32_16x16x32_bf16 v[72:75], v[120:123], a[8:11], v[72:75]\n"
"v_mfma_f32_16x16x32_bf16 v[76:79], v[120:123], a[12:15], v[76:79]\n"
"s_cmp_le_u32 %[lim], 3\n"
"s_cselect_b32 s60, %[soffBw], %[soffB]\n"
"s_add_u32 s60, s60, 384\n"
"s_add_u32 m0, %[m0B], 104448\n"
"s_nop 0\n"
"buffer_load_dwordx4 %[vB0], %[srdB], s60 offen offset:0 lds\n"
"s_waitcnt lgkmcnt(1)\n"
"v_mfma_f32_16x16x32_bf16 v[80:83], v[124:127], a[0:3], v[80:83]\n"
"ds_read_b128 v[112:115], %[vTA0] offset:44032\n"
"v_mfma_f32_16x16x32_bf16 v[84:87], v[124:127], a[4:7], v[84:87]\n"
"v_mfma_f32_16x16x32_bf16 v[88:91], v[124:127], a[8:11], v[88:91]\n"
"v_mfma_f32_16x16x32_bf16 v[92:95], v[124:127], a[12:15], v[92:95]\n"
"buffer_load_dwordx4 %[vB1], %[srdB], s60 offen offset:1024 lds\n"
"s_waitcnt lgkmcnt(1)\n"
"v_mfma_f32_16x16x32_bf16 v[96:99], v[128:131], a[0:3], v[96:99]\n"
"ds_read_b128 v[116:119], %[vTA0] offset:45056\n"
"v_mfma_f32_16x16x32_bf16 v[100:103], v[128:131], a[4:7], v[100:103]\n"
"s_waitcnt lgkmcnt(1)\n"
"v_mfma_f32_16x16x32_bf16 v[32:35], v[112:115], a[16:19], v[32:35]\n"
"ds_read_b128 v[120:123], %[vTA0] offset:46080\n"
"v_mfma_f32_16x16x32_bf16 v[36:39], v[112:115], a[20:23], v[36:39]\n"
"buffer_load_dwordx4 %[vB2], %[srdB], s60 offen offset:2048 lds\n"
"v_mfma_f32_16x16x32_bf16 v[40:43], v[112:115], a[24:27], v[40:43]\n"
"v_mfma_f32_16x16x32_bf16 v[44:47], v[112:115], a[28:31], v[44:47]\n"
"s_waitcnt lgkmcnt(1)\n"
"v_mfma_f32_16x16x32_bf16 v[48:51], v[116:119], a[16:19], v[48:51]\n"
"ds_read_b128 v[124:127], %[vTA0] offset:47104\n"
"v_mfma_f32_16x16x32_bf16 v[52:55], v[116:119], a[20:23], v[52:55]\n"
"buffer_load_dwordx4 %[vB3], %[srdB], s60 offen offset:3072 lds\n"
"v_mfma_f32_16x16x32_bf16 v[56:59], v[116:119], a[24:27], v[56:59]\n"
"v_mfma_f32_16x16x32_bf16 v[60:63], v[116:119], a[28:31], v[60:63]\n"
"s_waitcnt lgkmcnt(1)\n"
"v_mfma_f32_16x16x32_bf16 v[64:67], v[120:123], a[16:19], v[64:67]\n"
"ds_read_b128 v[128:131], %[vT80] offset:44032\n"
"v_mfma_f32_16x16x32_bf16 v[68:71], v[120:123], a[20:23], v[68:71]\n"
"s_waitcnt vmcnt(9)\n"
"s_barrier\n"
"ds_read_b128 a[0:3], %[vTBa01] offset:0\n"
"v_mfma_f32_16x16x32_bf16 v[72:75], v[120:123], a[24:27], v[72:75]\n"
"ds_read_b128 a[4:7], %[vTBa01] offset:2048\n"
"v_mfma_f32_16x16x32_bf16 v[76:79], v[120:123], a[28:31], v[76:79]\n"
"ds_read_b128 a[8:11], %[vTBb01] offset:4096\n"
"s_waitcnt lgkmcnt(4)\n"
"v_mfma_f32_16x16x32_bf16 v[80:83], v[124:127], a[16:19], v[80:83]\n"
"ds_read_b128 a[12:15], %[vTBb01] offset:6144\n"
"v_mfma_f32_16x16x32_bf16 v[84:87], v[124:127], a[20:23], v[84:87]\n"
"ds_read_b128 v[112:115], %[vTA1] offset:0\n"
"v_mfma_f32_16x16x32_bf16 v[88:91], v[124:127], a[24:27], v[88:91]\n"
"s_cmp_le_u32 %[lim], 4\n"
"s_cselect_b32 s61, %[soffAw], %[soffA]\n"
"s_add_u32 s61, s61, 73728\n"
"s_add_u32 m0, %[m0A], 0\n"
"s_nop 0\n"
"buffer_load_dwordx4 %[vA], %[srdA], s61 offen offset:0 sc1 lds\n"
"v_mfma_f32_16x16x32_bf16 v[92:95], v[124:127], a[28:31], v[92:95]\n"
"buffer_load_dwordx4 %[vA], %[srdA], s61 offen offset:1024 sc1 lds\n"
"s_waitcnt lgkmcnt(5)\n"
"v_mfma_f32_16x16x32_bf16 v[96:99], v[128:131], a[16:19], v[96:99]\n"
"ds_read_b128 v[116:119], %[vTA1] offset:1024\n"
"v_mfma_f32_16x16x32_bf16 v[100:103], v[128:131], a[20:23], v[100:103]\n"
"s_waitcnt lgkmcnt(1)\n"
"v_mfma_f32_16x16x32_bf16 v[32:35], v[112:115], a[0:3], v[32:35]\n"
"ds_read_b128 v[120:123], %[vTA1] offset:2048\n"
"v_mfma_f32_16x16x32_bf16 v[36:39], v[112:115], a[4:7], v[36:39]\n"
"buffer_load_dwordx4 %[vA], %[srdA], s61 offen offset:2048 sc1 lds\n"
"v_mfma_f32_16x16x32_bf16 v[40:43], v[112:115], a[8:11], v[40:43]\n"
"ds_read_b128 a[16:19], %[vTBa11] offset:0\n"
"v_mfma_f32_16x16x32_bf16 v[44:47], v[112:115], a[12:15], v[44:47]\n"
"ds_read_b128 a[20:23], %[vTBa11] offset:2048\n"
"s_waitcnt lgkmcnt(3)\n"
"v_mfma_f32_16x16x32_bf16 v[48:51], v[116:119], a[0:3], v[48:51]\n"
"ds_read_b128 a[24:27], %[vTBb11] offset:4096\n"
"v_mfma_f32_16x16x32_bf16 v[52:55], v[116:119], a[4:7], v[52:55]\n"
"ds_read_b128 a[28:31], %[vTBb11] offset:6144\n"
"v_mfma_f32_16x16x32_bf16 v[56:59], v[116:119], a[8:11], v[56:59]\n"
"ds_read_b128 v[124:127], %[vTA1] offset:3072\n"
"v_mfma_f32_16x16x32_bf16 v[60:63], v[116:119], a[12:15], v[60:63]\n"
"buffer_load_dwordx4 %[vA], %[srdA], s61 offen offset:3072 sc1 lds\n"
"s_waitcnt lgkmcnt(5)\n"
"v_mfma_f32_16x16x32_bf16 v[64:67], v[120:123], a[0:3], v[64:67]\n"
"ds_read_b128 v[128:131], %[vT81] offset:0\n"
"v_mfma_f32_16x16x32_bf16 v[68:71], v[120:123], a[4:7], v[68:71]\n"
"s_add_u32 m0, %[m0A2], 0\n"
"s_mov_b32 exec_hi, 0\n"
"buffer_load_dwordx4 %[vAh], %[srdA], s61 offen sc1 lds\n"
"s_mov_b32 exec_hi, -1\n"
"v_mfma_f32_16x16x32_bf16 v[72:75], v[120:123], a[8:11], v[72:75]\n"
"v_mfma_f32_16x16x32_bf16 v[76:79], v[120:123], a[12:15], v[76:79]\n"
"s_cmp_le_u32 %[lim], 4\n"
"s_cselect_b32 s60, %[soffBw], %[soffB]\n"
"s_add_u32 s60, s60, 512\n"
"s_add_u32 m0, %[m0B], 0\n"
"s_nop 0\n"
"buffer_load_dwordx4 %[vB0], %[srdB], s60 offen offset:0 lds\n"
"s_waitcnt lgkmcnt(1)\n"
"v_mfma_f32_16x16x32_bf16 v[80:83], v[124:127], a[0:3], v[80:83]\n"
"ds_read_b128 v[112:115], %[vTA1] offset:9216\n"
"v_mfma_f32_16x16x32_bf16 v[84:87], v[124:127], a[4:7], v[84:87]\n"
"v_mfma_f32_16x16x32_bf16 v[88:91], v[124:127], a[8:11], v[88:91]\n"
"v_mfma_f32_16x16x32_bf16 v[92:95], v[124:127], a[12:15], v[92:95]\n"
"buffer_load_dwordx4 %[vB1], %[srdB], s60 offen offset:1024 lds\n"
"s_waitcnt lgkmcnt(1)\n"
"v_mfma_f32_16x16x32_bf16 v[96:99], v[128:131], a[0:3], v[96:99]\n"
"ds_read_b128 v[116:119], %[vTA1] offset:10240\n"
"v_mfma_f32_16x16x32_bf16 v[100:103], v[128:131], a[4:7], v[100:103]\n"
"s_waitcnt lgkmcnt(1)\n"
"v_mfma_f32_16x16x32_bf16 v[32:35], v[112:115], a[16:19], v[32:35]\n"
"ds_read_b128 v[120:123], %[vTA1] offset:11264\n"
"v_mfma_f32_16x16x32_bf16 v[36:39], v[112:115], a[20:23], v[36:39]\n"
"buffer_load_dwordx4 %[vB2], %[srdB], s60 offen offset:2048 lds\n"
"v_mfma_f32_16x16x32_bf16 v[40:43], v[112:115], a[24:27], v[40:43]\n"
"v_mfma_f32_16x16x32_bf16 v[44:47], v[112:115], a[28:31], v[44:47]\n"
"s_waitcnt lgkmcnt(1)\n"
"v_mfma_f32_16x16x32_bf16 v[48:51], v[116:119], a[16:19], v[48:51]\n"
"ds_read_b128 v[124:127], %[vTA1] offset:12288\n"
"v_mfma_f32_16x16x32_bf16 v[52:55], v[116:119], a[20:23], v[52:55]\n"
"buffer_load_dwordx4 %[vB3], %[srdB], s60 offen offset:3072 lds\n"
"v_mfma_f32_16x16x32_bf16 v[56:59], v[116:119], a[24:27], v[56:59]\n"
"v_mfma_f32_16x16x32_bf16 v[60:63], v[116:119], a[28:31], v[60:63]\n"
"s_waitcnt lgkmcnt(1)\n"
"v_mfma_f32_16x16x32_bf16 v[64:67], v[120:123], a[16:19], v[64:67]\n"
"ds_read_b128 v[128:131], %[vT81] offset:9216\n"
"v_mfma_f32_16x16x32_bf16 v[68:71], v[120:123], a[20:23], v[68:71]\n"
"s_waitcnt vmcnt(9)\n"
"s_barrier\n"
"ds_read_b128 a[0:3], %[vTBa01] offset:34816\n"
"v_mfma_f32_16x16x32_bf16 v[72:75], v[120:123], a[24:27], v[72:75]\n"
"ds_read_b128 a[4:7], %[vTBa01] offset:36864\n"
"v_mfma_f32_16x16x32_bf16 v[76:79], v[120:123], a[28:31], v[76:79]\n"
"ds_read_b128 a[8:11], %[vTBb01] offset:38912\n"
"s_waitcnt lgkmcnt(4)\n"
"v_mfma_f32_16x16x32_bf16 v[80:83], v[124:127], a[16:19], v[80:83]\n"
"ds_read_b128 a[12:15], %[vTBb01] offset:40960\n"
"v_mfma_f32_16x16x32_bf16 v[84:87], v[124:127], a[20:23], v[84:87]\n"
"ds_read_b128 v[112:115], %[vTA1] offset:34816\n"
"v_mfma_f32_16x16x32_bf16 v[88:91], v[124:127], a[24:27], v[88:91]\n"
"s_cmp_le_u32 %[lim], 5\n"
"s_cselect_b32 s61, %[soffAw], %[soffA]\n"
"s_add_u32 s61, s61, 92160\n"
"s_add_u32 m0, %[m0A], 34816\n"
"s_nop 0\n"
"buffer_load_dwordx4 %[vA], %[srdA], s61 offen offset:0 sc1 lds\n"
"v_mfma_f32_16x16x32_bf16 v[92:95], v[124:127], a[28:31], v[92:95]\n"
"buffer_load_dwordx4 %[vA], %[srdA], s61 offen offset:1024 sc1 lds\n"
"s_waitcnt lgkmcnt(5)\n"
"v_mfma_f32_16x16x32_bf16 v[96:99], v[128:131], a[16:19], v[96:99]\n"
"ds_read_b128 v[116:119], %[vTA1] offset:35840\n"
"v_mfma_f32_16x16x32_bf16 v[100:103], v[128:131], a[20:23], v[100:103]\n"
"s_waitcnt lgkmcnt(1)\n"
"v_mfma_f32_16x16x32_bf16 v[32:35], v[112:115], a[0:3], v[32:35]\n"
"ds_read_b128 v[120:123], %[vTA1] offset:36864\n"
"v_mfma_f32_16x16x32_bf16 v[36:39], v[112:115], a[4:7], v[36:39]\n"
"buffer_load_dwordx4 %[vA], %[srdA], s61 offen offset:2048 sc1 lds\n"
"v_mfma_f32_16x16x32_bf16 v[40:43], v[112:115], a[8:11], v[40:43]\n"
"ds_read_b128 a[16:19], %[vTBa11] offset:34816\n"
"v_mfma_f32_16x16x32_bf16 v[44:47], v[112:115], a[12:15], v[44:47]\n"
"ds_read_b128 a[20:23], %[vTBa11] offset:36864\n"
"s_waitcnt lgkmcnt(3)\n"
"v_mfma_f32_16x16x32_bf16 v[48:51], v[116:119], a[0:3], v[48:51]\n"
"ds_read_b128 a[24:27], %[vTBb11] offset:38912\n"
"v_mfma_f32_16x16x32_bf16 v[52:55], v[116:119], a[4:7], v[52:55]\n"
"ds_read_b128 a[28:31], %[vTBb11] offset:40960\n"
"v_mfma_f32_16x16x32_bf16 v[56:59], v[116:119], a[8:11], v[56:59]\n"
"ds_read_b128 v[124:127], %[vTA1] offset:37888\n"
"v_mfma_f32_16x16x32_bf16 v[60:63], v[116:119], a[12:15], v[60:63]\n"
"buffer_load_dwordx4 %[vA], %[srdA], s61 offen offset:3072 sc1 lds\n"
"s_waitcnt lgkmcnt(5)\n"
"v_mfma_f32_16x16x32_bf16 v[64:67], v[120:123], a[0:3], v[64:67]\n"
"ds_read_b128 v[128:131], %[vT81] offset:34816\n"
"v_mfma_f32_16x16x32_bf16 v[68:71], v[120:123], a[4:7], v[68:71]\n"
"s_add_u32 m0, %[m0A2], 34816\n"
"s_mov_b32 exec_hi, 0\n"
"buffer_load_dwordx4 %[vAh], %[srdA], s61 offen sc1 lds\n"
"s_mov_b32 exec_hi, -1\n"
"v_mfma_f32_16x16x32_bf16 v[72:75], v[120:123], a[8:11], v[72:75]\n"
"v_mfma_f32_16x16x32_bf16 v[76:79], v[120:123], a[12:15], v[76:79]\n"
"s_cmp_le_u32 %[lim], 5\n"
"s_cselect_b32 s60, %[soffBw], %[soffB]\n"
"s_add_u32 s60, s60, 640\n"
"s_add_u32 m0, %[m0B], 34816\n"
"s_nop 0\n"
"buffer_load_dwordx4 %[vB0], %[srdB], s60 offen offset:0 lds\n"
"s_waitcnt lgkmcnt(1)\n"
"v_mfma_f32_16x16x32_bf16 v[80:83], v[124:127], a[0:3], v[80:83]\n"
"ds_read_b128 v[112:115], %[vTA1] offset:44032\n"
"v_mfma_f32_16x16x32_bf16 v[84:87], v[124:127], a[4:7], v[84:87]\n"
"v_mfma_f32_16x16x32_bf16 v[88:91], v[124:127], a[8:11], v[88:91]\n"
"v_mfma_f32_16x16x32_bf16 v[92:95], v[124:127], a[12:15], v[92:95]\n"
"buffer_load_dwordx4 %[vB1], %[srdB], s60 offen offset:1024 lds\n"
"s_waitcnt lgkmcnt(1)\n"
"v_mfma_f32_16x16x32_bf16 v[96:99], v[128:131], a[0:3], v[96:99]\n"
"ds_read_b128 v[116:119], %[vTA1] offset:45056\n"
"v_mfma_f32_16x16x32_bf16 v[100:103], v[128:131], a[4:7], v[100:103]\n"
"s_waitcnt lgkmcnt(1)\n"
"v_mfma_f32_16x16x32_bf16 v[32:35], v[112:115], a[16:19], v[32:35]\n"
"ds_read_b128 v[120:123], %[vTA1] offset:46080\n"
"v_mfma_f32_16x16x32_bf16 v[36:39], v[112:115], a[20:23], v[36:39]\n"
"buffer_load_dwordx4 %[vB2], %[srdB], s60 offen offset:2048 lds\n"
"v_mfma_f32_16x16x32_bf16 v[40:43], v[112:115], a[24:27], v[40:43]\n"
"v_mfma_f32_16x16x32_bf16 v[44:47], v[112:115], a[28:31], v[44:47]\n"
"s_waitcnt lgkmcnt(1)\n"
"v_mfma_f32_16x16x32_bf16 v[48:51], v[116:119], a[16:19], v[48:51]\n"
"ds_read_b128 v[124:127], %[vTA1] offset:47104\n"
"v_mfma_f32_16x16x32_bf16 v[52:55], v[116:119], a[20:23], v[52:55]\n"
"buffer_load_dwordx4 %[vB3], %[srdB], s60 offen offset:3072 lds\n"
"v_mfma_f32_16x16x32_bf16 v[56:59], v[116:119], a[24:27], v[56:59]\n"
"v_mfma_f32_16x16x32_bf16 v[60:63], v[116:119], a[28:31], v[60:63]\n"
"s_waitcnt lgkmcnt(1)\n"
"v_mfma_f32_16x16x32_bf16 v[64:67], v[120:123], a[16:19], v[64:67]\n"
"ds_read_b128 v[128:131], %[vT81] offset:44032\n"
"v_mfma_f32_16x16x32_bf16 v[68:71], v[120:123], a[20:23], v[68:71]\n"
"s_waitcnt vmcnt(9)\n"
"s_barrier\n"
"ds_read_b128 a[0:3], %[vTBa00] offset:0\n"
"v_mfma_f32_16x16x32_bf16 v[72:75], v[120:123], a[24:27], v[72:75]\n"
"ds_read_b128 a[4:7], %[vTBa00] offset:2048\n"
"v_mfma_f32_16x16x32_bf16 v[76:79], v[120:123], a[28:31], v[76:79]\n"
"ds_read_b128 a[8:11], %[vTBb00] offset:4096\n"
"s_waitcnt lgkmcnt(4)\n"
"v_mfma_f32_16x16x32_bf16 v[80:83], v[124:127], a[16:19], v[80:83]\n"
"ds_read_b128 a[12:15], %[vTBb00] offset:6144\n"
"v_mfma_f32_16x16x32_bf16 v[84:87], v[124:127], a[20:23], v[84:87]\n"
"ds_read_b128 v[112:115], %[vTA0] offset:0\n"
"v_mfma_f32_16x16x32_bf16 v[88:91], v[124:127], a[24:27], v[88:91]\n"
"s_cmp_le_u32 %[lim], 6\n"
"s_cselect_b32 s61, %[soffAw], %[soffA]\n"
"s_add_u32 s61, s61, 110592\n"
"s_add_u32 m0, %[m0A], 69632\n"
"s_nop 0\n"
"buffer_load_dwordx4 %[vA], %[srdA], s61 offen offset:0 sc1 lds\n"
"v_mfma_f32_16x16x32_bf16 v[92:95], v[124:127], a[28:31], v[92:95]\n"
"buffer_load_dwordx4 %[vA], %[srdA], s61 offen offset:1024 sc1 lds\n"
"s_waitcnt lgkmcnt(5)\n"
"v_mfma_f32_16x16x32_bf16 v[96:99], v[128:131], a[16:19], v[96:99]\n"
"ds_read_b128 v[116:119], %[vTA0] offset:1024\n"
"v_mfma_f32_16x16x32_bf16 v[100:103], v[128:131], a[20:23], v[100:103]\n"
"s_waitcnt lgkmcnt(1)\n"
"v_mfma_f32_16x16x32_bf16 v[32:35], v[112:115], a[0:3], v[32:35]\n"
"ds_read_b128 v[120:123], %[vTA0] offset:2048\n"
"v_mfma_f32_16x16x32_bf16 v[36:39], v[112:115], a[4:7], v[36:39]\n"
"buffer_load_dwordx4 %[vA], %[srdA], s61 offen offset:2048 sc1 lds\n"
"v_mfma_f32_16x16x32_bf16 v[40:43], v[112:115], a[8:11], v[40:43]\n"
"ds_read_b128 a[16:19], %[vTBa10] offset:0\n"
"v_mfma_f32_16x16x32_bf16 v[44:47], v[112:115], a[12:15], v[44:47]\n"
"ds_read_b128 a[20:23], %[vTBa10] offset:2048\n"
"s_waitcnt lgkmcnt(3)\n"
"v_mfma_f32_16x16x32_bf16 v[48:51], v[116:119], a[0:3], v[48:51]\n"
"ds_read_b128 a[24:27], %[vTBb10] offset:4096\n"
"v_mfma_f32_16x16x32_bf16 v[52:55], v[116:119], a[4:7], v[52:55]\n"
"ds_read_b128 a[28:31], %[vTBb10] offset:6144\n"
"v_mfma_f32_16x16x32_bf16 v[56:59], v[116:119], a[8:11], v[56:59]\n"
"ds_read_b128 v[124:127], %[vTA0] offset:3072\n"
"v_mfma_f32_16x16x32_bf16 v[60:63], v[116:119], a[12:15], v[60:63]\n"
"buffer_load_dwordx4 %[vA], %[srdA], s61 offen offset:3072 sc1 lds\n"
"s_waitcnt lgkmcnt(5)\n"
"v_mfma_f32_16x16x32_bf16 v[64:67], v[120:123], a[0:3], v[64:67]\n"
"ds_read_b128 v[128:131], %[vT80] offset:0\n"
"v_mfma_f32_16x16x32_bf16 v[68:71], v[120:123], a[4:7], v[68:71]\n"
"s_add_u32 m0, %[m0A2], 69632\n"
"s_mov_b32 exec_hi, 0\n"
"buffer_load_dwordx4 %[vAh], %[srdA], s61 offen sc1 lds\n"
"s_mov_b32 exec_hi, -1\n"
"v_mfma_f32_16x16x32_bf16 v[72:75], v[120:123], a[8:11], v[72:75]\n"
"v_mfma_f32_16x16x32_bf16 v[76:79], v[120:123], a[12:15], v[76:79]\n"
"s_cmp_le_u32 %[lim], 6\n"
"s_cselect_b32 s60, %[soffBw], %[soffB]\n"
"s_add_u32 s60, s60, 768\n"
"s_add_u32 m0, %[m0B], 69632\n"
"s_nop 0\n"
"buffer_load_dwordx4 %[vB0], %[srdB], s60 offen offset:0 lds\n"
"s_waitcnt lgkmcnt(1)\n"
"v_mfma_f32_16x16x32_bf16 v[80:83], v[124:127], a[0:3], v[80:83]\n"
"ds_read_b128 v[112:115], %[vTA0] offset:9216\n"
"v_mfma_f32_16x16x32_bf16 v[84:87], v[124:127], a[4:7], v[84:87]\n"
"v_mfma_f32_16x16x32_bf16 v[88:91], v[124:127], a[8:11], v[88:91]\n"
"v_mfma_f32_16x16x32_bf16 v[92:95], v[124:127], a[12:15], v[92:95]\n"
"buffer_load_dwordx4 %[vB1], %[srdB], s60 offen offset:1024 lds\n"
"s_waitcnt lgkmcnt(1)\n"
"v_mfma_f32_16x16x32_bf16 v[96:99], v[128:131], a[0:3], v[96:99]\n"
"ds_read_b128 v[116:119], %[vTA0] offset:10240\n"
"v_mfma_f32_16x16x32_bf16 v[100:103], v[128:131], a[4:7], v[100:103]\n"
"s_waitcnt lgkmcnt(1)\n"
"v_mfma_f32_16x16x32_bf16 v[32:35], v[112:115], a[16:19], v[32:35]\n"
"ds_read_b128 v[120:123], %[vTA0] offset:11264\n"
"v_mfma_f32_16x16x32_bf16 v[36:39], v[112:115], a[20:23], v[36:39]\n"
"buffer_load_dwordx4 %[vB2], %[srdB], s60 offen offset:2048 lds\n"
"v_mfma_f32_16x16x32_bf16 v[40:43], v[112:115], a[24:27], v[40:43]\n"
"v_mfma_f32_16x16x32_bf16 v[44:47], v[112:115], a[28:31], v[44:47]\n"
"s_waitcnt lgkmcnt(1)\n"
"v_mfma_f32_16x16x32_bf16 v[48:51], v[116:119], a[16:19], v[48:51]\n"
"ds_read_b128 v[124:127], %[vTA0] offset:12288\n"
"v_mfma_f32_16x16x32_bf16 v[52:55], v[116:119], a[20:23], v[52:55]\n"
"buffer_load_dwordx4 %[vB3], %[srdB], s60 offen offset:3072 lds\n"
"v_mfma_f32_16x16x32_bf16 v[56:59], v[116:119], a[24:27], v[56:59]\n"
"v_mfma_f32_16x16x32_bf16 v[60:63], v[116:119], a[28:31], v[60:63]\n"
"s_waitcnt lgkmcnt(1)\n"
"v_mfma_f32_16x16x32_bf16 v[64:67], v[120:123], a[16:19], v[64:67]\n"
"ds_read_b128 v[128:131], %[vT80] offset:9216\n"
"v_mfma_f32_16x16x32_bf16 v[68:71], v[120:123], a[20:23], v[68:71]\n"
"s_waitcnt vmcnt(9)\n"
"s_barrier\n"
"ds_read_b128 a[0:3], %[vTBa00] offset:34816\n"
"v_mfma_f32_16x16x32_bf16 v[72:75], v[120:123], a[24:27], v[72:75]\n"
"ds_read_b128 a[4:7], %[vTBa00] offset:36864\n"
"v_mfma_f32_16x16x32_bf16 v[76:79], v[120:123], a[28:31], v[76:79]\n"
"ds_read_b128 a[8:11], %[vTBb00] offset:38912\n"
"s_waitcnt lgkmcnt(4)\n"
"v_mfma_f32_16x16x32_bf16 v[80:83], v[124:127], a[16:19], v[80:83]\n"
"ds_read_b128 a[12:15], %[vTBb00] offset:40960\n"
"v_mfma_f32_16x16x32_bf16 v[84:87], v[124:127], a[20:23], v[84:87]\n"
"ds_read_b128 v[112:115], %[vTA0] offset:34816\n"
"v_mfma_f32_16x16x32_bf16 v[88:91], v[124:127], a[24:27], v[88:91]\n"
"s_cmp_le_u32 %[lim], 7\n"
"s_cselect_b32 s61, %[soffAw], %[soffA]\n"
"s_add_u32 s61, s61, 129024\n"
"s_add_u32 m0, %[m0A], 104448\n"
"s_nop 0\n"
"buffer_load_dwordx4 %[vA], %[srdA], s61 offen offset:0 sc1 lds\n"
"v_mfma_f32_16x16x32_bf16 v[92:95], v[124:127], a[28:31], v[92:95]\n"
"buffer_load_dwordx4 %[vA], %[srdA], s61 offen offset:1024 sc1 lds\n"
"s_waitcnt lgkmcnt(5)\n"
"v_mfma_f32_16x16x32_bf16 v[96:99], v[128:131], a[16:19], v[96:99]\n"
"ds_read_b128 v[116:119], %[vTA0] offset:35840\n"
"v_mfma_f32_16x16x32_bf16 v[100:103], v[128:131], a[20:23], v[100:103]\n"
"s_waitcnt lgkmcnt(1)\n"
"v_mfma_f32_16x16x32_bf16 v[32:35], v[112:115], a[0:3], v[32:35]\n"
"ds_read_b128 v[120:123], %[vTA0] offset:36864\n"
"v_mfma_f32_16x16x32_bf16 v[36:39], v[112:115], a[4:7], v[36:39]\n"
"buffer_load_dwordx4 %[vA], %[srdA], s61 offen offset:2048 sc1 lds\n"
"v_mfma_f32_16x16x32_bf16 v[40:43], v[112:115], a[8:11], v[40:43]\n"
"ds_read_b128 a[16:19], %[vTBa10] offset:34816\n"
"v_mfma_f32_16x16x32_bf16 v[44:47], v[112:115], a[12:15], v[44:47]\n"
"ds_read_b128 a[20:23], %[vTBa10] offset:36864\n"
"s_waitcnt lgkmcnt(3)\n"
"v_mfma_f32_16x16x32_bf16 v[48:51], v[116:119], a[0:3], v[48:51]\n"
"ds_read_b128 a[24:27], %[vTBb10] offset:38912\n"
"v_mfma_f32_16x16x32_bf16 v[52:55], v[116:119], a[4:7], v[52:55]\n"
"ds_read_b128 a[28:31], %[vTBb10] offset:40960\n"
"v_mfma_f32_16x16x32_bf16 v[56:59], v[116:119], a[8:11], v[56:59]\n"
"ds_read_b128 v[124:127], %[vTA0] offset:37888\n"
"v_mfma_f32_16x16x32_bf16 v[60:63], v[116:119], a[12:15], v[60:63]\n"
"buffer_load_dwordx4 %[vA], %[srdA], s61 offen offset:3072 sc1 lds\n"
"s_waitcnt lgkmcnt(5)\n"
"v_mfma_f32_16x16x32_bf16 v[64:67], v[120:123], a[0:3], v[64:67]\n"
"ds_read_b128 v[128:131], %[vT80] offset:34816\n"
"v_mfma_f32_16x16x32_bf16 v[68:71], v[120:123], a[4:7], v[68:71]\n"
"s_add_u32 m0, %[m0A2], 104448\n"
"s_mov_b32 exec_hi, 0\n"
"buffer_load_dwordx4 %[vAh], %[srdA], s61 offen sc1 lds\n"
"s_mov_b32 exec_hi, -1\n"
"v_mfma_f32_16x16x32_bf16 v[72:75], v[120:123], a[8:11], v[72:75]\n"
"v_mfma_f32_16x16x32_bf16 v[76:79], v[120:123], a[12:15], v[76:79]\n"
"s_cmp_le_u32 %[lim], 7\n"
"s_cselect_b32 s60, %[soffBw], %[soffB]\n"
"s_add_u32 s60, s60, 896\n"
"s_add_u32 m0, %[m0B], 104448\n"
"s_nop 0\n"
"buffer_load_dwordx4 %[vB0], %[srdB], s60 offen offset:0 lds\n"
"s_waitcnt lgkmcnt(1)\n"
"v_mfma_f32_16x16x32_bf16 v[80:83], v[124:127], a[0:3], v[80:83]\n"
"ds_read_b128 v[112:115], %[vTA0] offset:44032\n"
"v_mfma_f32_16x16x32_bf16 v[84:87], v[124:127], a[4:7], v[84:87]\n"
"v_mfma_f32_16x16x32_bf16 v[88:91], v[124:127], a[8:11], v[88:91]\n"
"v_mfma_f32_16x16x32_bf16 v[92:95], v[124:127], a[12:15], v[92:95]\n"
"buffer_load_dwordx4 %[vB1], %[srdB], s60 offen offset:1024 lds\n"
"s_waitcnt lgkmcnt(1)\n"
"v_mfma_f32_16x16x32_bf16 v[96:99], v[128:131], a[0:3], v[96:99]\n"
"ds_read_b128 v[116:119], %[vTA0] offset:45056\n"
"v_mfma_f32_16x16x32_bf16 v[100:103], v[128:131], a[4:7], v[100:103]\n"
"s_waitcnt lgkmcnt(1)\n"
"v_mfma_f32_16x16x32_bf16 v[32:35], v[112:115], a[16:19], v[32:35]\n"
"ds_read_b128 v[120:123], %[vTA0] offset:46080\n"
"v_mfma_f32_16x16x32_bf16 v[36:39], v[112:115], a[20:23], v[36:39]\n"
"buffer_load_dwordx4 %[vB2], %[srdB], s60 offen offset:2048 lds\n"
"v_mfma_f32_16x16x32_bf16 v[40:43], v[112:115], a[24:27], v[40:43]\n"
"v_mfma_f32_16x16x32_bf16 v[44:47], v[112:115], a[28:31], v[44:47]\n"
"s_waitcnt lgkmcnt(1)\n"
"v_mfma_f32_16x16x32_bf16 v[48:51], v[116:119], a[16:19], v[48:51]\n"
"ds_read_b128 v[124:127], %[vTA0] offset:47104\n"
"v_mfma_f32_16x16x32_bf16 v[52:55], v[116:119], a[20:23], v[52:55]\n"
"buffer_load_dwordx4 %[vB3], %[srdB], s60 offen offset:3072 lds\n"
"v_mfma_f32_16x16x32_bf16 v[56:59], v[116:119], a[24:27], v[56:59]\n"
"v_mfma_f32_16x16x32_bf16 v[60:63], v[116:119], a[28:31], v[60:63]\n"
"s_waitcnt lgkmcnt(1)\n"
"v_mfma_f32_16x16x32_bf16 v[64:67], v[120:123], a[16:19], v[64:67]\n"
"ds_read_b128 v[128:131], %[vT80] offset:44032\n"
"v_mfma_f32_16x16x32_bf16 v[68:71], v[120:123], a[20:23], v[68:71]\n"
"s_waitcnt vmcnt(9)\n"
"s_barrier\n"
"ds_read_b128 a[0:3], %[vTBa01] offset:0\n"
"v_mfma_f32_16x16x32_bf16 v[72:75], v[120:123], a[24:27], v[72:75]\n"
"ds_read_b128 a[4:7], %[vTBa01] offset:2048\n"
"v_mfma_f32_16x16x32_bf16 v[76:79], v[120:123], a[28:31], v[76:79]\n"
"ds_read_b128 a[8:11], %[vTBb01] offset:4096\n"
"s_waitcnt lgkmcnt(4)\n"
"v_mfma_f32_16x16x32_bf16 v[80:83], v[124:127], a[16:19], v[80:83]\n"
"ds_read_b128 a[12:15], %[vTBb01] offset:6144\n"
"v_mfma_f32_16x16x32_bf16 v[84:87], v[124:127], a[20:23], v[84:87]\n"
"ds_read_b128 v[112:115], %[vTA1] offset:0\n"
"v_mfma_f32_16x16x32_bf16 v[88:91], v[124:127], a[24:27], v[88:91]\n"
"s_cmp_le_u32 %[lim], 8\n"
"s_cselect_b32 s61, %[soffAw], %[soffA]\n"
"s_add_u32 s61, s61, 147456\n"
"s_add_u32 m0, %[m0A], 0\n"
"s_nop 0\n"
"buffer_load_dwordx4 %[vA], %[srdA], s61 offen offset:0 sc1 lds\n"
"v_mfma_f32_16x16x32_bf16 v[92:95], v[124:127], a[28:31], v[92:95]\n"
"buffer_load_dwordx4 %[vA], %[srdA], s61 offen offset:1024 sc1 lds\n"
"s_waitcnt lgkmcnt(5)\n"
"v_mfma_f32_16x16x32_bf16 v[96:99], v[128:131], a[16:19], v[96:99]\n"
"ds_read_b128 v[116:119], %[vTA1] offset:1024\n"
"v_mfma_f32_16x16x32_bf16 v[100:103], v[128:131], a[20:23], v[100:103]\n"
"s_waitcnt lgkmcnt(1)\n"
"v_mfma_f32_16x16x32_bf16 v[32:35], v[112:115], a[0:3], v[32:35]\n"
"ds_read_b128 v[120:123], %[vTA1] offset:2048\n"
"v_mfma_f32_16x16x32_bf16 v[36:39], v[112:115], a[4:7], v[36:39]\n"
"buffer_load_dwordx4 %[vA], %[srdA], s61 offen offset:2048 sc1 lds\n"
"v_mfma_f32_16x16x32_bf16 v[40:43], v[112:115], a[8:11], v[40:43]\n"
"ds_read_b128 a[16:19], %[vTBa11] offset:0\n"
"v_mfma_f32_16x16x32_bf16 v[44:47], v[112:115], a[12:15], v[44:47]\n"
"ds_read_b128 a[20:23], %[vTBa11] offset:2048\n"
"s_waitcnt lgkmcnt(3)\n"
"v_mfma_f32_16x16x32_bf16 v[48:51], v[116:119], a[0:3], v[48:51]\n"
"ds_read_b128 a[24:27], %[vTBb11] offset:4096\n"
"v_mfma_f32_16x16x32_bf16 v[52:55], v[116:119], a[4:7], v[52:55]\n"
"ds_read_b128 a[28:31], %[vTBb11] offset:6144\n"
"v_mfma_f32_16x16x32_bf16 v[56:59], v[116:119], a[8:11], v[56:59]\n"
"ds_read_b128 v[124:127], %[vTA1] offset:3072\n"
"v_mfma_f32_16x16x32_bf16 v[60:63], v[116:119], a[12:15], v[60:63]\n"
"buffer_load_dwordx4 %[vA], %[srdA], s61 offen offset:3072 sc1 lds\n"
"s_waitcnt lgkmcnt(5)\n"
"v_mfma_f32_16x16x32_bf16 v[64:67], v[120:123], a[0:3], v[64:67]\n"
"ds_read_b128 v[128:131], %[vT81] offset:0\n"
"v_mfma_f32_16x16x32_bf16 v[68:71], v[120:123], a[4:7], v[68:71]\n"
"s_add_u32 m0, %[m0A2], 0\n"
"s_mov_b32 exec_hi, 0\n"
"buffer_load_dwordx4 %[vAh], %[srdA], s61 offen sc1 lds\n"
"s_mov_b32 exec_hi, -1\n"
"v_mfma_f32_16x16x32_bf16 v[72:75], v[120:123], a[8:11], v[72:75]\n"
"v_mfma_f32_16x16x32_bf16 v[76:79], v[120:123], a[12:15], v[76:79]\n"
"s_cmp_le_u32 %[lim], 8\n"
"s_cselect_b32 s60, %[soffBw], %[soffB]\n"
"s_add_u32 s60, s60, 1024\n"
"s_add_u32 m0, %[m0B], 0\n"
"s_nop 0\n"
"buffer_load_dwordx4 %[vB0], %[srdB], s60 offen offset:0 lds\n"
"s_waitcnt lgkmcnt(1)\n"
"v_mfma_f32_16x16x32_bf16 v[80:83], v[124:127], a[0:3], v[80:83]\n"
"ds_read_b128 v[112:115], %[vTA1] offset:9216\n"
"v_mfma_f32_16x16x32_bf16 v[84:87], v[124:127], a[4:7], v[84:87]\n"
"v_mfma_f32_16x16x32_bf16 v[88:91], v[124:127], a[8:11], v[88:91]\n"
"v_mfma_f32_16x16x32_bf16 v[92:95], v[124:127], a[12:15], v[92:95]\n"
"buffer_load_dwordx4 %[vB1], %[srdB], s60 offen offset:1024 lds\n"
"s_waitcnt lgkmcnt(1)\n"
"v_mfma_f32_16x16x32_bf16 v[96:99], v[128:131], a[0:3], v[96:99]\n"
"ds_read_b128 v[116:119], %[vTA1] offset:10240\n"
"v_mfma_f32_16x16x32_bf16 v[100:103], v[128:131], a[4:7], v[100:103]\n"
"s_waitcnt lgkmcnt(1)\n"
"v_mfma_f32_16x16x32_bf16 v[32:35], v[112:115], a[16:19], v[32:35]\n"
"ds_read_b128 v[120:123], %[vTA1] offset:11264\n"
"v_mfma_f32_16x16x32_bf16 v[36:39], v[112:115], a[20:23], v[36:39]\n"
"buffer_load_dwordx4 %[vB2], %[srdB], s60 offen offset:2048 lds\n"
"v_mfma_f32_16x16x32_bf16 v[40:43], v[112:115], a[24:27], v[40:43]\n"
"v_mfma_f32_16x16x32_bf16 v[44:47], v[112:115], a[28:31], v[44:47]\n"
"s_waitcnt lgkmcnt(1)\n"
"v_mfma_f32_16x16x32_bf16 v[48:51], v[116:119], a[16:19], v[48:51]\n"
"ds_read_b128 v[124:127], %[vTA1] offset:12288\n"
"v_mfma_f32_16x16x32_bf16 v[52:55], v[116:119], a[20:23], v[52:55]\n"
"buffer_load_dwordx4 %[vB3], %[srdB], s60 offen offset:3072 lds\n"
"v_mfma_f32_16x16x32_bf16 v[56:59], v[116:119], a[24:27], v[56:59]\n"
"v_mfma_f32_16x16x32_bf16 v[60:63], v[116:119], a[28:31], v[60:63]\n"
"s_waitcnt lgkmcnt(1)\n"
"v_mfma_f32_16x16x32_bf16 v[64:67], v[120:123], a[16:19], v[64:67]\n"
"ds_read_b128 v[128:131], %[vT81] offset:9216\n"
"v_mfma_f32_16x16x32_bf16 v[68:71], v[120:123], a[20:23], v[68:71]\n"
"s_waitcnt vmcnt(9)\n"
"s_barrier\n"
"ds_read_b128 a[0:3], %[vTBa01] offset:34816\n"
"v_mfma_f32_16x16x32_bf16 v[72:75], v[120:123], a[24:27], v[72:75]\n"
"ds_read_b128 a[4:7], %[vTBa01] offset:36864\n"
"v_mfma_f32_16x16x32_bf16 v[76:79], v[120:123], a[28:31], v[76:79]\n"
"ds_read_b128 a[8:11], %[vTBb01] offset:38912\n"
"s_waitcnt lgkmcnt(4)\n"
"v_mfma_f32_16x16x32_bf16 v[80:83], v[124:127], a[16:19], v[80:83]\n"
"ds_read_b128 a[12:15], %[vTBb01] offset:40960\n"
"v_mfma_f32_16x16x32_bf16 v[84:87], v[124:127], a[20:23], v[84:87]\n"
"ds_read_b128 v[112:115], %[vTA1] offset:34816\n"
"v_mfma_f32_16x16x32_bf16 v[88:91], v[124:127], a[24:27], v[88:91]\n"
"s_cmp_le_u32 %[lim], 9\n"
"s_cselect_b32 s61, %[soffAw], %[soffA]\n"
"s_add_u32 s61, s61, 165888\n"
"s_add_u32 m0, %[m0A], 34816\n"
"s_nop 0\n"
"buffer_load_dwordx4 %[vA], %[srdA], s61 offen offset:0 sc1 lds\n"
"v_mfma_f32_16x16x32_bf16 v[92:95], v[124:127], a[28:31], v[92:95]\n"
"buffer_load_dwordx4 %[vA], %[srdA], s61 offen offset:1024 sc1 lds\n"
"s_waitcnt lgkmcnt(5)\n"
"v_mfma_f32_16x16x32_bf16 v[96:99], v[128:131], a[16:19], v[96:99]\n"
"ds_read_b128 v[116:119], %[vTA1] offset:35840\n"
"v_mfma_f32_16x16x32_bf16 v[100:103], v[128:131], a[20:23], v[100:103]\n"
"s_waitcnt lgkmcnt(1)\n"
"v_mfma_f32_16x16x32_bf16 v[32:35], v[112:115], a[0:3], v[32:35]\n"
"ds_read_b128 v[120:123], %[vTA1] offset:36864\n"
"v_mfma_f32_16x16x32_bf16 v[36:39], v[112:115], a[4:7], v[36:39]\n"
"buffer_load_dwordx4 %[vA], %[srdA], s61 offen offset:2048 sc1 lds\n"
"v_mfma_f32_16x16x32_bf16 v[40:43], v[112:115], a[8:11], v[40:43]\n"
"ds_read_b128 a[16:19], %[vTBa11] offset:34816\n"
"v_mfma_f32_16x16x32_bf16 v[44:47], v[112:115], a[12:15], v[44:47]\n"
"ds_read_b128 a[20:23], %[vTBa11] offset:36864\n"
"s_waitcnt lgkmcnt(3)\n"
"v_mfma_f32_16x16x32_bf16 v[48:51], v[116:119], a[0:3], v[48:51]\n"
"ds_read_b128 a[24:27], %[vTBb11] offset:38912\n"
"v_mfma_f32_16x16x32_bf16 v[52:55], v[116:119], a[4:7], v[52:55]\n"
"ds_read_b128 a[28:31], %[vTBb11] offset:40960\n"
"v_mfma_f32_16x16x32_bf16 v[56:59], v[116:119], a[8:11], v[56:59]\n"
"ds_read_b128 v[124:127], %[vTA1] offset:37888\n"
"v_mfma_f32_16x16x32_bf16 v[60:63], v[116:119], a[12:15], v[60:63]\n"
"buffer_load_dwordx4 %[vA], %[srdA], s61 offen offset:3072 sc1 lds\n"
"s_waitcnt lgkmcnt(5)\n"
"v_mfma_f32_16x16x32_bf16 v[64:67], v[120:123], a[0:3], v[64:67]\n"
"ds_read_b128 v[128:131], %[vT81] offset:34816\n"
"v_mfma_f32_16x16x32_bf16 v[68:71], v[120:123], a[4:7], v[68:71]\n"
"s_add_u32 m0, %[m0A2], 34816\n"
"s_mov_b32 exec_hi, 0\n"
"buffer_load_dwordx4 %[vAh], %[srdA], s61 offen sc1 lds\n"
"s_mov_b32 exec_hi, -1\n"
"v_mfma_f32_16x16x32_bf16 v[72:75], v[120:123], a[8:11], v[72:75]\n"
"v_mfma_f32_16x16x32_bf16 v[76:79], v[120:123], a[12:15], v[76:79]\n"
"s_cmp_le_u32 %[lim], 9\n"
"s_cselect_b32 s60, %[soffBw], %[soffB]\n"
"s_add_u32 s60, s60, 1152\n"
"s_add_u32 m0, %[m0B], 34816\n"
"s_nop 0\n"
"buffer_load_dwordx4 %[vB0], %[srdB], s60 offen offset:0 lds\n"
"s_waitcnt lgkmcnt(1)\n"
"v_mfma_f32_16x16x32_bf16 v[80:83], v[124:127], a[0:3], v[80:83]\n"
"ds_read_b128 v[112:115], %[vTA1] offset:44032\n"
"v_mfma_f32_16x16x32_bf16 v[84:87], v[124:127], a[4:7], v[84:87]\n"
"v_mfma_f32_16x16x32_bf16 v[88:91], v[124:127], a[8:11], v[88:91]\n"
"v_mfma_f32_16x16x32_bf16 v[92:95], v[124:127], a[12:15], v[92:95]\n"
"buffer_load_dwordx4 %[vB1], %[srdB], s60 offen offset:1024 lds\n"
"s_waitcnt lgkmcnt(1)\n"
"v_mfma_f32_16x16x32_bf16 v[96:99], v[128:131], a[0:3], v[96:99]\n"
"ds_read_b128 v[116:119], %[vTA1] offset:45056\n"
"v_mfma_f32_16x16x32_bf16 v[100:103], v[128:131], a[4:7], v[100:103]\n"
"s_waitcnt lgkmcnt(1)\n"
"v_mfma_f32_16x16x32_bf16 v[32:35], v[112:115], a[16:19], v[32:35]\n"
"ds_read_b128 v[120:123], %[vTA1] offset:46080\n"
"v_mfma_f32_16x16x32_bf16 v[36:39], v[112:115], a[20:23], v[36:39]\n"
"buffer_load_dwordx4 %[vB2], %[srdB], s60 offen offset:2048 lds\n"
"v_mfma_f32_16x16x32_bf16 v[40:43], v[112:115], a[24:27], v[40:43]\n"
"v_mfma_f32_16x16x32_bf16 v[44:47], v[112:115], a[28:31], v[44:47]\n"
"s_waitcnt lgkmcnt(1)\n"
"v_mfma_f32_16x16x32_bf16 v[48:51], v[116:119], a[16:19], v[48:51]\n"
"ds_read_b128 v[124:127], %[vTA1] offset:47104\n"
"v_mfma_f32_16x16x32_bf16 v[52:55], v[116:119], a[20:23], v[52:55]\n"
"buffer_load_dwordx4 %[vB3], %[srdB], s60 offen offset:3072 lds\n"
"v_mfma_f32_16x16x32_bf16 v[56:59], v[116:119], a[24:27], v[56:59]\n"
"v_mfma_f32_16x16x32_bf16 v[60:63], v[116:119], a[28:31], v[60:63]\n"
"s_waitcnt lgkmcnt(1)\n"
"v_mfma_f32_16x16x32_bf16 v[64:67], v[120:123], a[16:19], v[64:67]\n"
"ds_read_b128 v[128:131], %[vT81] offset:44032\n"
"v_mfma_f32_16x16x32_bf16 v[68:71], v[120:123], a[20:23], v[68:71]\n"
"s_waitcnt vmcnt(9)\n"
"s_barrier\n"
"ds_read_b128 a[0:3], %[vTBa00] offset:0\n"
"v_mfma_f32_16x16x32_bf16 v[72:75], v[120:123], a[24:27], v[72:75]\n"
"ds_read_b128 a[4:7], %[vTBa00] offset:2048\n"
"v_mfma_f32_16x16x32_bf16 v[76:79], v[120:123], a[28:31], v[76:79]\n"
"ds_read_b128 a[8:11], %[vTBb00] offset:4096\n"
"s_waitcnt lgkmcnt(4)\n"
"v_mfma_f32_16x16x32_bf16 v[80:83], v[124:127], a[16:19], v[80:83]\n"
"ds_read_b128 a[12:15], %[vTBb00] offset:6144\n"
"v_mfma_f32_16x16x32_bf16 v[84:87], v[124:127], a[20:23], v[84:87]\n"
"ds_read_b128 v[112:115], %[vTA0] offset:0\n"
"v_mfma_f32_16x16x32_bf16 v[88:91], v[124:127], a[24:27], v[88:91]\n"
"s_cmp_le_u32 %[lim], 10\n"
"s_cselect_b32 s61, %[soffAw], %[soffA]\n"
"s_add_u32 s61, s61, 184320\n"
"s_add_u32 m0, %[m0A], 69632\n"
"s_nop 0\n"
"buffer_load_dwordx4 %[vA], %[srdA], s61 offen offset:0 sc1 lds\n"
"v_mfma_f32_16x16x32_bf16 v[92:95], v[124:127], a[28:31], v[92:95]\n"
"buffer_load_dwordx4 %[vA], %[srdA], s61 offen offset:1024 sc1 lds\n"
"s_waitcnt lgkmcnt(5)\n"
"v_mfma_f32_16x16x32_bf16 v[96:99], v[128:131], a[16:19], v[96:99]\n"
"ds_read_b128 v[116:119], %[vTA0] offset:1024\n"
"v_mfma_f32_16x16x32_bf16 v[100:103], v[128:131], a[20:23], v[100:103]\n"
"s_waitcnt lgkmcnt(1)\n"
"v_mfma_f32_16x16x32_bf16 v[32:35], v[112:115], a[0:3], v[32:35]\n"
"ds_read_b128 v[120:123], %[vTA0] offset:2048\n"
"v_mfma_f32_16x16x32_bf16 v[36:39], v[112:115], a[4:7], v[36:39]\n"
"buffer_load_dwordx4 %[vA], %[srdA], s61 offen offset:2048 sc1 lds\n"
"v_mfma_f32_16x16x32_bf16 v[40:43], v[112:115], a[8:11], v[40:43]\n"
"ds_read_b128 a[16:19], %[vTBa10] offset:0\n"
"v_mfma_f32_16x16x32_bf16 v[44:47], v[112:115], a[12:15], v[44:47]\n"
"ds_read_b128 a[20:23], %[vTBa10] offset:2048\n"
"s_waitcnt lgkmcnt(3)\n"
"v_mfma_f32_16x16x32_bf16 v[48:51], v[116:119], a[0:3], v[48:51]\n"
"ds_read_b128 a[24:27], %[vTBb10] offset:4096\n"
"v_mfma_f32_16x16x32_bf16 v[52:55], v[116:119], a[4:7], v[52:55]\n"
"ds_read_b128 a[28:31], %[vTBb10] offset:6144\n"
"v_mfma_f32_16x16x32_bf16 v[56:59], v[116:119], a[8:11], v[56:59]\n"
"ds_read_b128 v[124:127], %[vTA0] offset:3072\n"
"v_mfma_f32_16x16x32_bf16 v[60:63], v[116:119], a[12:15], v[60:63]\n"
"buffer_load_dwordx4 %[vA], %[srdA], s61 offen offset:3072 sc1 lds\n"
"s_waitcnt lgkmcnt(5)\n"
"v_mfma_f32_16x16x32_bf16 v[64:67], v[120:123], a[0:3], v[64:67]\n"
"ds_read_b128 v[128:131], %[vT80] offset:0\n"
"v_mfma_f32_16x16x32_bf16 v[68:71], v[120:123], a[4:7], v[68:71]\n"
"s_add_u32 m0, %[m0A2], 69632\n"
"s_mov_b32 exec_hi, 0\n"
"buffer_load_dwordx4 %[vAh], %[srdA], s61 offen sc1 lds\n"
"s_mov_b32 exec_hi, -1\n"
"v_mfma_f32_16x16x32_bf16 v[72:75], v[120:123], a[8:11], v[72:75]\n"
"v_mfma_f32_16x16x32_bf16 v[76:79], v[120:123], a[12:15], v[76:79]\n"
"s_cmp_le_u32 %[lim], 10\n"
"s_cselect_b32 s60, %[soffBw], %[soffB]\n"
"s_add_u32 s60, s60, 1280\n"
"s_add_u32 m0, %[m0B], 69632\n"
"s_nop 0\n"
"buffer_load_dwordx4 %[vB0], %[srdB], s60 offen offset:0 lds\n"
"s_waitcnt lgkmcnt(1)\n"
"v_mfma_f32_16x16x32_bf16 v[80:83], v[124:127], a[0:3], v[80:83]\n"
"ds_read_b128 v[112:115], %[vTA0] offset:9216\n"
"v_mfma_f32_16x16x32_bf16 v[84:87], v[124:127], a[4:7], v[84:87]\n"
"v_mfma_f32_16x16x32_bf16 v[88:91], v[124:127], a[8:11], v[88:91]\n"
"v_mfma_f32_16x16x32_bf16 v[92:95], v[124:127], a[12:15], v[92:95]\n"
"buffer_load_dwordx4 %[vB1], %[srdB], s60 offen offset:1024 lds\n"
"s_waitcnt lgkmcnt(1)\n"
"v_mfma_f32_16x16x32_bf16 v[96:99], v[128:131], a[0:3], v[96:99]\n"
"ds_read_b128 v[116:119], %[vTA0] offset:10240\n"
"v_mfma_f32_16x16x32_bf16 v[100:103], v[128:131], a[4:7], v[100:103]\n"
"s_waitcnt lgkmcnt(1)\n"
"v_mfma_f32_16x16x32_bf16 v[32:35], v[112:115], a[16:19], v[32:35]\n"
"ds_read_b128 v[120:123], %[vTA0] offset:11264\n"
"v_mfma_f32_16x16x32_bf16 v[36:39], v[112:115], a[20:23], v[36:39]\n"
"buffer_load_dwordx4 %[vB2], %[srdB], s60 offen offset:2048 lds\n"
"v_mfma_f32_16x16x32_bf16 v[40:43], v[112:115], a[24:27], v[40:43]\n"
"v_mfma_f32_16x16x32_bf16 v[44:47], v[112:115], a[28:31], v[44:47]\n"
"s_waitcnt lgkmcnt(1)\n"
"v_mfma_f32_16x16x32_bf16 v[48:51], v[116:119], a[16:19], v[48:51]\n"
"ds_read_b128 v[124:127], %[vTA0] offset:12288\n"
"v_mfma_f32_16x16x32_bf16 v[52:55], v[116:119], a[20:23], v[52:55]\n"
"buffer_load_dwordx4 %[vB3], %[srdB], s60 offen offset:3072 lds\n"
"v_mfma_f32_16x16x32_bf16 v[56:59], v[116:119], a[24:27], v[56:59]\n"
"v_mfma_f32_16x16x32_bf16 v[60:63], v[116:119], a[28:31], v[60:63]\n"
"s_waitcnt lgkmcnt(1)\n"
"v_mfma_f32_16x16x32_bf16 v[64:67], v[120:123], a[16:19], v[64:67]\n"
"ds_read_b128 v[128:131], %[vT80] offset:9216\n"
"v_mfma_f32_16x16x32_bf16 v[68:71], v[120:123], a[20:23], v[68:71]\n"
"s_waitcnt vmcnt(9)\n"
"s_barrier\n"
"ds_read_b128 a[0:3], %[vTBa00] offset:34816\n"
"v_mfma_f32_16x16x32_bf16 v[72:75], v[120:123], a[24:27], v[72:75]\n"
"ds_read_b128 a[4:7], %[vTBa00] offset:36864\n"
"v_mfma_f32_16x16x32_bf16 v[76:79], v[120:123], a[28:31], v[76:79]\n"
"ds_read_b128 a[8:11], %[vTBb00] offset:38912\n"
"s_waitcnt lgkmcnt(4)\n"
"v_mfma_f32_16x16x32_bf16 v[80:83], v[124:127], a[16:19], v[80:83]\n"
"ds_read_b128 a[12:15], %[vTBb00] offset:40960\n"
"v_mfma_f32_16x16x32_bf16 v[84:87], v[124:127], a[20:23], v[84:87]\n"
"ds_read_b128 v[112:115], %[vTA0] offset:34816\n"
"v_mfma_f32_16x16x32_bf16 v[88:91], v[124:127], a[24:27], v[88:91]\n"
"s_cmp_le_u32 %[lim], 11\n"
"s_cselect_b32 s61, %[soffAw], %[soffA]\n"
"s_add_u32 s61, s61, 202752\n"
"s_add_u32 m0, %[m0A], 104448\n"
"s_nop 0\n"
"buffer_load_dwordx4 %[vA], %[srdA], s61 offen offset:0 sc1 lds\n"
"v_mfma_f32_16x16x32_bf16 v[92:95], v[124:127], a[28:31], v[92:95]\n"
"buffer_load_dwordx4 %[vA], %[srdA], s61 offen offset:1024 sc1 lds\n"
"s_waitcnt lgkmcnt(5)\n"
"v_mfma_f32_16x16x32_bf16 v[96:99], v[128:131], a[16:19], v[96:99]\n"
"ds_read_b128 v[116:119], %[vTA0] offset:35840\n"
"v_mfma_f32_16x16x32_bf16 v[100:103], v[128:131], a[20:23], v[100:103]\n"
"s_waitcnt lgkmcnt(1)\n"
"v_mfma_f32_16x16x32_bf16 v[32:35], v[112:115], a[0:3], v[32:35]\n"
"ds_read_b128 v[120:123], %[vTA0] offset:36864\n"
"v_mfma_f32_16x16x32_bf16 v[36:39], v[112:115], a[4:7], v[36:39]\n"
"buffer_load_dwordx4 %[vA], %[srdA], s61 offen offset:2048 sc1 lds\n"
"v_mfma_f32_16x16x32_bf16 v[40:43], v[112:115], a[8:11], v[40:43]\n"
"ds_read_b128 a[16:19], %[vTBa10] offset:34816\n"
"v_mfma_f32_16x16x32_bf16 v[44:47], v[112:115], a[12:15], v[44:47]\n"
"ds_read_b128 a[20:23], %[vTBa10] offset:36864\n"
"s_waitcnt lgkmcnt(3)\n"
"v_mfma_f32_16x16x32_bf16 v[48:51], v[116:119], a[0:3], v[48:51]\n"
"ds_read_b128 a[24:27], %[vTBb10] offset:38912\n"
"v_mfma_f32_16x16x32_bf16 v[52:55], v[116:119], a[4:7], v[52:55]\n"
"ds_read_b128 a[28:31], %[vTBb10] offset:40960\n"
"v_mfma_f32_16x16x32_bf16 v[56:59], v[116:119], a[8:11], v[56:59]\n"
"ds_read_b128 v[124:127], %[vTA0] offset:37888\n"
"v_mfma_f32_16x16x32_bf16 v[60:63], v[116:119], a[12:15], v[60:63]\n"
"buffer_load_dwordx4 %[vA], %[srdA], s61 offen offset:3072 sc1 lds\n"
"s_waitcnt lgkmcnt(5)\n"
"v_mfma_f32_16x16x32_bf16 v[64:67], v[120:123], a[0:3], v[64:67]\n"
"ds_read_b128 v[128:131], %[vT80] offset:34816\n"
"v_mfma_f32_16x16x32_bf16 v[68:71], v[120:123], a[4:7], v[68:71]\n"
"s_add_u32 m0, %[m0A2], 104448\n"
"s_mov_b32 exec_hi, 0\n"
"buffer_load_dwordx4 %[vAh], %[srdA], s61 offen sc1 lds\n"
"s_mov_b32 exec_hi, -1\n"
"v_mfma_f32_16x16x32_bf16 v[72:75], v[120:123], a[8:11], v[72:75]\n"
"v_mfma_f32_16x16x32_bf16 v[76:79], v[120:123], a[12:15], v[76:79]\n"
"s_cmp_le_u32 %[lim], 11\n"
"s_cselect_b32 s60, %[soffBw], %[soffB]\n"
"s_add_u32 s60, s60, 1408\n"
"s_add_u32 m0, %[m0B], 104448\n"
"s_nop 0\n"
"buffer_load_dwordx4 %[vB0], %[srdB], s60 offen offset:0 lds\n"
"s_waitcnt lgkmcnt(1)\n"
"v_mfma_f32_16x16x32_bf16 v[80:83], v[124:127], a[0:3], v[80:83]\n"
"ds_read_b128 v[112:115], %[vTA0] offset:44032\n"
"v_mfma_f32_16x16x32_bf16 v[84:87], v[124:127], a[4:7], v[84:87]\n"
"v_mfma_f32_16x16x32_bf16 v[88:91], v[124:127], a[8:11], v[88:91]\n"
"v_mfma_f32_16x16x32_bf16 v[92:95], v[124:127], a[12:15], v[92:95]\n"
"buffer_load_dwordx4 %[vB1], %[srdB], s60 offen offset:1024 lds\n"
"s_waitcnt lgkmcnt(1)\n"
"v_mfma_f32_16x16x32_bf16 v[96:99], v[128:131], a[0:3], v[96:99]\n"
"ds_read_b128 v[116:119], %[vTA0] offset:45056\n"
"v_mfma_f32_16x16x32_bf16 v[100:103], v[128:131], a[4:7], v[100:103]\n"
"s_waitcnt lgkmcnt(1)\n"
"v_mfma_f32_16x16x32_bf16 v[32:35], v[112:115], a[16:19], v[32:35]\n"
"ds_read_b128 v[120:123], %[vTA0] offset:46080\n"
"v_mfma_f32_16x16x32_bf16 v[36:39], v[112:115], a[20:23], v[36:39]\n"
"buffer_load_dwordx4 %[vB2], %[srdB], s60 offen offset:2048 lds\n"
"v_mfma_f32_16x16x32_bf16 v[40:43], v[112:115], a[24:27], v[40:43]\n"
"v_mfma_f32_16x16x32_bf16 v[44:47], v[112:115], a[28:31], v[44:47]\n"
"s_waitcnt lgkmcnt(1)\n"
"v_mfma_f32_16x16x32_bf16 v[48:51], v[116:119], a[16:19], v[48:51]\n"
"ds_read_b128 v[124:127], %[vTA0] offset:47104\n"
"v_mfma_f32_16x16x32_bf16 v[52:55], v[116:119], a[20:23], v[52:55]\n"
"buffer_load_dwordx4 %[vB3], %[srdB], s60 offen offset:3072 lds\n"
"v_mfma_f32_16x16x32_bf16 v[56:59], v[116:119], a[24:27], v[56:59]\n"
"v_mfma_f32_16x16x32_bf16 v[60:63], v[116:119], a[28:31], v[60:63]\n"
"s_waitcnt lgkmcnt(1)\n"
"v_mfma_f32_16x16x32_bf16 v[64:67], v[120:123], a[16:19], v[64:67]\n"
"ds_read_b128 v[128:131], %[vT80] offset:44032\n"
"v_mfma_f32_16x16x32_bf16 v[68:71], v[120:123], a[20:23], v[68:71]\n"
"s_waitcnt vmcnt(9)\n"
"s_barrier\n"
"ds_read_b128 a[0:3], %[vTBa01] offset:0\n"
"v_mfma_f32_16x16x32_bf16 v[72:75], v[120:123], a[24:27], v[72:75]\n"
"ds_read_b128 a[4:7], %[vTBa01] offset:2048\n"
"v_mfma_f32_16x16x32_bf16 v[76:79], v[120:123], a[28:31], v[76:79]\n"
"ds_read_b128 a[8:11], %[vTBb01] offset:4096\n"
"s_waitcnt lgkmcnt(4)\n"
"v_mfma_f32_16x16x32_bf16 v[80:83], v[124:127], a[16:19], v[80:83]\n"
"ds_read_b128 a[12:15], %[vTBb01] offset:6144\n"
"v_mfma_f32_16x16x32_bf16 v[84:87], v[124:127], a[20:23], v[84:87]\n"
"ds_read_b128 v[112:115], %[vTA1] offset:0\n"
"v_mfma_f32_16x16x32_bf16 v[88:91], v[124:127], a[24:27], v[88:91]\n"
"s_cmp_le_u32 %[lim], 12\n"
"s_cselect_b32 s61, %[soffAw], %[soffA]\n"
"s_add_u32 s61, s61, 221184\n"
"s_add_u32 m0, %[m0A], 0\n"
"s_nop 0\n"
"buffer_load_dwordx4 %[vA], %[srdA], s61 offen offset:0 sc1 lds\n"
"v_mfma_f32_16x16x32_bf16 v[92:95], v[124:127], a[28:31], v[92:95]\n"
"buffer_load_dwordx4 %[vA], %[srdA], s61 offen offset:1024 sc1 lds\n"
"s_waitcnt lgkmcnt(5)\n"
"v_mfma_f32_16x16x32_bf16 v[96:99], v[128:131], a[16:19], v[96:99]\n"
"ds_read_b128 v[116:119], %[vTA1] offset:1024\n"
"v_mfma_f32_16x16x32_bf16 v[100:103], v[128:131], a[20:23], v[100:103]\n"
"s_waitcnt lgkmcnt(1)\n"
"v_mfma_f32_16x16x32_bf16 v[32:35], v[112:115], a[0:3], v[32:35]\n"
"ds_read_b128 v[120:123], %[vTA1] offset:2048\n"
"v_mfma_f32_16x16x32_bf16 v[36:39], v[112:115], a[4:7], v[36:39]\n"
"buffer_load_dwordx4 %[vA], %[srdA], s61 offen offset:2048 sc1 lds\n"
"v_mfma_f32_16x16x32_bf16 v[40:43], v[112:115], a[8:11], v[40:43]\n"
"ds_read_b128 a[16:19], %[vTBa11] offset:0\n"
"v_mfma_f32_16x16x32_bf16 v[44:47], v[112:115], a[12:15], v[44:47]\n"
"ds_read_b128 a[20:23], %[vTBa11] offset:2048\n"
"s_waitcnt lgkmcnt(3)\n"
"v_mfma_f32_16x16x32_bf16 v[48:51], v[116:119], a[0:3], v[48:51]\n"
"ds_read_b128 a[24:27], %[vTBb11] offset:4096\n"
"v_mfma_f32_16x16x32_bf16 v[52:55], v[116:119], a[4:7], v[52:55]\n"
"ds_read_b128 a[28:31], %[vTBb11] offset:6144\n"
"v_mfma_f32_16x16x32_bf16 v[56:59], v[116:119], a[8:11], v[56:59]\n"
"ds_read_b128 v[124:127], %[vTA1] offset:3072\n"
"v_mfma_f32_16x16x32_bf16 v[60:63], v[116:119], a[12:15], v[60:63]\n"
"buffer_load_dwordx4 %[vA], %[srdA], s61 offen offset:3072 sc1 lds\n"
"s_waitcnt lgkmcnt(5)\n"
"v_mfma_f32_16x16x32_bf16 v[64:67], v[120:123], a[0:3], v[64:67]\n"
"ds_read_b128 v[128:131], %[vT81] offset:0\n"
"v_mfma_f32_16x16x32_bf16 v[68:71], v[120:123], a[4:7], v[68:71]\n"
"s_add_u32 m0, %[m0A2], 0\n"
"s_mov_b32 exec_hi, 0\n"
"buffer_load_dwordx4 %[vAh], %[srdA], s61 offen sc1 lds\n"
"s_mov_b32 exec_hi, -1\n"
"v_mfma_f32_16x16x32_bf16 v[72:75], v[120:123], a[8:11], v[72:75]\n"
"v_mfma_f32_16x16x32_bf16 v[76:79], v[120:123], a[12:15], v[76:79]\n"
"s_cmp_le_u32 %[lim], 12\n"
"s_cselect_b32 s60, %[soffBw], %[soffB]\n"
"s_add_u32 s60, s60, 1536\n"
"s_add_u32 m0, %[m0B], 0\n"
"s_nop 0\n"
"buffer_load_dwordx4 %[vB0], %[srdB], s60 offen offset:0 lds\n"
"s_waitcnt lgkmcnt(1)\n"
"v_mfma_f32_16x16x32_bf16 v[80:83], v[124:127], a[0:3], v[80:83]\n"
"ds_read_b128 v[112:115], %[vTA1] offset:9216\n"
"v_mfma_f32_16x16x32_bf16 v[84:87], v[124:127], a[4:7], v[84:87]\n"
"v_mfma_f32_16x16x32_bf16 v[88:91], v[124:127], a[8:11], v[88:91]\n"
"v_mfma_f32_16x16x32_bf16 v[92:95], v[124:127], a[12:15], v[92:95]\n"
"buffer_load_dwordx4 %[vB1], %[srdB], s60 offen offset:1024 lds\n"
"s_waitcnt lgkmcnt(1)\n"
"v_mfma_f32_16x16x32_bf16 v[96:99], v[128:131], a[0:3], v[96:99]\n"
"ds_read_b128 v[116:119], %[vTA1] offset:10240\n"
"v_mfma_f32_16x16x32_bf16 v[100:103], v[128:131], a[4:7], v[100:103]\n"
"s_waitcnt lgkmcnt(1)\n"
"v_mfma_f32_16x16x32_bf16 v[32:35], v[112:115], a[16:19], v[32:35]\n"
"ds_read_b128 v[120:123], %[vTA1] offset:11264\n"
"v_mfma_f32_16x16x32_bf16 v[36:39], v[112:115], a[20:23], v[36:39]\n"
"buffer_load_dwordx4 %[vB2], %[srdB], s60 offen offset:2048 lds\n"
"v_mfma_f32_16x16x32_bf16 v[40:43], v[112:115], a[24:27], v[40:43]\n"
"v_mfma_f32_16x16x32_bf16 v[44:47], v[112:115], a[28:31], v[44:47]\n"
"s_waitcnt lgkmcnt(1)\n"
"v_mfma_f32_16x16x32_bf16 v[48:51], v[116:119], a[16:19], v[48:51]\n"
"ds_read_b128 v[124:127], %[vTA1] offset:12288\n"
"v_mfma_f32_16x16x32_bf16 v[52:55], v[116:119], a[20:23], v[52:55]\n"
"buffer_load_dwordx4 %[vB3], %[srdB], s60 offen offset:3072 lds\n"
"v_mfma_f32_16x16x32_bf16 v[56:59], v[116:119], a[24:27], v[56:59]\n"
"v_mfma_f32_16x16x32_bf16 v[60:63], v[116:119], a[28:31], v[60:63]\n"
"s_waitcnt lgkmcnt(1)\n"
"v_mfma_f32_16x16x32_bf16 v[64:67], v[120:123], a[16:19], v[64:67]\n"
"ds_read_b128 v[128:131], %[vT81] offset:9216\n"
"v_mfma_f32_16x16x32_bf16 v[68:71], v[120:123], a[20:23], v[68:71]\n"
"s_waitcnt vmcnt(9)\n"
"s_barrier\n"
"ds_read_b128 a[0:3], %[vTBa01] offset:34816\n"
"v_mfma_f32_16x16x32_bf16 v[72:75], v[120:123], a[24:27], v[72:75]\n"
"ds_read_b128 a[4:7], %[vTBa01] offset:36864\n"
"v_mfma_f32_16x16x32_bf16 v[76:79], v[120:123], a[28:31], v[76:79]\n"
"ds_read_b128 a[8:11], %[vTBb01] offset:38912\n"
"s_waitcnt lgkmcnt(4)\n"
"v_mfma_f32_16x16x32_bf16 v[80:83], v[124:127], a[16:19], v[80:83]\n"
"ds_read_b128 a[12:15], %[vTBb01] offset:40960\n"
"v_mfma_f32_16x16x32_bf16 v[84:87], v[124:127], a[20:23], v[84:87]\n"
"ds_read_b128 v[112:115], %[vTA1] offset:34816\n"
"v_mfma_f32_16x16x32_bf16 v[88:91], v[124:127], a[24:27], v[88:91]\n"
"s_cmp_le_u32 %[lim], 13\n"
"s_cselect_b32 s61, %[soffAw], %[soffA]\n"
"s_add_u32 s61, s61, 239616\n"
"s_add_u32 m0, %[m0A], 34816\n"
"s_nop 0\n"
"buffer_load_dwordx4 %[vA], %[srdA], s61 offen offset:0 sc1 lds\n"
"v_mfma_f32_16x16x32_bf16 v[92:95], v[124:127], a[28:31], v[92:95]\n"
"buffer_load_dwordx4 %[vA], %[srdA], s61 offen offset:1024 sc1 lds\n"
"s_waitcnt lgkmcnt(5)\n"
"v_mfma_f32_16x16x32_bf16 v[96:99], v[128:131], a[16:19], v[96:99]\n"
"ds_read_b128 v[116:119], %[vTA1] offset:35840\n"
"v_mfma_f32_16x16x32_bf16 v[100:103], v[128:131], a[20:23], v[100:103]\n"
"s_waitcnt lgkmcnt(1)\n"
"v_mfma_f32_16x16x32_bf16 v[32:35], v[112:115], a[0:3], v[32:35]\n"
"ds_read_b128 v[120:123], %[vTA1] offset:36864\n"
"v_mfma_f32_16x16x32_bf16 v[36:39], v[112:115], a[4:7], v[36:39]\n"
"buffer_load_dwordx4 %[vA], %[srdA], s61 offen offset:2048 sc1 lds\n"
"v_mfma_f32_16x16x32_bf16 v[40:43], v[112:115], a[8:11], v[40:43]\n"
"ds_read_b128 a[16:19], %[vTBa11] offset:34816\n"
"v_mfma_f32_16x16x32_bf16 v[44:47], v[112:115], a[12:15], v[44:47]\n"
"ds_read_b128 a[20:23], %[vTBa11] offset:36864\n"
"s_waitcnt lgkmcnt(3)\n"
"v_mfma_f32_16x16x32_bf16 v[48:51], v[116:119], a[0:3], v[48:51]\n"
"ds_read_b128 a[24:27], %[vTBb11] offset:38912\n"
"v_mfma_f32_16x16x32_bf16 v[52:55], v[116:119], a[4:7], v[52:55]\n"
"ds_read_b128 a[28:31], %[vTBb11] offset:40960\n"
"v_mfma_f32_16x16x32_bf16 v[56:59], v[116:119], a[8:11], v[56:59]\n"
"ds_read_b128 v[124:127], %[vTA1] offset:37888\n"
"v_mfma_f32_16x16x32_bf16 v[60:63], v[116:119], a[12:15], v[60:63]\n"
"buffer_load_dwordx4 %[vA], %[srdA], s61 offen offset:3072 sc1 lds\n"
"s_waitcnt lgkmcnt(5)\n"
"v_mfma_f32_16x16x32_bf16 v[64:67], v[120:123], a[0:3], v[64:67]\n"
"ds_read_b128 v[128:131], %[vT81] offset:34816\n"
"v_mfma_f32_16x16x32_bf16 v[68:71], v[120:123], a[4:7], v[68:71]\n"
"s_add_u32 m0, %[m0A2], 34816\n"
"s_mov_b32 exec_hi, 0\n"
"buffer_load_dwordx4 %[vAh], %[srdA], s61 offen sc1 lds\n"
"s_mov_b32 exec_hi, -1\n"
"v_mfma_f32_16x16x32_bf16 v[72:75], v[120:123], a[8:11], v[72:75]\n"
"v_mfma_f32_16x16x32_bf16 v[76:79], v[120:123], a[12:15], v[76:79]\n"
"s_cmp_le_u32 %[lim], 13\n"
"s_cselect_b32 s60, %[soffBw], %[soffB]\n"
"s_add_u32 s60, s60, 1664\n"
"s_add_u32 m0, %[m0B], 34816\n"
"s_nop 0\n"
"buffer_load_dwordx4 %[vB0], %[srdB], s60 offen offset:0 lds\n"
"s_waitcnt lgkmcnt(1)\n"
"v_mfma_f32_16x16x32_bf16 v[80:83], v[124:127], a[0:3], v[80:83]\n"
"ds_read_b128 v[112:115], %[vTA1] offset:44032\n"
"v_mfma_f32_16x16x32_bf16 v[84:87], v[124:127], a[4:7], v[84:87]\n"
"v_mfma_f32_16x16x32_bf16 v[88:91], v[124:127], a[8:11], v[88:91]\n"
"v_mfma_f32_16x16x32_bf16 v[92:95], v[124:127], a[12:15], v[92:95]\n"
"buffer_load_dwordx4 %[vB1], %[srdB], s60 offen offset:1024 lds\n"
"s_waitcnt lgkmcnt(1)\n"
"v_mfma_f32_16x16x32_bf16 v[96:99], v[128:131], a[0:3], v[96:99]\n"
"ds_read_b128 v[116:119], %[vTA1] offset:45056\n"
"v_mfma_f32_16x16x32_bf16 v[100:103], v[128:131], a[4:7], v[100:103]\n"
"s_waitcnt lgkmcnt(1)\n"
"v_mfma_f32_16x16x32_bf16 v[32:35], v[112:115], a[16:19], v[32:35]\n"
"ds_read_b128 v[120:123], %[vTA1] offset:46080\n"
"v_mfma_f32_16x16x32_bf16 v[36:39], v[112:115], a[20:23], v[36:39]\n"
"buffer_load_dwordx4 %[vB2], %[srdB], s60 offen offset:2048 lds\n"
"v_mfma_f32_16x16x32_bf16 v[40:43], v[112:115], a[24:27], v[40:43]\n"
"v_mfma_f32_16x16x32_bf16 v[44:47], v[112:115], a[28:31], v[44:47]\n"
"s_waitcnt lgkmcnt(1)\n"
"v_mfma_f32_16x16x32_bf16 v[48:51], v[116:119], a[16:19], v[48:51]\n"
"ds_read_b128 v[124:127], %[vTA1] offset:47104\n"
"v_mfma_f32_16x16x32_bf16 v[52:55], v[116:119], a[20:23], v[52:55]\n"
"buffer_load_dwordx4 %[vB3], %[srdB], s60 offen offset:3072 lds\n"
"v_mfma_f32_16x16x32_bf16 v[56:59], v[116:119], a[24:27], v[56:59]\n"
"v_mfma_f32_16x16x32_bf16 v[60:63], v[116:119], a[28:31], v[60:63]\n"
"s_waitcnt lgkmcnt(1)\n"
"v_mfma_f32_16x16x32_bf16 v[64:67], v[120:123], a[16:19], v[64:67]\n"
"ds_read_b128 v[128:131], %[vT81] offset:44032\n"
"v_mfma_f32_16x16x32_bf16 v[68:71], v[120:123], a[20:23], v[68:71]\n"
"s_waitcnt vmcnt(9)\n"
"s_barrier\n"
"ds_read_b128 a[0:3], %[vTBa00] offset:0\n"
"v_mfma_f32_16x16x32_bf16 v[72:75], v[120:123], a[24:27], v[72:75]\n"
"ds_read_b128 a[4:7], %[vTBa00] offset:2048\n"
"v_mfma_f32_16x16x32_bf16 v[76:79], v[120:123], a[28:31], v[76:79]\n"
"ds_read_b128 a[8:11], %[vTBb00] offset:4096\n"
"s_waitcnt lgkmcnt(4)\n"
"v_mfma_f32_16x16x32_bf16 v[80:83], v[124:127], a[16:19], v[80:83]\n"
"ds_read_b128 a[12:15], %[vTBb00] offset:6144\n"
"v_mfma_f32_16x16x32_bf16 v[84:87], v[124:127], a[20:23], v[84:87]\n"
"ds_read_b128 v[112:115], %[vTA0] offset:0\n"
"v_mfma_f32_16x16x32_bf16 v[88:91], v[124:127], a[24:27], v[88:91]\n"
"s_cmp_le_u32 %[lim], 14\n"
"s_cselect_b32 s61, %[soffAw], %[soffA]\n"
"s_add_u32 s61, s61, 258048\n"
"s_add_u32 m0, %[m0A], 69632\n"
"s_nop 0\n"
"buffer_load_dwordx4 %[vA], %[srdA], s61 offen offset:0 sc1 lds\n"
"v_mfma_f32_16x16x32_bf16 v[92:95], v[124:127], a[28:31], v[92:95]\n"
"buffer_load_dwordx4 %[vA], %[srdA], s61 offen offset:1024 sc1 lds\n"
"s_waitcnt lgkmcnt(5)\n"
"v_mfma_f32_16x16x32_bf16 v[96:99], v[128:131], a[16:19], v[96:99]\n"
"ds_read_b128 v[116:119], %[vTA0] offset:1024\n"
"v_mfma_f32_16x16x32_bf16 v[100:103], v[128:131], a[20:23], v[100:103]\n"
"s_waitcnt lgkmcnt(1)\n"
"v_mfma_f32_16x16x32_bf16 v[32:35], v[112:115], a[0:3], v[32:35]\n"
"ds_read_b128 v[120:123], %[vTA0] offset:2048\n"
"v_mfma_f32_16x16x32_bf16 v[36:39], v[112:115], a[4:7], v[36:39]\n"
"buffer_load_dwordx4 %[vA], %[srdA], s61 offen offset:2048 sc1 lds\n"
"v_mfma_f32_16x16x32_bf16 v[40:43], v[112:115], a[8:11], v[40:43]\n"
"ds_read_b128 a[16:19], %[vTBa10] offset:0\n"
"v_mfma_f32_16x16x32_bf16 v[44:47], v[112:115], a[12:15], v[44:47]\n"
"ds_read_b128 a[20:23], %[vTBa10] offset:2048\n"
"s_waitcnt lgkmcnt(3)\n"
"v_mfma_f32_16x16x32_bf16 v[48:51], v[116:119], a[0:3], v[48:51]\n"
"ds_read_b128 a[24:27], %[vTBb10] offset:4096\n"
"v_mfma_f32_16x16x32_bf16 v[52:55], v[116:119], a[4:7], v[52:55]\n"
"ds_read_b128 a[28:31], %[vTBb10] offset:6144\n"
"v_mfma_f32_16x16x32_bf16 v[56:59], v[116:119], a[8:11], v[56:59]\n"
"ds_read_b128 v[124:127], %[vTA0] offset:3072\n"
"v_mfma_f32_16x16x32_bf16 v[60:63], v[116:119], a[12:15], v[60:63]\n"
"buffer_load_dwordx4 %[vA], %[srdA], s61 offen offset:3072 sc1 lds\n"
"s_waitcnt lgkmcnt(5)\n"
"v_mfma_f32_16x16x32_bf16 v[64:67], v[120:123], a[0:3], v[64:67]\n"
"ds_read_b128 v[128:131], %[vT80] offset:0\n"
"v_mfma_f32_16x16x32_bf16 v[68:71], v[120:123], a[4:7], v[68:71]\n"
"s_add_u32 m0, %[m0A2], 69632\n"
"s_mov_b32 exec_hi, 0\n"
"buffer_load_dwordx4 %[vAh], %[srdA], s61 offen sc1 lds\n"
"s_mov_b32 exec_hi, -1\n"
"v_mfma_f32_16x16x32_bf16 v[72:75], v[120:123], a[8:11], v[72:75]\n"
"v_mfma_f32_16x16x32_bf16 v[76:79], v[120:123], a[12:15], v[76:79]\n"
"s_cmp_le_u32 %[lim], 14\n"
"s_cselect_b32 s60, %[soffBw], %[soffB]\n"
"s_add_u32 s60, s60, 1792\n"
"s_add_u32 m0, %[m0B], 69632\n"
"s_nop 0\n"
"buffer_load_dwordx4 %[vB0], %[srdB], s60 offen offset:0 lds\n"
"s_waitcnt lgkmcnt(1)\n"
"v_mfma_f32_16x16x32_bf16 v[80:83], v[124:127], a[0:3], v[80:83]\n"
"ds_read_b128 v[112:115], %[vTA0] offset:9216\n"
"v_mfma_f32_16x16x32_bf16 v[84:87], v[124:127], a[4:7], v[84:87]\n"
"v_mfma_f32_16x16x32_bf16 v[88:91], v[124:127], a[8:11], v[88:91]\n"
"v_mfma_f32_16x16x32_bf16 v[92:95], v[124:127], a[12:15], v[92:95]\n"
"buffer_load_dwordx4 %[vB1], %[srdB], s60 offen offset:1024 lds\n"
"s_waitcnt lgkmcnt(1)\n"
"v_mfma_f32_16x16x32_bf16 v[96:99], v[128:131], a[0:3], v[96:99]\n"
"ds_read_b128 v[116:119], %[vTA0] offset:10240\n"
"v_mfma_f32_16x16x32_bf16 v[100:103], v[128:131], a[4:7], v[100:103]\n"
"s_waitcnt lgkmcnt(1)\n"
"v_mfma_f32_16x16x32_bf16 v[32:35], v[112:115], a[16:19], v[32:35]\n"
"ds_read_b128 v[120:123], %[vTA0] offset:11264\n"
"v_mfma_f32_16x16x32_bf16 v[36:39], v[112:115], a[20:23], v[36:39]\n"
"buffer_load_dwordx4 %[vB2], %[srdB], s60 offen offset:2048 lds\n"
"v_mfma_f32_16x16x32_bf16 v[40:43], v[112:115], a[24:27], v[40:43]\n"
"v_mfma_f32_16x16x32_bf16 v[44:47], v[112:115], a[28:31], v[44:47]\n"
"s_waitcnt lgkmcnt(1)\n"
"v_mfma_f32_16x16x32_bf16 v[48:51], v[116:119], a[16:19], v[48:51]\n"
"ds_read_b128 v[124:127], %[vTA0] offset:12288\n"
"v_mfma_f32_16x16x32_bf16 v[52:55], v[116:119], a[20:23], v[52:55]\n"
"buffer_load_dwordx4 %[vB3], %[srdB], s60 offen offset:3072 lds\n"
"v_mfma_f32_16x16x32_bf16 v[56:59], v[116:119], a[24:27], v[56:59]\n"
"v_mfma_f32_16x16x32_bf16 v[60:63], v[116:119], a[28:31], v[60:63]\n"
"s_waitcnt lgkmcnt(1)\n"
"v_mfma_f32_16x16x32_bf16 v[64:67], v[120:123], a[16:19], v[64:67]\n"
"ds_read_b128 v[128:131], %[vT80] offset:9216\n"
"v_mfma_f32_16x16x32_bf16 v[68:71], v[120:123], a[20:23], v[68:71]\n"
"s_waitcnt vmcnt(9)\n"
"s_barrier\n"
"ds_read_b128 a[0:3], %[vTBa00] offset:34816\n"
"v_mfma_f32_16x16x32_bf16 v[72:75], v[120:123], a[24:27], v[72:75]\n"
"ds_read_b128 a[4:7], %[vTBa00] offset:36864\n"
"v_mfma_f32_16x16x32_bf16 v[76:79], v[120:123], a[28:31], v[76:79]\n"
"ds_read_b128 a[8:11], %[vTBb00] offset:38912\n"
"s_waitcnt lgkmcnt(4)\n"
"v_mfma_f32_16x16x32_bf16 v[80:83], v[124:127], a[16:19], v[80:83]\n"
"ds_read_b128 a[12:15], %[vTBb00] offset:40960\n"
"v_mfma_f32_16x16x32_bf16 v[84:87], v[124:127], a[20:23], v[84:87]\n"
"ds_read_b128 v[112:115], %[vTA0] offset:34816\n"
"v_mfma_f32_16x16x32_bf16 v[88:91], v[124:127], a[24:27], v[88:91]\n"
"s_cmp_le_u32 %[lim], 15\n"
"s_cselect_b32 s61, %[soffAw], %[soffA]\n"
"s_add_u32 s61, s61, 276480\n"
"s_add_u32 m0, %[m0A], 104448\n"
"s_nop 0\n"
"buffer_load_dwordx4 %[vA], %[srdA], s61 offen offset:0 sc1 lds\n"
"v_mfma_f32_16x16x32_bf16 v[92:95], v[124:127], a[28:31], v[92:95]\n"
"buffer_load_dwordx4 %[vA], %[srdA], s61 offen offset:1024 sc1 lds\n"
"s_waitcnt lgkmcnt(5)\n"
"v_mfma_f32_16x16x32_bf16 v[96:99], v[128:131], a[16:19], v[96:99]\n"
"ds_read_b128 v[116:119], %[vTA0] offset:35840\n"
"v_mfma_f32_16x16x32_bf16 v[100:103], v[128:131], a[20:23], v[100:103]\n"
"s_waitcnt lgkmcnt(1)\n"
"v_mfma_f32_16x16x32_bf16 v[32:35], v[112:115], a[0:3], v[32:35]\n"
"ds_read_b128 v[120:123], %[vTA0] offset:36864\n"
"v_mfma_f32_16x16x32_bf16 v[36:39], v[112:115], a[4:7], v[36:39]\n"
"buffer_load_dwordx4 %[vA], %[srdA], s61 offen offset:2048 sc1 lds\n"
"v_mfma_f32_16x16x32_bf16 v[40:43], v[112:115], a[8:11], v[40:43]\n"
"ds_read_b128 a[16:19], %[vTBa10] offset:34816\n"
"v_mfma_f32_16x16x32_bf16 v[44:47], v[112:115], a[12:15], v[44:47]\n"
"ds_read_b128 a[20:23], %[vTBa10] offset:36864\n"
"s_waitcnt lgkmcnt(3)\n"
"v_mfma_f32_16x16x32_bf16 v[48:51], v[116:119], a[0:3], v[48:51]\n"
"ds_read_b128 a[24:27], %[vTBb10] offset:38912\n"
"v_mfma_f32_16x16x32_bf16 v[52:55], v[116:119], a[4:7], v[52:55]\n"
"ds_read_b128 a[28:31], %[vTBb10] offset:40960\n"
"v_mfma_f32_16x16x32_bf16 v[56:59], v[116:119], a[8:11], v[56:59]\n"
"ds_read_b128 v[124:127], %[vTA0] offset:37888\n"
"v_mfma_f32_16x16x32_bf16 v[60:63], v[116:119], a[12:15], v[60:63]\n"
"buffer_load_dwordx4 %[vA], %[srdA], s61 offen offset:3072 sc1 lds\n"
"s_waitcnt lgkmcnt(5)\n"
"v_mfma_f32_16x16x32_bf16 v[64:67], v[120:123], a[0:3], v[64:67]\n"
"ds_read_b128 v[128:131], %[vT80] offset:34816\n"
"v_mfma_f32_16x16x32_bf16 v[68:71], v[120:123], a[4:7], v[68:71]\n"
"s_add_u32 m0, %[m0A2], 104448\n"
"s_mov_b32 exec_hi, 0\n"
"buffer_load_dwordx4 %[vAh], %[srdA], s61 offen sc1 lds\n"
"s_mov_b32 exec_hi, -1\n"
"v_mfma_f32_16x16x32_bf16 v[72:75], v[120:123], a[8:11], v[72:75]\n"
"v_mfma_f32_16x16x32_bf16 v[76:79], v[120:123], a[12:15], v[76:79]\n"
"s_cmp_le_u32 %[lim], 15\n"
"s_cselect_b32 s60, %[soffBw], %[soffB]\n"
"s_add_u32 s60, s60, 1920\n"
"s_add_u32 m0, %[m0B], 104448\n"
"s_nop 0\n"
"buffer_load_dwordx4 %[vB0], %[srdB], s60 offen offset:0 lds\n"
"s_waitcnt lgkmcnt(1)\n"
"v_mfma_f32_16x16x32_bf16 v[80:83], v[124:127], a[0:3], v[80:83]\n"
"ds_read_b128 v[112:115], %[vTA0] offset:44032\n"
"v_mfma_f32_16x16x32_bf16 v[84:87], v[124:127], a[4:7], v[84:87]\n"
"v_mfma_f32_16x16x32_bf16 v[88:91], v[124:127], a[8:11], v[88:91]\n"
"v_mfma_f32_16x16x32_bf16 v[92:95], v[124:127], a[12:15], v[92:95]\n"
"buffer_load_dwordx4 %[vB1], %[srdB], s60 offen offset:1024 lds\n"
"s_waitcnt lgkmcnt(1)\n"
"v_mfma_f32_16x16x32_bf16 v[96:99], v[128:131], a[0:3], v[96:99]\n"
"ds_read_b128 v[116:119], %[vTA0] offset:45056\n"
"v_mfma_f32_16x16x32_bf16 v[100:103], v[128:131], a[4:7], v[100:103]\n"
"s_waitcnt lgkmcnt(1)\n"
"v_mfma_f32_16x16x32_bf16 v[32:35], v[112:115], a[16:19], v[32:35]\n"
"ds_read_b128 v[120:123], %[vTA0] offset:46080\n"
"v_mfma_f32_16x16x32_bf16 v[36:39], v[112:115], a[20:23], v[36:39]\n"
"buffer_load_dwordx4 %[vB2], %[srdB], s60 offen offset:2048 lds\n"
"v_mfma_f32_16x16x32_bf16 v[40:43], v[112:115], a[24:27], v[40:43]\n"
"v_mfma_f32_16x16x32_bf16 v[44:47], v[112:115], a[28:31], v[44:47]\n"
"s_waitcnt lgkmcnt(1)\n"
"v_mfma_f32_16x16x32_bf16 v[48:51], v[116:119], a[16:19], v[48:51]\n"
"ds_read_b128 v[124:127], %[vTA0] offset:47104\n"
"v_mfma_f32_16x16x32_bf16 v[52:55], v[116:119], a[20:23], v[52:55]\n"
"buffer_load_dwordx4 %[vB3], %[srdB], s60 offen offset:3072 lds\n"
"v_mfma_f32_16x16x32_bf16 v[56:59], v[116:119], a[24:27], v[56:59]\n"
"v_mfma_f32_16x16x32_bf16 v[60:63], v[116:119], a[28:31], v[60:63]\n"
"s_waitcnt lgkmcnt(1)\n"
"v_mfma_f32_16x16x32_bf16 v[64:67], v[120:123], a[16:19], v[64:67]\n"
"ds_read_b128 v[128:131], %[vT80] offset:44032\n"
"v_mfma_f32_16x16x32_bf16 v[68:71], v[120:123], a[20:23], v[68:71]\n"
"s_waitcnt vmcnt(9)\n"
"s_barrier\n"
"ds_read_b128 a[0:3], %[vTBa01] offset:0\n"
"v_mfma_f32_16x16x32_bf16 v[72:75], v[120:123], a[24:27], v[72:75]\n"
"ds_read_b128 a[4:7], %[vTBa01] offset:2048\n"
"v_mfma_f32_16x16x32_bf16 v[76:79], v[120:123], a[28:31], v[76:79]\n"
"ds_read_b128 a[8:11], %[vTBb01] offset:4096\n"
"s_waitcnt lgkmcnt(4)\n"
"v_mfma_f32_16x16x32_bf16 v[80:83], v[124:127], a[16:19], v[80:83]\n"
"ds_read_b128 a[12:15], %[vTBb01] offset:6144\n"
"v_mfma_f32_16x16x32_bf16 v[84:87], v[124:127], a[20:23], v[84:87]\n"
"ds_read_b128 v[112:115], %[vTA1] offset:0\n"
"v_mfma_f32_16x16x32_bf16 v[88:91], v[124:127], a[24:27], v[88:91]\n"
"v_mfma_f32_16x16x32_bf16 v[92:95], v[124:127], a[28:31], v[92:95]\n"
"s_waitcnt lgkmcnt(5)\n"
"v_mfma_f32_16x16x32_bf16 v[96:99], v[128:131], a[16:19], v[96:99]\n"
"ds_read_b128 v[116:119], %[vTA1] offset:1024\n"
"v_mfma_f32_16x16x32_bf16 v[100:103], v[128:131], a[20:23], v[100:103]\n"
"s_waitcnt lgkmcnt(1)\n"
"v_mfma_f32_16x16x32_bf16 v[32:35], v[112:115], a[0:3], v[32:35]\n"
"ds_read_b128 v[120:123], %[vTA1] offset:2048\n"
"v_mfma_f32_16x16x32_bf16 v[36:39], v[112:115], a[4:7], v[36:39]\n"
"v_mfma_f32_16x16x32_bf16 v[40:43], v[112:115], a[8:11], v[40:43]\n"
"ds_read_b128 a[16:19], %[vTBa11] offset:0\n"
"v_mfma_f32_16x16x32_bf16 v[44:47], v[112:115], a[12:15], v[44:47]\n"
"ds_read_b128 a[20:23], %[vTBa11] offset:2048\n"
"s_waitcnt lgkmcnt(3)\n"
"v_mfma_f32_16x16x32_bf16 v[48:51], v[116:119], a[0:3], v[48:51]\n"
"ds_read_b128 a[24:27], %[vTBb11] offset:4096\n"
"v_mfma_f32_16x16x32_bf16 v[52:55], v[116:119], a[4:7], v[52:55]\n"
"ds_read_b128 a[28:31], %[vTBb11] offset:6144\n"
"v_mfma_f32_16x16x32_bf16 v[56:59], v[116:119], a[8:11], v[56:59]\n"
"ds_read_b128 v[124:127], %[vTA1] offset:3072\n"
"v_mfma_f32_16x16x32_bf16 v[60:63], v[116:119], a[12:15], v[60:63]\n"
"s_waitcnt lgkmcnt(5)\n"
"v_mfma_f32_16x16x32_bf16 v[64:67], v[120:123], a[0:3], v[64:67]\n"
"ds_read_b128 v[128:131], %[vT81] offset:0\n"
"v_mfma_f32_16x16x32_bf16 v[68:71], v[120:123], a[4:7], v[68:71]\n"
"v_mfma_f32_16x16x32_bf16 v[72:75], v[120:123], a[8:11], v[72:75]\n"
"v_mfma_f32_16x16x32_bf16 v[76:79], v[120:123], a[12:15], v[76:79]\n"
"s_waitcnt lgkmcnt(1)\n"
"v_mfma_f32_16x16x32_bf16 v[80:83], v[124:127], a[0:3], v[80:83]\n"
"ds_read_b128 v[112:115], %[vTA1] offset:9216\n"
"v_mfma_f32_16x16x32_bf16 v[84:87], v[124:127], a[4:7], v[84:87]\n"
"v_mfma_f32_16x16x32_bf16 v[88:91], v[124:127], a[8:11], v[88:91]\n"
"v_mfma_f32_16x16x32_bf16 v[92:95], v[124:127], a[12:15], v[92:95]\n"
"s_waitcnt lgkmcnt(1)\n"
"v_mfma_f32_16x16x32_bf16 v[96:99], v[128:131], a[0:3], v[96:99]\n"
"ds_read_b128 v[116:119], %[vTA1] offset:10240\n"
"v_mfma_f32_16x16x32_bf16 v[100:103], v[128:131], a[4:7], v[100:103]\n"
"s_waitcnt lgkmcnt(1)\n"
"v_mfma_f32_16x16x32_bf16 v[32:35], v[112:115], a[16:19], v[32:35]\n"
"ds_read_b128 v[120:123], %[vTA1] offset:11264\n"
"v_mfma_f32_16x16x32_bf16 v[36:39], v[112:115], a[20:23], v[36:39]\n"
"v_mfma_f32_16x16x32_bf16 v[40:43], v[112:115], a[24:27], v[40:43]\n"
"v_mfma_f32_16x16x32_bf16 v[44:47], v[112:115], a[28:31], v[44:47]\n"
"s_waitcnt lgkmcnt(1)\n"
"v_mfma_f32_16x16x32_bf16 v[48:51], v[116:119], a[16:19], v[48:51]\n"
"ds_read_b128 v[124:127], %[vTA1] offset:12288\n"
"v_mfma_f32_16x16x32_bf16 v[52:55], v[116:119], a[20:23], v[52:55]\n"
"v_mfma_f32_16x16x32_bf16 v[56:59], v[116:119], a[24:27], v[56:59]\n"
"v_mfma_f32_16x16x32_bf16 v[60:63], v[116:119], a[28:31], v[60:63]\n"
"s_waitcnt lgkmcnt(1)\n"
"v_mfma_f32_16x16x32_bf16 v[64:67], v[120:123], a[16:19], v[64:67]\n"
"ds_read_b128 v[128:131], %[vT81] offset:9216\n"
"v_mfma_f32_16x16x32_bf16 v[68:71], v[120:123], a[20:23], v[68:71]\n"
"s_waitcnt vmcnt(0)\n"
"s_barrier\n"
"ds_read_b128 a[0:3], %[vTBa01] offset:34816\n"
"v_mfma_f32_16x16x32_bf16 v[72:75], v[120:123], a[24:27], v[72:75]\n"
"ds_read_b128 a[4:7], %[vTBa01] offset:36864\n"
"v_mfma_f32_16x16x32_bf16 v[76:79], v[120:123], a[28:31], v[76:79]\n"
"ds_read_b128 a[8:11], %[vTBb01] offset:38912\n"
"s_waitcnt lgkmcnt(4)\n"
"v_mfma_f32_16x16x32_bf16 v[80:83], v[124:127], a[16:19], v[80:83]\n"
"ds_read_b128 a[12:15], %[vTBb01] offset:40960\n"
"v_mfma_f32_16x16x32_bf16 v[84:87], v[124:127], a[20:23], v[84:87]\n"
"ds_read_b128 v[112:115], %[vTA1] offset:34816\n"
"v_mfma_f32_16x16x32_bf16 v[88:91], v[124:127], a[24:27], v[88:91]\n"
"v_mfma_f32_16x16x32_bf16 v[92:95], v[124:127], a[28:31], v[92:95]\n"
"s_waitcnt lgkmcnt(5)\n"
"v_mfma_f32_16x16x32_bf16 v[96:99], v[128:131], a[16:19], v[96:99]\n"
"ds_read_b128 v[116:119], %[vTA1] offset:35840\n"
"v_mfma_f32_16x16x32_bf16 v[100:103], v[128:131], a[20:23], v[100:103]\n"
"s_waitcnt lgkmcnt(1)\n"
"v_mfma_f32_16x16x32_bf16 v[32:35], v[112:115], a[0:3], v[32:35]\n"
"ds_read_b128 v[120:123], %[vTA1] offset:36864\n"
"v_mfma_f32_16x16x32_bf16 v[36:39], v[112:115], a[4:7], v[36:39]\n"
"v_mfma_f32_16x16x32_bf16 v[40:43], v[112:115], a[8:11], v[40:43]\n"
"ds_read_b128 a[16:19], %[vTBa11] offset:34816\n"
"v_mfma_f32_16x16x32_bf16 v[44:47], v[112:115], a[12:15], v[44:47]\n"
"ds_read_b128 a[20:23], %[vTBa11] offset:36864\n"
"s_waitcnt lgkmcnt(3)\n"
"v_mfma_f32_16x16x32_bf16 v[48:51], v[116:119], a[0:3], v[48:51]\n"
"ds_read_b128 a[24:27], %[vTBb11] offset:38912\n"
"v_mfma_f32_16x16x32_bf16 v[52:55], v[116:119], a[4:7], v[52:55]\n"
"ds_read_b128 a[28:31], %[vTBb11] offset:40960\n"
"v_mfma_f32_16x16x32_bf16 v[56:59], v[116:119], a[8:11], v[56:59]\n"
"ds_read_b128 v[124:127], %[vTA1] offset:37888\n"
"v_mfma_f32_16x16x32_bf16 v[60:63], v[116:119], a[12:15], v[60:63]\n"
"s_waitcnt lgkmcnt(5)\n"
"v_mfma_f32_16x16x32_bf16 v[64:67], v[120:123], a[0:3], v[64:67]\n"
"ds_read_b128 v[128:131], %[vT81] offset:34816\n"
"v_mfma_f32_16x16x32_bf16 v[68:71], v[120:123], a[4:7], v[68:71]\n"
"v_mfma_f32_16x16x32_bf16 v[72:75], v[120:123], a[8:11], v[72:75]\n"
"v_mfma_f32_16x16x32_bf16 v[76:79], v[120:123], a[12:15], v[76:79]\n"
"s_waitcnt lgkmcnt(1)\n"
"v_mfma_f32_16x16x32_bf16 v[80:83], v[124:127], a[0:3], v[80:83]\n"
"ds_read_b128 v[112:115], %[vTA1] offset:44032\n"
"v_mfma_f32_16x16x32_bf16 v[84:87], v[124:127], a[4:7], v[84:87]\n"
"v_mfma_f32_16x16x32_bf16 v[88:91], v[124:127], a[8:11], v[88:91]\n"
"v_mfma_f32_16x16x32_bf16 v[92:95], v[124:127], a[12:15], v[92:95]\n"
"s_waitcnt lgkmcnt(1)\n"
"v_mfma_f32_16x16x32_bf16 v[96:99], v[128:131], a[0:3], v[96:99]\n"
"ds_read_b128 v[116:119], %[vTA1] offset:45056\n"
"v_mfma_f32_16x16x32_bf16 v[100:103], v[128:131], a[4:7], v[100:103]\n"
"s_waitcnt lgkmcnt(1)\n"
"v_mfma_f32_16x16x32_bf16 v[32:35], v[112:115], a[16:19], v[32:35]\n"
"ds_read_b128 v[120:123], %[vTA1] offset:46080\n"
"v_mfma_f32_16x16x32_bf16 v[36:39], v[112:115], a[20:23], v[36:39]\n"
"v_mfma_f32_16x16x32_bf16 v[40:43], v[112:115], a[24:27], v[40:43]\n"
"v_mfma_f32_16x16x32_bf16 v[44:47], v[112:115], a[28:31], v[44:47]\n"
"s_waitcnt lgkmcnt(1)\n"
"v_mfma_f32_16x16x32_bf16 v[48:51], v[116:119], a[16:19], v[48:51]\n"
"ds_read_b128 v[124:127], %[vTA1] offset:47104\n"
"v_mfma_f32_16x16x32_bf16 v[52:55], v[116:119], a[20:23], v[52:55]\n"
"v_mfma_f32_16x16x32_bf16 v[56:59], v[116:119], a[24:27], v[56:59]\n"
"v_mfma_f32_16x16x32_bf16 v[60:63], v[116:119], a[28:31], v[60:63]\n"
"s_waitcnt lgkmcnt(1)\n"
"v_mfma_f32_16x16x32_bf16 v[64:67], v[120:123], a[16:19], v[64:67]\n"
"ds_read_b128 v[128:131], %[vT81] offset:44032\n"
"v_mfma_f32_16x16x32_bf16 v[68:71], v[120:123], a[20:23], v[68:71]\n"
"v_mfma_f32_16x16x32_bf16 v[72:75], v[120:123], a[24:27], v[72:75]\n"
"v_mfma_f32_16x16x32_bf16 v[76:79], v[120:123], a[28:31], v[76:79]\n"
"s_waitcnt lgkmcnt(1)\n"
"v_mfma_f32_16x16x32_bf16 v[80:83], v[124:127], a[16:19], v[80:83]\n"
"v_cvt_pk_bf16_f32 v144, v32, v33\n"
"v_cvt_pk_bf16_f32 v145, v34, v35\n"
"v_mfma_f32_16x16x32_bf16 v[84:87], v[124:127], a[20:23], v[84:87]\n"
"v_cvt_pk_bf16_f32 v146, v48, v49\n"
"v_cvt_pk_bf16_f32 v147, v50, v51\n"
"v_mfma_f32_16x16x32_bf16 v[88:91], v[124:127], a[24:27], v[88:91]\n"
"s_nop 1\n"
"v_permlane16_swap_b32 v144, v146\n"
"v_permlane16_swap_b32 v145, v147\n"
"s_nop 1\n"
"s_add_u32 s60, %[soffP], 0\n"
"buffer_store_dwordx4 v[144:147], %[vP], %[srdP], s60 offen sc1\n"
"v_mfma_f32_16x16x32_bf16 v[92:95], v[124:127], a[28:31], v[92:95]\n"
"v_cvt_pk_bf16_f32 v148, v36, v37\n"
"v_cvt_pk_bf16_f32 v149, v38, v39\n"
"s_waitcnt lgkmcnt(0)\n"
"v_mfma_f32_16x16x32_bf16 v[96:99], v[128:131], a[16:19], v[96:99]\n"
"v_cvt_pk_bf16_f32 v150, v52, v53\n"
"v_cvt_pk_bf16_f32 v151, v54, v55\n"
"v_mfma_f32_16x16x32_bf16 v[100:103], v[128:131], a[20:23], v[100:103]\n"
"s_nop 1\n"
"v_permlane16_swap_b32 v148, v150\n"
"v_permlane16_swap_b32 v149, v151\n"
"s_nop 1\n"
"s_add_u32 s60, %[soffP], 2048\n"
"buffer_store_dwordx4 v[148:151], %[vP], %[srdP], s60 offen sc1\n"
"v_cvt_pk_bf16_f32 v152, v40, v41\n"
"v_cvt_pk_bf16_f32 v153, v42, v43\n"
"v_cvt_pk_bf16_f32 v154, v56, v57\n"
"v_cvt_pk_bf16_f32 v155, v58, v59\n"
"s_nop 1\n"
"v_permlane16_swap_b32 v152, v154\n"
"v_permlane16_swap_b32 v153, v155\n"
"s_nop 1\n"
"s_add_u32 s60, %[soffP], 4096\n"
"buffer_store_dwordx4 v[152:155], %[vP], %[srdP], s60 offen sc1\n"
"v_cvt_pk_bf16_f32 v156, v44, v45\n"
"v_cvt_pk_bf16_f32 v157, v46, v47\n"
"v_cvt_pk_bf16_f32 v158, v60, v61\n"
"v_cvt_pk_bf16_f32 v159, v62, v63\n"
"s_nop 1\n"
"v_permlane16_swap_b32 v156, v158\n"
"v_permlane16_swap_b32 v157, v159\n"
"s_nop 1\n"
"s_add_u32 s60, %[soffP], 6144\n"
"buffer_store_dwordx4 v[156:159], %[vP], %[srdP], s60 offen sc1\n"
"s_nop 7\n"
"v_cvt_pk_bf16_f32 v160, v64, v65\n"
"v_cvt_pk_bf16_f32 v161, v66, v67\n"
"v_cvt_pk_bf16_f32 v162, v80, v81\n"
"v_cvt_pk_bf16_f32 v163, v82, v83\n"
"s_nop 1\n"
"v_permlane16_swap_b32 v160, v162\n"
"v_permlane16_swap_b32 v161, v163\n"
"s_nop 1\n"
"s_add_u32 s60, %[soffP], 1024\n"
"buffer_store_dwordx4 v[160:163], %[vP], %[srdP], s60 offen sc1\n"
"v_cvt_pk_bf16_f32 v164, v68, v69\n"
"v_cvt_pk_bf16_f32 v165, v70, v71\n"
"v_cvt_pk_bf16_f32 v166, v84, v85\n"
"v_cvt_pk_bf16_f32 v167, v86, v87\n"
"s_nop 1\n"
"v_permlane16_swap_b32 v164, v166\n"
"v_permlane16_swap_b32 v165, v167\n"
"s_nop 1\n"
"s_add_u32 s60, %[soffP], 3072\n"
"buffer_store_dwordx4 v[164:167], %[vP], %[srdP], s60 offen sc1\n"
"v_cvt_pk_bf16_f32 v168, v72, v73\n"
"v_cvt_pk_bf16_f32 v169, v74, v75\n"
"v_cvt_pk_bf16_f32 v170, v88, v89\n"
"v_cvt_pk_bf16_f32 v171, v90, v91\n"
"s_nop 1\n"
"v_permlane16_swap_b32 v168, v170\n"
"v_permlane16_swap_b32 v169, v171\n"
"s_nop 1\n"
"s_add_u32 s60, %[soffP], 5120\n"
"buffer_store_dwordx4 v[168:171], %[vP], %[srdP], s60 offen sc1\n"
"v_cvt_pk_bf16_f32 v172, v76, v77\n"
"v_cvt_pk_bf16_f32 v173, v78, v79\n"
"v_cvt_pk_bf16_f32 v174, v92, v93\n"
"v_cvt_pk_bf16_f32 v175, v94, v95\n"
"s_nop 1\n"
"v_permlane16_swap_b32 v172, v174\n"
"v_permlane16_swap_b32 v173, v175\n"
"s_nop 1\n"
"s_add_u32 s60, %[soffP], 7168\n"
"buffer_store_dwordx4 v[172:175], %[vP], %[srdP], s60 offen sc1\n"
"v_cvt_pk_bf16_f32 v144, v96, v97\n"
"v_cvt_pk_bf16_f32 v145, v98, v99\n"
"v_cvt_pk_bf16_f32 v146, v100, v101\n"
"v_cvt_pk_bf16_f32 v147, v102, v103\n"
"s_nop 1\n"
"v_permlane16_swap_b32 v144, v146\n"
"v_permlane16_swap_b32 v145, v147\n"
"s_nop 1\n"
"s_add_u32 s60, %[soffP], 8192\n"
"buffer_store_dwordx4 v[144:147], %[vP], %[srdP], s60 offen sc1\n"
    :: [srdA]"s"(srdA), [srdB]"s"(srdB), [srdP]"s"(srdP), [soffA]"s"(soffA), [m0A]"s"(m0A), [m0A2]"s"(m0A2), [m0B]"s"(m0B), [soffB]"s"(soffB), [soffAw]"s"(soffAw), [soffBw]"s"(soffBw), [lim]"s"(lim), [soffP]"s"(soffP),
       [vA]"v"(vA), [vAh]"v"(vAh), [vB0]"v"(vB0), [vB1]"v"(vB1), [vB2]"v"(vB2), [vB3]"v"(vB3),
       [vTA0]"v"(vTA0), [vTA1]"v"(vTA1), [vT80]"v"(vT80), [vT81]"v"(vT81),
       [vTBa00]"v"(vTBa00), [vTBa10]"v"(vTBa10), [vTBb00]"v"(vTBb00), [vTBb10]"v"(vTBb10),
       [vTBa01]"v"(vTBa01), [vTBa11]"v"(vTBa11), [vTBb01]"v"(vTBb01), [vTBb11]"v"(vTBb11), [vP]"v"(vP)
    : "memory", "s60", "s61", "s62", "s63",  "scc", "v32","v33","v34","v35","v36","v37","v38","v39","v40","v41","v42","v43","v44","v45","v46","v47","v48","v49","v50","v51","v52","v53","v54","v55","v56","v57","v58","v59","v60","v61","v62","v63","v64","v65","v66","v67","v68","v69","v70","v71","v72","v73","v74","v75","v76","v77","v78","v79","v80","v81","v82","v83","v84","v85","v86","v87","v88","v89","v90","v91","v92","v93","v94","v95","v96","v97","v98","v99","v100","v101","v102","v103","v104","v105","v106","v107","v108","v109","v110","v111","v112","v113","v114","v115","v116","v117","v118","v119","v120","v121","v122","v123","v124","v125","v126","v127","v128","v129","v130","v131","v132","v133","v134","v135","v136","v137","v138","v139","v140","v141","v142","v143","v144","v145","v146","v147","v148","v149","v150","v151","v152","v153","v154","v155","v156","v157","v158","v159","v160","v161","v162","v163","v164","v165","v166","v167","v168","v169","v170","v171","v172","v173","v174","v175", "a0","a1","a2","a3","a4","a5","a6","a7","a8","a9","a10","a11","a12","a13","a14","a15","a16","a17","a18","a19","a20","a21","a22","a23","a24","a25","a26","a27","a28","a29","a30","a31");
  asm volatile("kc2e:");
  CODE_LABEL("kc3b");
  {
    unsigned b_ = b, w_ = w, lt = lane_id();
    unsigned ep1;
    asm volatile("ds_read_b32 %0, %1\n s_waitcnt lgkmcnt(0)" : "=&v"(ep1) : "v"((unsigned)(size_t)smem + 147456u) : "memory");
    asm volatile("" : "+s"(b_), "+s"(w_), "+v"(lt));
    const unsigned w = w_, rt = 8u * (b_ & 1u) + ((b_ >> 3) & 7u), kq = (b_ & 7u) >> 1, ct = b_ >> 6;
    const unsigned gi = ct * 4u + kq, blk = rt * 16u + gi;
    const unsigned long long pb = ((unsigned long long)ka[7] << 32) | ka[6];
    const unsigned long long hb = pb + (9u << 20), nb = pb + (12u << 20), fb = pb + 0xB84000u;
    const u32x4 srdH = srd_of((unsigned)hb, (unsigned)(hb >> 32), 2048u * 576u * 2u);
    const u32x4 srdN = srd_of((unsigned)nb, (unsigned)(nb >> 32), 2048u * 576u * 2u);
    const u32x4 srdF = srd_of((unsigned)fb, (unsigned)(fb >> 32), 256u * 64u);
    const u32x4 srdQ = srd_of(ka[6], ka[7], 4u * 2048u * 576u * 2u);
    const u32x4 srdX = srd_of(kc[2], kc[3], 2048u * 576u * 2u);
    const unsigned vX = (lt & 7u) * 1152u + (lt >> 3) * 16u, sX = (128u * rt + 8u * gi) * 1152u + 384u * w;
    u32x4 h0, h1, h2;
    asm volatile("buffer_load_dwordx4 %0, %1, %2, %3 offen" : "=&v"(h0) : "v"(vX), "s"(srdX), "s"(sX) : "memory");
    asm volatile("buffer_load_dwordx4 %0, %1, %2, %3 offen offset:128" : "=&v"(h1) : "v"(vX), "s"(srdX), "s"(sX) : "memory");
    asm volatile("buffer_load_dwordx4 %0, %1, %2, %3 offen offset:256" : "=&v"(h2) : "v"(vX), "s"(srdX), "s"(sX) : "memory");
        const unsigned vN = ((lt >> 5) * 64u + 16u * ((lt >> 3) & 3u) + (lt & 7u)) * 16u;
    const unsigned sN = (((8u * rt + (gi >> 1)) * 18u + 6u * w) * 64u + 8u * (gi & 1u)) * 16u, sN2 = sN + 4096u;
    const unsigned rb = (gi >> 1) & 3u, rgr = gi >> 3, n8 = 8u * (gi & 1u);
    const unsigned vPa = ((lt >> 5) * 64u + (lt & 7u) + 16u * ((lt >> 4) & 1u) + 32u * ((lt >> 3) & 1u)) * 16u;
    const unsigned vPb = (lt >> 4) * 36864u + ((lt & 7u) + 32u * ((lt >> 3) & 1u)) * 16u;
    unsigned vQ[3], sQ[12];
#pragma unroll
    for (unsigned j = 0; j < 3; ++j) {
      const unsigned s = 3u * w + j;
      const unsigned sm = (((rt * 4u + (s >> 1)) * 4u + rgr + 2u * (s & 1u)) * 9u + 2u * ((rb + 2u * (s & 1u)) & 3u)) * 1024u + n8 * 16u;
      const unsigned s8 = ((rt * 16u + rgr + 2u * (rb >> 1)) * 9u + 8u) * 1024u + (n8 + 16u * (rb & 1u)) * 16u;
      vQ[j] = s < 8u ? vPa : vPb;
#pragma unroll
      for (unsigned k = 0; k < 4; ++k) sQ[4 * j + k] = (s < 8u ? sm : s8) + k * 2359296u;
    }
    const unsigned vFs = lt == 0u ? blk * 64u : 0x7fffffffu, vFp = (rt * 16u + (lt & 15u)) * 64u;
    const unsigned tw = lds + w * 64u + (lt & 7u) * 8u, tr = lds + (lt & 7u) * 8u;
    asm volatile("s_waitcnt vmcnt(0)\n s_barrier" : "+v"(h0), "+v"(h1), "+v"(h2) : "v"(vPa), "v"(vQ[2]), "v"(vFs), "v"(vFp), "v"(tw), "v"(tr), "v"(vN),
"s"(sQ[0]), "s"(sQ[1]), "s"(sQ[2]), "s"(sQ[3]), "s"(sQ[4]), "s"(sQ[5]), "s"(sQ[6]), "s"(sQ[7]), "s"(sQ[8]), "s"(sQ[9]), "s"(sQ[10]), "s"(sQ[11]), "s"(sN), "s"(sN2), "s"(srdF), "s"(srdH), "s"(srdN) : "memory");
    if (w == 3u) {
      unsigned f0, f1, f2, cnt; unsigned long long fm;
      asm volatile("buffer_store_dword %[one], %[vS], %[sd], 0 offen sc1\n"
"buffer_load_dword %[f0], %[vP], %[sd], 0 offen sc1\n s_sleep 3\n"
"buffer_load_dword %[f1], %[vP], %[sd], 0 offen sc1\n s_sleep 3\n"
"buffer_load_dword %[f2], %[vP], %[sd], 0 offen sc1\n"
"s_movk_i32 %[cnt], 0x1000\n"
"Lpoll%=:\n"
"s_waitcnt vmcnt(2)\n v_cmp_ne_u32 %[fm], %[one], %[f0]\n s_cmp_eq_u64 %[fm], 0\n s_cbranch_scc1 Ldone%=\n"
"buffer_load_dword %[f0], %[vP], %[sd], 0 offen sc1\n"
"s_waitcnt vmcnt(2)\n v_cmp_ne_u32 %[fm], %[one], %[f1]\n s_cmp_eq_u64 %[fm], 0\n s_cbranch_scc1 Ldone%=\n"
"buffer_load_dword %[f1], %[vP], %[sd], 0 offen sc1\n"
"s_waitcnt vmcnt(2)\n v_cmp_ne_u32 %[fm], %[one], %[f2]\n s_cmp_eq_u64 %[fm], 0\n s_cbranch_scc1 Ldone%=\n"
"buffer_load_dword %[f2], %[vP], %[sd], 0 offen sc1\n"
"s_sub_u32 %[cnt], %[cnt], 1\n s_cbranch_scc0 Lpoll%=\n"
"Ldone%=:"
                   : [f0]"=&v"(f0), [f1]"=&v"(f1), [f2]"=&v"(f2), [cnt]"=&s"(cnt), [fm]"=&s"(fm)
                   : [one]"v"(ep1), [vS]"v"(vFs), [vP]"v"(vFp), [sd]"s"(srdF) : "memory", "scc");
    }
    asm volatile("s_barrier" ::: "memory");
    if (w < 3u) {
      u32x4 p[3][4];
#pragma unroll
      for (unsigned j = 0; j < 3; ++j)
#pragma unroll
        for (unsigned k = 0; k < 4; ++k)
          asm volatile("buffer_load_dwordx4 %0, %1, %2, %3 offen sc1" : "=&v"(p[j][k]) : "v"(vQ[j]), "s"(srdQ), "s"(sQ[4 * j + k]) : "memory");
      float h[3][8];
      u32x4 oh[3];
      float sm = 0.f;
#pragma unroll
      for (unsigned j = 0; j < 3; ++j) {
        if (j == 0) asm volatile("s_waitcnt vmcnt(8)" : "+v"(p[0][0]), "+v"(p[0][1]), "+v"(p[0][2]), "+v"(p[0][3]) :: "memory");
        if (j == 1) asm volatile("s_waitcnt vmcnt(4)" : "+v"(p[1][0]), "+v"(p[1][1]), "+v"(p[1][2]), "+v"(p[1][3]) :: "memory");
        if (j == 2) asm volatile("s_waitcnt vmcnt(0)" : "+v"(p[2][0]), "+v"(p[2][1]), "+v"(p[2][2]), "+v"(p[2][3]) :: "memory");
        if (j == 2) { }
        const u32x4 hh = j == 0 ? h0 : (j == 1 ? h1 : h2);
#pragma unroll
        for (int i = 0; i < 4; ++i) {
          h[j][2 * i] = bf_lo(hh[i]) + ((bf_lo(p[j][0][i]) + bf_lo(p[j][1][i])) + (bf_lo(p[j][2][i]) + bf_lo(p[j][3][i])));
          h[j][2 * i + 1] = bf_hi(hh[i]) + ((bf_hi(p[j][0][i]) + bf_hi(p[j][1][i])) + (bf_hi(p[j][2][i]) + bf_hi(p[j][3][i])));
          sm += h[j][2 * i] + h[j][2 * i + 1];
          oh[j][i] = pk_bf16(h[j][2 * i], h[j][2 * i + 1]);
        }
        __builtin_amdgcn_sched_barrier(0);
      }
      asm volatile("buffer_store_dwordx4 %0, %1, %2, %3 offen sc1\n s_nop 1" :: "v"(oh[0]), "v"(vQ[0]), "s"(srdH), "s"(sQ[0]) : "memory");
      asm volatile("buffer_store_dwordx4 %0, %1, %2, %3 offen sc1\n s_nop 1" :: "v"(oh[1]), "v"(vQ[1]), "s"(srdH), "s"(sQ[4]) : "memory");
      asm volatile("buffer_store_dwordx4 %0, %1, %2, %3 offen sc1\n s_nop 1" :: "v"(oh[2]), "v"(vQ[2]), "s"(srdH), "s"(sQ[8]) : "memory");
      const float mw = sum_rows8(sm) * (1.0f / 192.0f);
      float qs = 0.f;
#pragma unroll
      for (unsigned j = 0; j < 3; ++j)
#pragma unroll
        for (int i = 0; i < 8; ++i) { h[j][i] -= mw; qs += h[j][i] * h[j][i]; }
      const f32x2 mq = {mw, sum_rows8(qs)};
      f32x2 a0, a1, a2;
      asm volatile("ds_write_b64 %3, %4\n s_waitcnt lgkmcnt(0)\n s_barrier\n ds_read_b64 %0, %5\n ds_read_b64 %1, %5 offset:64\n ds_read_b64 %2, %5 offset:128\n s_waitcnt lgkmcnt(0)"
                   : "=&v"(a0), "=&v"(a1), "=&v"(a2) : "v"(tw), "v"(mq), "v"(tr) : "memory");
      const float m = (a0[0] + a1[0] + a2[0]) * (1.0f / 3.0f);
      const float d0 = a0[0] - m, d1 = a1[0] - m, d2 = a2[0] - m;
      const float var = (a0[1] + a1[1] + a2[1] + 192.0f * (d0 * d0 + d1 * d1 + d2 * d2)) * (1.0f / 576.0f);
      const float rs = __builtin_amdgcn_rsqf(var + 1e-5f), dm = mw - m;
#pragma unroll
      for (unsigned j = 0; j < 3; ++j) {
        u32x4 o;
#pragma unroll
        for (int i = 0; i < 4; ++i) o[i] = pk_bf16((h[j][2 * i] + dm) * rs, (h[j][2 * i + 1] + dm) * rs);
        if (j == 0) asm volatile("buffer_store_dwordx4 %0, %1, %2, %3 offen sc1\n s_nop 1" :: "v"(o), "v"(vN), "s"(srdN), "s"(sN) : "memory");
        if (j == 1) asm volatile("buffer_store_dwordx4 %0, %1, %2, %3 offen offset:2048 sc1\n s_nop 1" :: "v"(o), "v"(vN), "s"(srdN), "s"(sN) : "memory");
        if (j == 2) asm volatile("buffer_store_dwordx4 %0, %1, %2, %3 offen sc1\n s_nop 1" :: "v"(o), "v"(vN), "s"(srdN), "s"(sN2) : "memory");
      }
    }
  }
  asm volatile("kc3e:");
  }
}


__global__ __launch_bounds__(256) void k45_ffn(const u16* __restrict__ hnF, const u16* __restrict__ wg, const u16* __restrict__ wu, u16* __restrict__ actF, const u16* __restrict__ wd, u16* __restrict__ part, u16* __restrict__ out) {
  __shared__ __attribute__((aligned(16))) char smem[147456];
  CODE_LABEL("kc4b");
  KARG_LOAD8(ka, 0x0); KARG_LOAD4(kb, 0x20);
  __builtin_amdgcn_sched_barrier(0);
  unsigned l = threadIdx.x & 63;
  const unsigned w = __builtin_amdgcn_readfirstlane(threadIdx.x >> 6);
  const unsigned b = blockIdx.x, xcd = b & 7, bi = b >> 3;
  const unsigned rt = 4 * (xcd & 1) + (bi & 3), kq = xcd >> 1, fi = bi >> 2, fs = 8 * kq + fi;
  const unsigned lds = (unsigned)(size_t)smem;
  const unsigned soffW = 8u * w * 9216u + 256u * fs, m0W = lds + w * 2048u;
  const unsigned r8 = l >> 3, fw = ((r8 >> 1) & 1) | ((w & 1) << 1);
  const unsigned vW = __umul24(r8, 9216u) + 16u * ((l & 7) ^ (fw << 1));
  const unsigned rb0 = 16 * rt + 4 * w;
  const unsigned vH = l * 16u, soffH = rb0 * 18432u;
  asm volatile("s_waitcnt lgkmcnt(0)" : "+s"(ka), "+s"(kb) : "v"(vW), "v"(vH), "s"(soffW), "s"(m0W), "s"(soffH));
  u32x4 srdG = srd_of(ka[2], ka[3], 576u * 4608u * 2u);
  const unsigned long long wu1 = (((unsigned long long)ka[5] << 32) | ka[4]) - 1024u;
  u32x4 srdU = srd_of((unsigned)wu1, (unsigned)(wu1 >> 32), 576u * 4608u * 2u + 1024u);
  u32x4 srdH = srd_of(ka[0], ka[1], 2048u * 576u * 2u);
  asm volatile(
"s_add_u32 m0, %[m0W], 0\n"
"s_add_u32 s60, %[soffW], 0\n"
"buffer_load_dwordx4 %[vW], %[srdG], s60 offen lds\n"
"buffer_load_dwordx4 %[vW], %[srdU], s60 offen offset:1024 lds\n"
"s_add_u32 s63, %[soffH], 0\n"
"buffer_load_dwordx4 a[0:3], %[vH], %[srdH], s63 offen\n"
"s_add_u32 s63, %[soffH], 18432\n"
"buffer_load_dwordx4 a[4:7], %[vH], %[srdH], s63 offen\n"
"s_add_u32 s63, %[soffH], 36864\n"
"buffer_load_dwordx4 a[8:11], %[vH], %[srdH], s63 offen\n"
"s_add_u32 s63, %[soffH], 55296\n"
"buffer_load_dwordx4 a[12:15], %[vH], %[srdH], s63 offen\n"
"s_add_u32 m0, %[m0W], 8192\n"
"s_add_u32 s60, %[soffW], 294912\n"
"buffer_load_dwordx4 %[vW], %[srdG], s60 offen lds\n"
"buffer_load_dwordx4 %[vW], %[srdU], s60 offen offset:1024 lds\n"
"s_add_u32 s63, %[soffH], 1024\n"
"buffer_load_dwordx4 a[16:19], %[vH], %[srdH], s63 offen\n"
"s_add_u32 s63, %[soffH], 19456\n"
"buffer_load_dwordx4 a[20:23], %[vH], %[srdH], s63 offen\n"
"s_add_u32 s63, %[soffH], 37888\n"
"buffer_load_dwordx4 a[24:27], %[vH], %[srdH], s63 offen\n"
"s_add_u32 s63, %[soffH], 56320\n"
"buffer_load_dwordx4 a[28:31], %[vH], %[srdH], s63 offen\n"
"s_add_u32 m0, %[m0W], 16384\n"
"s_add_u32 s60, %[soffW], 589824\n"
"buffer_load_dwordx4 %[vW], %[srdG], s60 offen lds\n"
"buffer_load_dwordx4 %[vW], %[srdU], s60 offen offset:1024 lds\n"
"s_add_u32 s63, %[soffH], 2048\n"
"buffer_load_dwordx4 a[32:35], %[vH], %[srdH], s63 offen\n"
"s_add_u32 s63, %[soffH], 20480\n"
"buffer_load_dwordx4 a[36:39], %[vH], %[srdH], s63 offen\n"
"s_add_u32 s63, %[soffH], 38912\n"
"buffer_load_dwordx4 a[40:43], %[vH], %[srdH], s63 offen\n"
"s_add_u32 s63, %[soffH], 57344\n"
"buffer_load_dwordx4 a[44:47], %[vH], %[srdH], s63 offen\n"
    : "+v"(l) : [m0W]"s"(m0W), [soffW]"s"(soffW), [vW]"v"(vW), [srdG]"s"(srdG), [srdU]"s"(srdU), [soffH]"s"(soffH), [vH]"v"(vH), [srdH]"s"(srdH)
    : "memory", "scc", "s60", "s61", "s62", "s63", "a0","a1","a2","a3","a4","a5","a6","a7","a8","a9","a10","a11","a12","a13","a14","a15","a16","a17","a18","a19","a20","a21","a22","a23","a24","a25","a26","a27","a28","a29","a30","a31","a32","a33","a34","a35","a36","a37","a38","a39","a40","a41","a42","a43","a44","a45","a46","a47");
  const unsigned long long rm = (((unsigned long long)ka[7] << 32) | ka[6]) + (18u << 20);
  u32x4 srdR = srd_of((unsigned)rm, (unsigned)(rm >> 32), 1152u * 1024u);
  u32x4 srdA = srd_of(ka[6], ka[7], 2048u * 4608u * 2u);
  const unsigned soffR = (fs * 36u + w) * 1024u, m0R = lds + w * 1024u;
  const unsigned vR = l * 16u;
  const unsigned g = l >> 4, n = l & 15, q = n >> 2, p = l & 3, sw = (q >> 1) | ((g & 1) << 1);
  const unsigned tf = lds + g * 2048u + q * 128u + 8u * p;
  const unsigned vT0 = tf + 32u * (0 ^ sw), vT1 = tf + 32u * (1 ^ sw), vT2 = tf + 32u * (2 ^ sw), vT3 = tf + 32u * (3 ^ sw);
  const unsigned vS0 = lds + (8 * g + 4 * (g & 1) + q) * 32u + 8u * p, vS1 = lds + (8 * g + 4 * ((g & 1) ^ 1) + q) * 32u + 8u * p;
  const unsigned vS = (g & 1) * (144u * 1024u) + (n + 16u * (g >> 1)) * 16u;
  const unsigned soffA = (rb0 * 144u + 36u * kq + 4u * fi) * 1024u;
  const unsigned soffAR = (rb0 * 144u + 36u * kq + 32u + (fi >> 1)) * 1024u + (fi & 1) * 512u;
  const unsigned long long fb4 = (((unsigned long long)kb[3] << 32) | kb[2]) + 0xB84000u;
  const u32x4 srdF4 = srd_of((unsigned)fb4, (unsigned)(fb4 >> 32), 3u * 16384u);
  const unsigned vFA = (w == 3u && l == 0u) ? 0x8000u + b * 64u : 0x7fffffffu, vFAp = 0x8000u + (xcd + 8u * (((bi & 7u) >> 1) + 4u * (l & 7u))) * 64u;
  const unsigned rt5 = 8u * (xcd & 1u) + (bi & 7u), ct5 = bi >> 3;
  const unsigned soffA5 = (1024u * kq + 16u * w) * 1152u, soffB5 = 36u * kq * 1024u, soffRm5 = (kq * 4u + ct5) * 36864u + w * 512u;
  const unsigned m0A5 = lds + (w >> 1) * 9216u + (w & 1u) * 4096u, m0A25 = lds + (w >> 1) * 9216u + 8192u + (w & 1u) * 512u, m0B5 = lds + 18432u + w * 4096u;
  const unsigned sB05 = (8u * rt5 + 2u * w) * 147456u, sC5 = 256u * ct5;
  const unsigned long long rm5 = (((unsigned long long)ka[7] << 32) | ka[6]) + (18u << 20) + 1152u * 1024u;
  const u32x4 srdRm5 = srd_of((unsigned)rm5, (unsigned)(rm5 >> 32), 576u * 1024u);
  const u32x4 srdA5 = srd_of(kb[0], kb[1], 4608u * 576u * 2u), srdB5 = srdA;
  u32x4 srdPF = srd_of(kb[0], kb[1], 4608u * 576u * 2u);
  const unsigned vPF = (l < 41u && w * 41u + l < 162u) ? (b * 162u + w * 41u + l) * 128u : 0x7fffffffu;
  CODE_SRD(srdC, "kc5b", "kc6e");
  const unsigned vC = (l * 32u + bi) * 128u;

  asm volatile(
"v_mov_b32 v224, %[vT0]\n"
"v_mov_b32 v225, %[vT1]\n"
"v_mov_b32 v226, %[vT2]\n"
"v_mov_b32 v227, %[vT3]\n"
"s_mov_b32 s61, 0xbfb8aa3b\n"
"s_waitcnt vmcnt(12)\n"
"s_barrier\n"
"ds_read_b64_tr_b16 v[208:209], v224 offset:0\n"
"ds_read_b64_tr_b16 v[210:211], v224 offset:512\n"
"ds_read_b64_tr_b16 v[212:213], v224 offset:1024\n"
"ds_read_b64_tr_b16 v[214:215], v224 offset:1536\n"
"s_waitcnt lgkmcnt(2)\n"
"v_mfma_f32_16x16x32_bf16 v[48:51], v[208:211], a[0:3], 0\n"
"ds_read_b64_tr_b16 v[216:217], v225 offset:0\n"
"v_mfma_f32_16x16x32_bf16 v[52:55], v[208:211], a[4:7], 0\n"
"ds_read_b64_tr_b16 v[218:219], v225 offset:512\n"
"v_mfma_f32_16x16x32_bf16 v[56:59], v[208:211], a[8:11], 0\n"
"s_add_u32 m0, %[m0W], 24576\n"
"s_add_u32 s60, %[soffW], 884736\n"
"buffer_load_dwordx4 %[vW], %[srdG], s60 offen lds\n"
"v_mfma_f32_16x16x32_bf16 v[60:63], v[208:211], a[12:15], 0\n"
"s_waitcnt lgkmcnt(2)\n"
"v_mfma_f32_16x16x32_bf16 v[64:67], v[212:215], a[0:3], 0\n"
"ds_read_b64_tr_b16 v[220:221], v225 offset:1024\n"
"v_mfma_f32_16x16x32_bf16 v[68:71], v[212:215], a[4:7], 0\n"
"ds_read_b64_tr_b16 v[222:223], v225 offset:1536\n"
"v_mfma_f32_16x16x32_bf16 v[72:75], v[212:215], a[8:11], 0\n"
"buffer_load_dwordx4 %[vW], %[srdU], s60 offen offset:1024 lds\n"
"v_mfma_f32_16x16x32_bf16 v[76:79], v[212:215], a[12:15], 0\n"
"s_waitcnt lgkmcnt(2)\n"
"v_mfma_f32_16x16x32_bf16 v[80:83], v[216:219], a[0:3], 0\n"
"ds_read_b64_tr_b16 v[208:209], v226 offset:0\n"
"v_mfma_f32_16x16x32_bf16 v[84:87], v[216:219], a[4:7], 0\n"
"ds_read_b64_tr_b16 v[210:211], v226 offset:512\n"
"v_mfma_f32_16x16x32_bf16 v[88:91], v[216:219], a[8:11], 0\n"
"s_add_u32 s63, %[soffH], 3072\n"
"buffer_load_dwordx4 a[48:51], %[vH], %[srdH], s63 offen\n"
"v_mfma_f32_16x16x32_bf16 v[92:95], v[216:219], a[12:15], 0\n"
"s_waitcnt lgkmcnt(2)\n"
"v_mfma_f32_16x16x32_bf16 v[96:99], v[220:223], a[0:3], 0\n"
"ds_read_b64_tr_b16 v[212:213], v226 offset:1024\n"
"v_mfma_f32_16x16x32_bf16 v[100:103], v[220:223], a[4:7], 0\n"
"ds_read_b64_tr_b16 v[214:215], v226 offset:1536\n"
"v_mfma_f32_16x16x32_bf16 v[104:107], v[220:223], a[8:11], 0\n"
"s_add_u32 s63, %[soffH], 21504\n"
"buffer_load_dwordx4 a[52:55], %[vH], %[srdH], s63 offen\n"
"v_mfma_f32_16x16x32_bf16 v[108:111], v[220:223], a[12:15], 0\n"
"s_waitcnt lgkmcnt(2)\n"
"v_mfma_f32_16x16x32_bf16 v[112:115], v[208:211], a[0:3], 0\n"
"ds_read_b64_tr_b16 v[216:217], v227 offset:0\n"
"v_mfma_f32_16x16x32_bf16 v[116:119], v[208:211], a[4:7], 0\n"
"ds_read_b64_tr_b16 v[218:219], v227 offset:512\n"
"v_mfma_f32_16x16x32_bf16 v[120:123], v[208:211], a[8:11], 0\n"
"s_add_u32 s63, %[soffH], 39936\n"
"buffer_load_dwordx4 a[56:59], %[vH], %[srdH], s63 offen\n"
"v_mfma_f32_16x16x32_bf16 v[124:127], v[208:211], a[12:15], 0\n"
"s_waitcnt lgkmcnt(2)\n"
"v_mfma_f32_16x16x32_bf16 v[128:131], v[212:215], a[0:3], 0\n"
"ds_read_b64_tr_b16 v[220:221], v227 offset:1024\n"
"v_mfma_f32_16x16x32_bf16 v[132:135], v[212:215], a[4:7], 0\n"
"ds_read_b64_tr_b16 v[222:223], v227 offset:1536\n"
"v_mfma_f32_16x16x32_bf16 v[136:139], v[212:215], a[8:11], 0\n"
"v_mfma_f32_16x16x32_bf16 v[140:143], v[212:215], a[12:15], 0\n"
"s_add_u32 s63, %[soffH], 58368\n"
"buffer_load_dwordx4 a[60:63], %[vH], %[srdH], s63 offen\n"
"s_waitcnt vmcnt(12)\n"
"s_barrier\n"
"s_waitcnt lgkmcnt(2)\n"
"v_mfma_f32_16x16x32_bf16 v[144:147], v[216:219], a[0:3], 0\n"
"ds_read_b64_tr_b16 v[208:209], v224 offset:8192\n"
"v_mfma_f32_16x16x32_bf16 v[148:151], v[216:219], a[4:7], 0\n"
"ds_read_b64_tr_b16 v[210:211], v224 offset:8704\n"
"v_mfma_f32_16x16x32_bf16 v[152:155], v[216:219], a[8:11], 0\n"
"v_mfma_f32_16x16x32_bf16 v[156:159], v[216:219], a[12:15], 0\n"
"s_add_u32 m0, %[m0W], 32768\n"
"s_add_u32 s60, %[soffW], 1179648\n"
"buffer_load_dwordx4 %[vW], %[srdG], s60 offen lds\n"
"s_waitcnt lgkmcnt(2)\n"
"v_mfma_f32_16x16x32_bf16 v[160:163], v[220:223], a[0:3], 0\n"
"ds_read_b64_tr_b16 v[212:213], v224 offset:9216\n"
"v_mfma_f32_16x16x32_bf16 v[164:167], v[220:223], a[4:7], 0\n"
"ds_read_b64_tr_b16 v[214:215], v224 offset:9728\n"
"v_mfma_f32_16x16x32_bf16 v[168:171], v[220:223], a[8:11], 0\n"
"v_mfma_f32_16x16x32_bf16 v[172:175], v[220:223], a[12:15], 0\n"
"s_waitcnt lgkmcnt(2)\n"
"v_mfma_f32_16x16x32_bf16 v[48:51], v[208:211], a[16:19], v[48:51]\n"
"ds_read_b64_tr_b16 v[216:217], v225 offset:8192\n"
"v_mfma_f32_16x16x32_bf16 v[52:55], v[208:211], a[20:23], v[52:55]\n"
"ds_read_b64_tr_b16 v[218:219], v225 offset:8704\n"
"v_mfma_f32_16x16x32_bf16 v[56:59], v[208:211], a[24:27], v[56:59]\n"
"buffer_load_dwordx4 %[vW], %[srdU], s60 offen offset:1024 lds\n"
"v_mfma_f32_16x16x32_bf16 v[60:63], v[208:211], a[28:31], v[60:63]\n"
"s_waitcnt lgkmcnt(2)\n"
"v_mfma_f32_16x16x32_bf16 v[64:67], v[212:215], a[16:19], v[64:67]\n"
"ds_read_b64_tr_b16 v[220:221], v225 offset:9216\n"
"v_mfma_f32_16x16x32_bf16 v[68:71], v[212:215], a[20:23], v[68:71]\n"
"ds_read_b64_tr_b16 v[222:223], v225 offset:9728\n"
"v_mfma_f32_16x16x32_bf16 v[72:75], v[212:215], a[24:27], v[72:75]\n"
"s_add_u32 s63, %[soffH], 4096\n"
"buffer_load_dwordx4 a[64:67], %[vH], %[srdH], s63 offen\n"
"v_mfma_f32_16x16x32_bf16 v[76:79], v[212:215], a[28:31], v[76:79]\n"
"s_waitcnt lgkmcnt(2)\n"
"v_mfma_f32_16x16x32_bf16 v[80:83], v[216:219], a[16:19], v[80:83]\n"
"ds_read_b64_tr_b16 v[208:209], v226 offset:8192\n"
"v_mfma_f32_16x16x32_bf16 v[84:87], v[216:219], a[20:23], v[84:87]\n"
"ds_read_b64_tr_b16 v[210:211], v226 offset:8704\n"
"v_mfma_f32_16x16x32_bf16 v[88:91], v[216:219], a[24:27], v[88:91]\n"
"s_add_u32 s63, %[soffH], 22528\n"
"buffer_load_dwordx4 a[68:71], %[vH], %[srdH], s63 offen\n"
"v_mfma_f32_16x16x32_bf16 v[92:95], v[216:219], a[28:31], v[92:95]\n"
"s_waitcnt lgkmcnt(2)\n"
"v_mfma_f32_16x16x32_bf16 v[96:99], v[220:223], a[16:19], v[96:99]\n"
"ds_read_b64_tr_b16 v[212:213], v226 offset:9216\n"
"v_mfma_f32_16x16x32_bf16 v[100:103], v[220:223], a[20:23], v[100:103]\n"
"ds_read_b64_tr_b16 v[214:215], v226 offset:9728\n"
"v_mfma_f32_16x16x32_bf16 v[104:107], v[220:223], a[24:27], v[104:107]\n"
"s_add_u32 s63, %[soffH], 40960\n"
"buffer_load_dwordx4 a[72:75], %[vH], %[srdH], s63 offen\n"
"v_mfma_f32_16x16x32_bf16 v[108:111], v[220:223], a[28:31], v[108:111]\n"
"s_waitcnt lgkmcnt(2)\n"
"v_mfma_f32_16x16x32_bf16 v[112:115], v[208:211], a[16:19], v[112:115]\n"
"ds_read_b64_tr_b16 v[216:217], v227 offset:8192\n"
"v_mfma_f32_16x16x32_bf16 v[116:119], v[208:211], a[20:23], v[116:119]\n"
"ds_read_b64_tr_b16 v[218:219], v227 offset:8704\n"
"v_mfma_f32_16x16x32_bf16 v[120:123], v[208:211], a[24:27], v[120:123]\n"
"s_add_u32 s63, %[soffH], 59392\n"
"buffer_load_dwordx4 a[76:79], %[vH], %[srdH], s63 offen\n"
"v_mfma_f32_16x16x32_bf16 v[124:127], v[208:211], a[28:31], v[124:127]\n"
"s_waitcnt lgkmcnt(2)\n"
"v_mfma_f32_16x16x32_bf16 v[128:131], v[212:215], a[16:19], v[128:131]\n"
"ds_read_b64_tr_b16 v[220:221], v227 offset:9216\n"
"v_mfma_f32_16x16x32_bf16 v[132:135], v[212:215], a[20:23], v[132:135]\n"
"ds_read_b64_tr_b16 v[222:223], v227 offset:9728\n"
"v_mfma_f32_16x16x32_bf16 v[136:139], v[212:215], a[24:27], v[136:139]\n"
"s_add_u32 m0, %[m0W], 40960\n"
"s_add_u32 s60, %[soffW], 1474560\n"
"buffer_load_dwordx4 %[vW], %[srdG], s60 offen lds\n"
"v_mfma_f32_16x16x32_bf16 v[140:143], v[212:215], a[28:31], v[140:143]\n"
"s_waitcnt vmcnt(13)\n"
"s_barrier\n"
"s_waitcnt lgkmcnt(2)\n"
"v_mfma_f32_16x16x32_bf16 v[144:147], v[216:219], a[16:19], v[144:147]\n"
"ds_read_b64_tr_b16 v[208:209], v224 offset:16384\n"
"v_mfma_f32_16x16x32_bf16 v[148:151], v[216:219], a[20:23], v[148:151]\n"
"ds_read_b64_tr_b16 v[210:211], v224 offset:16896\n"
"v_mfma_f32_16x16x32_bf16 v[152:155], v[216:219], a[24:27], v[152:155]\n"
"v_mfma_f32_16x16x32_bf16 v[156:159], v[216:219], a[28:31], v[156:159]\n"
"buffer_load_dwordx4 %[vW], %[srdU], s60 offen offset:1024 lds\n"
"s_waitcnt lgkmcnt(2)\n"
"v_mfma_f32_16x16x32_bf16 v[160:163], v[220:223], a[16:19], v[160:163]\n"
"ds_read_b64_tr_b16 v[212:213], v224 offset:17408\n"
"v_mfma_f32_16x16x32_bf16 v[164:167], v[220:223], a[20:23], v[164:167]\n"
"ds_read_b64_tr_b16 v[214:215], v224 offset:17920\n"
"v_mfma_f32_16x16x32_bf16 v[168:171], v[220:223], a[24:27], v[168:171]\n"
"v_mfma_f32_16x16x32_bf16 v[172:175], v[220:223], a[28:31], v[172:175]\n"
"s_add_u32 s63, %[soffH], 5120\n"
"buffer_load_dwordx4 a[80:83], %[vH], %[srdH], s63 offen\n"
"s_waitcnt lgkmcnt(2)\n"
"v_mfma_f32_16x16x32_bf16 v[48:51], v[208:211], a[32:35], v[48:51]\n"
"ds_read_b64_tr_b16 v[216:217], v225 offset:16384\n"
"v_mfma_f32_16x16x32_bf16 v[52:55], v[208:211], a[36:39], v[52:55]\n"
"ds_read_b64_tr_b16 v[218:219], v225 offset:16896\n"
"v_mfma_f32_16x16x32_bf16 v[56:59], v[208:211], a[40:43], v[56:59]\n"
"v_mfma_f32_16x16x32_bf16 v[60:63], v[208:211], a[44:47], v[60:63]\n"
"s_waitcnt lgkmcnt(2)\n"
"v_mfma_f32_16x16x32_bf16 v[64:67], v[212:215], a[32:35], v[64:67]\n"
"ds_read_b64_tr_b16 v[220:221], v225 offset:17408\n"
"v_mfma_f32_16x16x32_bf16 v[68:71], v[212:215], a[36:39], v[68:71]\n"
"ds_read_b64_tr_b16 v[222:223], v225 offset:17920\n"
"v_mfma_f32_16x16x32_bf16 v[72:75], v[212:215], a[40:43], v[72:75]\n"
"s_add_u32 s63, %[soffH], 23552\n"
"buffer_load_dwordx4 a[84:87], %[vH], %[srdH], s63 offen\n"
"v_mfma_f32_16x16x32_bf16 v[76:79], v[212:215], a[44:47], v[76:79]\n"
"s_waitcnt lgkmcnt(2)\n"
"v_mfma_f32_16x16x32_bf16 v[80:83], v[216:219], a[32:35], v[80:83]\n"
"ds_read_b64_tr_b16 v[208:209], v226 offset:16384\n"
"v_mfma_f32_16x16x32_bf16 v[84:87], v[216:219], a[36:39], v[84:87]\n"
"ds_read_b64_tr_b16 v[210:211], v226 offset:16896\n"
"v_mfma_f32_16x16x32_bf16 v[88:91], v[216:219], a[40:43], v[88:91]\n"
"s_add_u32 s63, %[soffH], 41984\n"
"buffer_load_dwordx4 a[88:91], %[vH], %[srdH], s63 offen\n"
"v_mfma_f32_16x16x32_bf16 v[92:95], v[216:219], a[44:47], v[92:95]\n"
"s_waitcnt lgkmcnt(2)\n"
"v_mfma_f32_16x16x32_bf16 v[96:99], v[220:223], a[32:35], v[96:99]\n"
"ds_read_b64_tr_b16 v[212:213], v226 offset:17408\n"
"v_mfma_f32_16x16x32_bf16 v[100:103], v[220:223], a[36:39], v[100:103]\n"
"ds_read_b64_tr_b16 v[214:215], v226 offset:17920\n"
"v_mfma_f32_16x16x32_bf16 v[104:107], v[220:223], a[40:43], v[104:107]\n"
"s_add_u32 s63, %[soffH], 60416\n"
"buffer_load_dwordx4 a[92:95], %[vH], %[srdH], s63 offen\n"
"v_mfma_f32_16x16x32_bf16 v[108:111], v[220:223], a[44:47], v[108:111]\n"
"s_waitcnt lgkmcnt(2)\n"
"v_mfma_f32_16x16x32_bf16 v[112:115], v[208:211], a[32:35], v[112:115]\n"
"ds_read_b64_tr_b16 v[216:217], v227 offset:16384\n"
"v_mfma_f32_16x16x32_bf16 v[116:119], v[208:211], a[36:39], v[116:119]\n"
"ds_read_b64_tr_b16 v[218:219], v227 offset:16896\n"
"v_mfma_f32_16x16x32_bf16 v[120:123], v[208:211], a[40:43], v[120:123]\n"
"s_add_u32 m0, %[m0W], 49152\n"
"s_add_u32 s60, %[soffW], 1769472\n"
"buffer_load_dwordx4 %[vW], %[srdG], s60 offen lds\n"
"v_mfma_f32_16x16x32_bf16 v[124:127], v[208:211], a[44:47], v[124:127]\n"
"s_waitcnt lgkmcnt(2)\n"
"v_mfma_f32_16x16x32_bf16 v[128:131], v[212:215], a[32:35], v[128:131]\n"
"ds_read_b64_tr_b16 v[220:221], v227 offset:17408\n"
"v_mfma_f32_16x16x32_bf16 v[132:135], v[212:215], a[36:39], v[132:135]\n"
"ds_read_b64_tr_b16 v[222:223], v227 offset:17920\n"
"v_mfma_f32_16x16x32_bf16 v[136:139], v[212:215], a[40:43], v[136:139]\n"
"buffer_load_dwordx4 %[vW], %[srdU], s60 offen offset:1024 lds\n"
"v_mfma_f32_16x16x32_bf16 v[140:143], v[212:215], a[44:47], v[140:143]\n"
"s_waitcnt vmcnt(14)\n"
"s_barrier\n"
"s_waitcnt lgkmcnt(2)\n"
"v_mfma_f32_16x16x32_bf16 v[144:147], v[216:219], a[32:35], v[144:147]\n"
"ds_read_b64_tr_b16 v[208:209], v224 offset:24576\n"
"v_mfma_f32_16x16x32_bf16 v[148:151], v[216:219], a[36:39], v[148:151]\n"
"ds_read_b64_tr_b16 v[210:211], v224 offset:25088\n"
"v_mfma_f32_16x16x32_bf16 v[152:155], v[216:219], a[40:43], v[152:155]\n"
"s_add_u32 s63, %[soffH], 6144\n"
"buffer_load_dwordx4 a[96:99], %[vH], %[srdH], s63 offen\n"
"v_mfma_f32_16x16x32_bf16 v[156:159], v[216:219], a[44:47], v[156:159]\n"
"s_waitcnt lgkmcnt(2)\n"
"v_mfma_f32_16x16x32_bf16 v[160:163], v[220:223], a[32:35], v[160:163]\n"
"ds_read_b64_tr_b16 v[212:213], v224 offset:25600\n"
"v_mfma_f32_16x16x32_bf16 v[164:167], v[220:223], a[36:39], v[164:167]\n"
"ds_read_b64_tr_b16 v[214:215], v224 offset:26112\n"
"v_mfma_f32_16x16x32_bf16 v[168:171], v[220:223], a[40:43], v[168:171]\n"
"v_mfma_f32_16x16x32_bf16 v[172:175], v[220:223], a[44:47], v[172:175]\n"
"s_add_u32 s63, %[soffH], 24576\n"
"buffer_load_dwordx4 a[100:103], %[vH], %[srdH], s63 offen\n"
"s_waitcnt lgkmcnt(2)\n"
"v_mfma_f32_16x16x32_bf16 v[48:51], v[208:211], a[48:51], v[48:51]\n"
"ds_read_b64_tr_b16 v[216:217], v225 offset:24576\n"
"v_mfma_f32_16x16x32_bf16 v[52:55], v[208:211], a[52:55], v[52:55]\n"
"ds_read_b64_tr_b16 v[218:219], v225 offset:25088\n"
"v_mfma_f32_16x16x32_bf16 v[56:59], v[208:211], a[56:59], v[56:59]\n"
"v_mfma_f32_16x16x32_bf16 v[60:63], v[208:211], a[60:63], v[60:63]\n"
"s_add_u32 s63, %[soffH], 43008\n"
"buffer_load_dwordx4 a[104:107], %[vH], %[srdH], s63 offen\n"
"s_waitcnt lgkmcnt(2)\n"
"v_mfma_f32_16x16x32_bf16 v[64:67], v[212:215], a[48:51], v[64:67]\n"
"ds_read_b64_tr_b16 v[220:221], v225 offset:25600\n"
"v_mfma_f32_16x16x32_bf16 v[68:71], v[212:215], a[52:55], v[68:71]\n"
"ds_read_b64_tr_b16 v[222:223], v225 offset:26112\n"
"v_mfma_f32_16x16x32_bf16 v[72:75], v[212:215], a[56:59], v[72:75]\n"
"v_mfma_f32_16x16x32_bf16 v[76:79], v[212:215], a[60:63], v[76:79]\n"
"s_waitcnt lgkmcnt(2)\n"
"v_mfma_f32_16x16x32_bf16 v[80:83], v[216:219], a[48:51], v[80:83]\n"
"ds_read_b64_tr_b16 v[208:209], v226 offset:24576\n"
"v_mfma_f32_16x16x32_bf16 v[84:87], v[216:219], a[52:55], v[84:87]\n"
"ds_read_b64_tr_b16 v[210:211], v226 offset:25088\n"
"v_mfma_f32_16x16x32_bf16 v[88:91], v[216:219], a[56:59], v[88:91]\n"
"s_add_u32 s63, %[soffH], 61440\n"
"buffer_load_dwordx4 a[108:111], %[vH], %[srdH], s63 offen\n"
"v_mfma_f32_16x16x32_bf16 v[92:95], v[216:219], a[60:63], v[92:95]\n"
"s_waitcnt lgkmcnt(2)\n"
"v_mfma_f32_16x16x32_bf16 v[96:99], v[220:223], a[48:51], v[96:99]\n"
"ds_read_b64_tr_b16 v[212:213], v226 offset:25600\n"
"v_mfma_f32_16x16x32_bf16 v[100:103], v[220:223], a[52:55], v[100:103]\n"
"ds_read_b64_tr_b16 v[214:215], v226 offset:26112\n"
"v_mfma_f32_16x16x32_bf16 v[104:107], v[220:223], a[56:59], v[104:107]\n"
"s_add_u32 m0, %[m0W], 57344\n"
"s_add_u32 s60, %[soffW], 2064384\n"
"buffer_load_dwordx4 %[vW], %[srdG], s60 offen lds\n"
"v_mfma_f32_16x16x32_bf16 v[108:111], v[220:223], a[60:63], v[108:111]\n"
"s_waitcnt lgkmcnt(2)\n"
"v_mfma_f32_16x16x32_bf16 v[112:115], v[208:211], a[48:51], v[112:115]\n"
"ds_read_b64_tr_b16 v[216:217], v227 offset:24576\n"
"v_mfma_f32_16x16x32_bf16 v[116:119], v[208:211], a[52:55], v[116:119]\n"
"ds_read_b64_tr_b16 v[218:219], v227 offset:25088\n"
"v_mfma_f32_16x16x32_bf16 v[120:123], v[208:211], a[56:59], v[120:123]\n"
"buffer_load_dwordx4 %[vW], %[srdU], s60 offen offset:1024 lds\n"
"v_mfma_f32_16x16x32_bf16 v[124:127], v[208:211], a[60:63], v[124:127]\n"
"s_waitcnt lgkmcnt(2)\n"
"v_mfma_f32_16x16x32_bf16 v[128:131], v[212:215], a[48:51], v[128:131]\n"
"ds_read_b64_tr_b16 v[220:221], v227 offset:25600\n"
"v_mfma_f32_16x16x32_bf16 v[132:135], v[212:215], a[52:55], v[132:135]\n"
"ds_read_b64_tr_b16 v[222:223], v227 offset:26112\n"
"v_mfma_f32_16x16x32_bf16 v[136:139], v[212:215], a[56:59], v[136:139]\n"
"s_add_u32 s63, %[soffH], 7168\n"
"buffer_load_dwordx4 a[112:115], %[vH], %[srdH], s63 offen\n"
"v_mfma_f32_16x16x32_bf16 v[140:143], v[212:215], a[60:63], v[140:143]\n"
"s_waitcnt vmcnt(15)\n"
"s_barrier\n"
"s_waitcnt lgkmcnt(2)\n"
"v_mfma_f32_16x16x32_bf16 v[144:147], v[216:219], a[48:51], v[144:147]\n"
"ds_read_b64_tr_b16 v[208:209], v224 offset:32768\n"
"v_mfma_f32_16x16x32_bf16 v[148:151], v[216:219], a[52:55], v[148:151]\n"
"ds_read_b64_tr_b16 v[210:211], v224 offset:33280\n"
"v_mfma_f32_16x16x32_bf16 v[152:155], v[216:219], a[56:59], v[152:155]\n"
"s_add_u32 s63, %[soffH], 25600\n"
"buffer_load_dwordx4 a[116:119], %[vH], %[srdH], s63 offen\n"
"v_mfma_f32_16x16x32_bf16 v[156:159], v[216:219], a[60:63], v[156:159]\n"
"s_waitcnt lgkmcnt(2)\n"
"v_mfma_f32_16x16x32_bf16 v[160:163], v[220:223], a[48:51], v[160:163]\n"
"ds_read_b64_tr_b16 v[212:213], v224 offset:33792\n"
"v_mfma_f32_16x16x32_bf16 v[164:167], v[220:223], a[52:55], v[164:167]\n"
"ds_read_b64_tr_b16 v[214:215], v224 offset:34304\n"
"v_mfma_f32_16x16x32_bf16 v[168:171], v[220:223], a[56:59], v[168:171]\n"
"s_add_u32 s63, %[soffH], 44032\n"
"buffer_load_dwordx4 a[120:123], %[vH], %[srdH], s63 offen\n"
"v_mfma_f32_16x16x32_bf16 v[172:175], v[220:223], a[60:63], v[172:175]\n"
"s_waitcnt lgkmcnt(2)\n"
"v_mfma_f32_16x16x32_bf16 v[48:51], v[208:211], a[64:67], v[48:51]\n"
"ds_read_b64_tr_b16 v[216:217], v225 offset:32768\n"
"v_mfma_f32_16x16x32_bf16 v[52:55], v[208:211], a[68:71], v[52:55]\n"
"ds_read_b64_tr_b16 v[218:219], v225 offset:33280\n"
"v_mfma_f32_16x16x32_bf16 v[56:59], v[208:211], a[72:75], v[56:59]\n"
"v_mfma_f32_16x16x32_bf16 v[60:63], v[208:211], a[76:79], v[60:63]\n"
"s_add_u32 s63, %[soffH], 62464\n"
"buffer_load_dwordx4 a[124:127], %[vH], %[srdH], s63 offen\n"
"s_waitcnt lgkmcnt(2)\n"
"v_mfma_f32_16x16x32_bf16 v[64:67], v[212:215], a[64:67], v[64:67]\n"
"ds_read_b64_tr_b16 v[220:221], v225 offset:33792\n"
"v_mfma_f32_16x16x32_bf16 v[68:71], v[212:215], a[68:71], v[68:71]\n"
"ds_read_b64_tr_b16 v[222:223], v225 offset:34304\n"
"v_mfma_f32_16x16x32_bf16 v[72:75], v[212:215], a[72:75], v[72:75]\n"
"v_mfma_f32_16x16x32_bf16 v[76:79], v[212:215], a[76:79], v[76:79]\n"
"s_add_u32 m0, %[m0W], 65536\n"
"s_add_u32 s60, %[soffW], 2359296\n"
"buffer_load_dwordx4 %[vW], %[srdG], s60 offen lds\n"
"s_waitcnt lgkmcnt(2)\n"
"v_mfma_f32_16x16x32_bf16 v[80:83], v[216:219], a[64:67], v[80:83]\n"
"ds_read_b64_tr_b16 v[208:209], v226 offset:32768\n"
"v_mfma_f32_16x16x32_bf16 v[84:87], v[216:219], a[68:71], v[84:87]\n"
"ds_read_b64_tr_b16 v[210:211], v226 offset:33280\n"
"v_mfma_f32_16x16x32_bf16 v[88:91], v[216:219], a[72:75], v[88:91]\n"
"v_mfma_f32_16x16x32_bf16 v[92:95], v[216:219], a[76:79], v[92:95]\n"
"s_waitcnt lgkmcnt(2)\n"
"v_mfma_f32_16x16x32_bf16 v[96:99], v[220:223], a[64:67], v[96:99]\n"
"ds_read_b64_tr_b16 v[212:213], v226 offset:33792\n"
"v_mfma_f32_16x16x32_bf16 v[100:103], v[220:223], a[68:71], v[100:103]\n"
"ds_read_b64_tr_b16 v[214:215], v226 offset:34304\n"
"v_mfma_f32_16x16x32_bf16 v[104:107], v[220:223], a[72:75], v[104:107]\n"
"buffer_load_dwordx4 %[vW], %[srdU], s60 offen offset:1024 lds\n"
"v_mfma_f32_16x16x32_bf16 v[108:111], v[220:223], a[76:79], v[108:111]\n"
"s_waitcnt lgkmcnt(2)\n"
"v_mfma_f32_16x16x32_bf16 v[112:115], v[208:211], a[64:67], v[112:115]\n"
"ds_read_b64_tr_b16 v[216:217], v227 offset:32768\n"
"v_mfma_f32_16x16x32_bf16 v[116:119], v[208:211], a[68:71], v[116:119]\n"
"ds_read_b64_tr_b16 v[218:219], v227 offset:33280\n"
"v_mfma_f32_16x16x32_bf16 v[120:123], v[208:211], a[72:75], v[120:123]\n"
"s_add_u32 s63, %[soffH], 8192\n"
"buffer_load_dwordx4 a[128:131], %[vH], %[srdH], s63 offen\n"
"v_mfma_f32_16x16x32_bf16 v[124:127], v[208:211], a[76:79], v[124:127]\n"
"s_waitcnt lgkmcnt(2)\n"
"v_mfma_f32_16x16x32_bf16 v[128:131], v[212:215], a[64:67], v[128:131]\n"
"ds_read_b64_tr_b16 v[220:221], v227 offset:33792\n"
"v_mfma_f32_16x16x32_bf16 v[132:135], v[212:215], a[68:71], v[132:135]\n"
"ds_read_b64_tr_b16 v[222:223], v227 offset:34304\n"
"v_mfma_f32_16x16x32_bf16 v[136:139], v[212:215], a[72:75], v[136:139]\n"
"s_add_u32 s63, %[soffH], 26624\n"
"buffer_load_dwordx4 a[132:135], %[vH], %[srdH], s63 offen\n"
"v_mfma_f32_16x16x32_bf16 v[140:143], v[212:215], a[76:79], v[140:143]\n"
"s_waitcnt vmcnt(16)\n"
"s_barrier\n"
"s_waitcnt lgkmcnt(2)\n"
"v_mfma_f32_16x16x32_bf16 v[144:147], v[216:219], a[64:67], v[144:147]\n"
"ds_read_b64_tr_b16 v[208:209], v224 offset:40960\n"
"v_mfma_f32_16x16x32_bf16 v[148:151], v[216:219], a[68:71], v[148:151]\n"
"ds_read_b64_tr_b16 v[210:211], v224 offset:41472\n"
"v_mfma_f32_16x16x32_bf16 v[152:155], v[216:219], a[72:75], v[152:155]\n"
"s_add_u32 s63, %[soffH], 45056\n"
"buffer_load_dwordx4 a[136:139], %[vH], %[srdH], s63 offen\n"
"v_mfma_f32_16x16x32_bf16 v[156:159], v[216:219], a[76:79], v[156:159]\n"
"s_waitcnt lgkmcnt(2)\n"
"v_mfma_f32_16x16x32_bf16 v[160:163], v[220:223], a[64:67], v[160:163]\n"
"ds_read_b64_tr_b16 v[212:213], v224 offset:41984\n"
"v_mfma_f32_16x16x32_bf16 v[164:167], v[220:223], a[68:71], v[164:167]\n"
"ds_read_b64_tr_b16 v[214:215], v224 offset:42496\n"
"v_mfma_f32_16x16x32_bf16 v[168:171], v[220:223], a[72:75], v[168:171]\n"
"s_add_u32 s63, %[soffH], 63488\n"
"buffer_load_dwordx4 a[140:143], %[vH], %[srdH], s63 offen\n"
"v_mfma_f32_16x16x32_bf16 v[172:175], v[220:223], a[76:79], v[172:175]\n"
"s_waitcnt lgkmcnt(2)\n"
"v_mfma_f32_16x16x32_bf16 v[48:51], v[208:211], a[80:83], v[48:51]\n"
"ds_read_b64_tr_b16 v[216:217], v225 offset:40960\n"
"v_mfma_f32_16x16x32_bf16 v[52:55], v[208:211], a[84:87], v[52:55]\n"
"ds_read_b64_tr_b16 v[218:219], v225 offset:41472\n"
"v_mfma_f32_16x16x32_bf16 v[56:59], v[208:211], a[88:91], v[56:59]\n"
"s_add_u32 m0, %[m0W], 73728\n"
"s_add_u32 s60, %[soffW], 2654208\n"
"buffer_load_dwordx4 %[vW], %[srdG], s60 offen lds\n"
"v_mfma_f32_16x16x32_bf16 v[60:63], v[208:211], a[92:95], v[60:63]\n"
"s_waitcnt lgkmcnt(2)\n"
"v_mfma_f32_16x16x32_bf16 v[64:67], v[212:215], a[80:83], v[64:67]\n"
"ds_read_b64_tr_b16 v[220:221], v225 offset:41984\n"
"v_mfma_f32_16x16x32_bf16 v[68:71], v[212:215], a[84:87], v[68:71]\n"
"ds_read_b64_tr_b16 v[222:223], v225 offset:42496\n"
"v_mfma_f32_16x16x32_bf16 v[72:75], v[212:215], a[88:91], v[72:75]\n"
"v_mfma_f32_16x16x32_bf16 v[76:79], v[212:215], a[92:95], v[76:79]\n"
"buffer_load_dwordx4 %[vW], %[srdU], s60 offen offset:1024 lds\n"
"s_waitcnt lgkmcnt(2)\n"
"v_mfma_f32_16x16x32_bf16 v[80:83], v[216:219], a[80:83], v[80:83]\n"
"ds_read_b64_tr_b16 v[208:209], v226 offset:40960\n"
"v_mfma_f32_16x16x32_bf16 v[84:87], v[216:219], a[84:87], v[84:87]\n"
"ds_read_b64_tr_b16 v[210:211], v226 offset:41472\n"
"v_mfma_f32_16x16x32_bf16 v[88:91], v[216:219], a[88:91], v[88:91]\n"
"v_mfma_f32_16x16x32_bf16 v[92:95], v[216:219], a[92:95], v[92:95]\n"
"s_add_u32 s63, %[soffH], 9216\n"
"buffer_load_dwordx4 a[144:147], %[vH], %[srdH], s63 offen\n"
"s_waitcnt lgkmcnt(2)\n"
"v_mfma_f32_16x16x32_bf16 v[96:99], v[220:223], a[80:83], v[96:99]\n"
"ds_read_b64_tr_b16 v[212:213], v226 offset:41984\n"
"v_mfma_f32_16x16x32_bf16 v[100:103], v[220:223], a[84:87], v[100:103]\n"
"ds_read_b64_tr_b16 v[214:215], v226 offset:42496\n"
"v_mfma_f32_16x16x32_bf16 v[104:107], v[220:223], a[88:91], v[104:107]\n"
"v_mfma_f32_16x16x32_bf16 v[108:111], v[220:223], a[92:95], v[108:111]\n"
"s_waitcnt lgkmcnt(2)\n"
"v_mfma_f32_16x16x32_bf16 v[112:115], v[208:211], a[80:83], v[112:115]\n"
"ds_read_b64_tr_b16 v[216:217], v227 offset:40960\n"
"v_mfma_f32_16x16x32_bf16 v[116:119], v[208:211], a[84:87], v[116:119]\n"
"ds_read_b64_tr_b16 v[218:219], v227 offset:41472\n"
"v_mfma_f32_16x16x32_bf16 v[120:123], v[208:211], a[88:91], v[120:123]\n"
"s_add_u32 s63, %[soffH], 27648\n"
"buffer_load_dwordx4 a[148:151], %[vH], %[srdH], s63 offen\n"
"v_mfma_f32_16x16x32_bf16 v[124:127], v[208:211], a[92:95], v[124:127]\n"
"s_waitcnt lgkmcnt(2)\n"
"v_mfma_f32_16x16x32_bf16 v[128:131], v[212:215], a[80:83], v[128:131]\n"
"ds_read_b64_tr_b16 v[220:221], v227 offset:41984\n"
"v_mfma_f32_16x16x32_bf16 v[132:135], v[212:215], a[84:87], v[132:135]\n"
"ds_read_b64_tr_b16 v[222:223], v227 offset:42496\n"
"v_mfma_f32_16x16x32_bf16 v[136:139], v[212:215], a[88:91], v[136:139]\n"
"s_add_u32 s63, %[soffH], 46080\n"
"buffer_load_dwordx4 a[152:155], %[vH], %[srdH], s63 offen\n"
"v_mfma_f32_16x16x32_bf16 v[140:143], v[212:215], a[92:95], v[140:143]\n"
"s_waitcnt vmcnt(17)\n"
"s_barrier\n"
"s_waitcnt lgkmcnt(2)\n"
"v_mfma_f32_16x16x32_bf16 v[144:147], v[216:219], a[80:83], v[144:147]\n"
"ds_read_b64_tr_b16 v[208:209], v224 offset:49152\n"
"v_mfma_f32_16x16x32_bf16 v[148:151], v[216:219], a[84:87], v[148:151]\n"
"ds_read_b64_tr_b16 v[210:211], v224 offset:49664\n"
"v_mfma_f32_16x16x32_bf16 v[152:155], v[216:219], a[88:91], v[152:155]\n"
"s_add_u32 s63, %[soffH], 64512\n"
"buffer_load_dwordx4 a[156:159], %[vH], %[srdH], s63 offen\n"
"v_mfma_f32_16x16x32_bf16 v[156:159], v[216:219], a[92:95], v[156:159]\n"
"s_waitcnt lgkmcnt(2)\n"
"v_mfma_f32_16x16x32_bf16 v[160:163], v[220:223], a[80:83], v[160:163]\n"
"ds_read_b64_tr_b16 v[212:213], v224 offset:50176\n"
"v_mfma_f32_16x16x32_bf16 v[164:167], v[220:223], a[84:87], v[164:167]\n"
"ds_read_b64_tr_b16 v[214:215], v224 offset:50688\n"
"v_mfma_f32_16x16x32_bf16 v[168:171], v[220:223], a[88:91], v[168:171]\n"
"s_add_u32 m0, %[m0W], 81920\n"
"s_add_u32 s60, %[soffW], 2949120\n"
"buffer_load_dwordx4 %[vW], %[srdG], s60 offen lds\n"
"v_mfma_f32_16x16x32_bf16 v[172:175], v[220:223], a[92:95], v[172:175]\n"
"s_waitcnt lgkmcnt(2)\n"
"v_mfma_f32_16x16x32_bf16 v[48:51], v[208:211], a[96:99], v[48:51]\n"
"ds_read_b64_tr_b16 v[216:217], v225 offset:49152\n"
"v_mfma_f32_16x16x32_bf16 v[52:55], v[208:211], a[100:103], v[52:55]\n"
"ds_read_b64_tr_b16 v[218:219], v225 offset:49664\n"
"v_mfma_f32_16x16x32_bf16 v[56:59], v[208:211], a[104:107], v[56:59]\n"
"buffer_load_dwordx4 %[vW], %[srdU], s60 offen offset:1024 lds\n"
"v_mfma_f32_16x16x32_bf16 v[60:63], v[208:211], a[108:111], v[60:63]\n"
"s_waitcnt lgkmcnt(2)\n"
"v_mfma_f32_16x16x32_bf16 v[64:67], v[212:215], a[96:99], v[64:67]\n"
"ds_read_b64_tr_b16 v[220:221], v225 offset:50176\n"
"v_mfma_f32_16x16x32_bf16 v[68:71], v[212:215], a[100:103], v[68:71]\n"
"ds_read_b64_tr_b16 v[222:223], v225 offset:50688\n"
"v_mfma_f32_16x16x32_bf16 v[72:75], v[212:215], a[104:107], v[72:75]\n"
"s_add_u32 s63, %[soffH], 10240\n"
"buffer_load_dwordx4 a[160:163], %[vH], %[srdH], s63 offen\n"
"v_mfma_f32_16x16x32_bf16 v[76:79], v[212:215], a[108:111], v[76:79]\n"
"s_waitcnt lgkmcnt(2)\n"
"v_mfma_f32_16x16x32_bf16 v[80:83], v[216:219], a[96:99], v[80:83]\n"
"ds_read_b64_tr_b16 v[208:209], v226 offset:49152\n"
"v_mfma_f32_16x16x32_bf16 v[84:87], v[216:219], a[100:103], v[84:87]\n"
"ds_read_b64_tr_b16 v[210:211], v226 offset:49664\n"
"v_mfma_f32_16x16x32_bf16 v[88:91], v[216:219], a[104:107], v[88:91]\n"
"v_mfma_f32_16x16x32_bf16 v[92:95], v[216:219], a[108:111], v[92:95]\n"
"s_add_u32 s63, %[soffH], 28672\n"
"buffer_load_dwordx4 a[164:167], %[vH], %[srdH], s63 offen\n"
"s_waitcnt lgkmcnt(2)\n"
"v_mfma_f32_16x16x32_bf16 v[96:99], v[220:223], a[96:99], v[96:99]\n"
"ds_read_b64_tr_b16 v[212:213], v226 offset:50176\n"
"v_mfma_f32_16x16x32_bf16 v[100:103], v[220:223], a[100:103], v[100:103]\n"
"ds_read_b64_tr_b16 v[214:215], v226 offset:50688\n"
"v_mfma_f32_16x16x32_bf16 v[104:107], v[220:223], a[104:107], v[104:107]\n"
"v_mfma_f32_16x16x32_bf16 v[108:111], v[220:223], a[108:111], v[108:111]\n"
"s_add_u32 s63, %[soffH], 47104\n"
"buffer_load_dwordx4 a[168:171], %[vH], %[srdH], s63 offen\n"
"s_waitcnt lgkmcnt(2)\n"
"v_mfma_f32_16x16x32_bf16 v[112:115], v[208:211], a[96:99], v[112:115]\n"
"ds_read_b64_tr_b16 v[216:217], v227 offset:49152\n"
"v_mfma_f32_16x16x32_bf16 v[116:119], v[208:211], a[100:103], v[116:119]\n"
"ds_read_b64_tr_b16 v[218:219], v227 offset:49664\n"
"v_mfma_f32_16x16x32_bf16 v[120:123], v[208:211], a[104:107], v[120:123]\n"
"v_mfma_f32_16x16x32_bf16 v[124:127], v[208:211], a[108:111], v[124:127]\n"
"s_waitcnt lgkmcnt(2)\n"
"v_mfma_f32_16x16x32_bf16 v[128:131], v[212:215], a[96:99], v[128:131]\n"
"ds_read_b64_tr_b16 v[220:221], v227 offset:50176\n"
"v_mfma_f32_16x16x32_bf16 v[132:135], v[212:215], a[100:103], v[132:135]\n"
"ds_read_b64_tr_b16 v[222:223], v227 offset:50688\n"
"v_mfma_f32_16x16x32_bf16 v[136:139], v[212:215], a[104:107], v[136:139]\n"
"s_add_u32 s63, %[soffH], 65536\n"
"buffer_load_dwordx4 a[172:175], %[vH], %[srdH], s63 offen\n"
"v_mfma_f32_16x16x32_bf16 v[140:143], v[212:215], a[108:111], v[140:143]\n"
"s_waitcnt vmcnt(18)\n"
"s_barrier\n"
"s_waitcnt lgkmcnt(2)\n"
"v_mfma_f32_16x16x32_bf16 v[144:147], v[216:219], a[96:99], v[144:147]\n"
"ds_read_b64_tr_b16 v[208:209], v224 offset:57344\n"
"v_mfma_f32_16x16x32_bf16 v[148:151], v[216:219], a[100:103], v[148:151]\n"
"ds_read_b64_tr_b16 v[210:211], v224 offset:57856\n"
"v_mfma_f32_16x16x32_bf16 v[152:155], v[216:219], a[104:107], v[152:155]\n"
"s_add_u32 m0, %[m0W], 90112\n"
"s_add_u32 s60, %[soffW], 3244032\n"
"buffer_load_dwordx4 %[vW], %[srdG], s60 offen lds\n"
"v_mfma_f32_16x16x32_bf16 v[156:159], v[216:219], a[108:111], v[156:159]\n"
"s_waitcnt lgkmcnt(2)\n"
"v_mfma_f32_16x16x32_bf16 v[160:163], v[220:223], a[96:99], v[160:163]\n"
"ds_read_b64_tr_b16 v[212:213], v224 offset:58368\n"
"v_mfma_f32_16x16x32_bf16 v[164:167], v[220:223], a[100:103], v[164:167]\n"
"ds_read_b64_tr_b16 v[214:215], v224 offset:58880\n"
"v_mfma_f32_16x16x32_bf16 v[168:171], v[220:223], a[104:107], v[168:171]\n"
"buffer_load_dwordx4 %[vW], %[srdU], s60 offen offset:1024 lds\n"
"v_mfma_f32_16x16x32_bf16 v[172:175], v[220:223], a[108:111], v[172:175]\n"
"s_waitcnt lgkmcnt(2)\n"
"v_mfma_f32_16x16x32_bf16 v[48:51], v[208:211], a[112:115], v[48:51]\n"
"ds_read_b64_tr_b16 v[216:217], v225 offset:57344\n"
"v_mfma_f32_16x16x32_bf16 v[52:55], v[208:211], a[116:119], v[52:55]\n"
"ds_read_b64_tr_b16 v[218:219], v225 offset:57856\n"
"v_mfma_f32_16x16x32_bf16 v[56:59], v[208:211], a[120:123], v[56:59]\n"
"s_add_u32 s63, %[soffH], 11264\n"
"buffer_load_dwordx4 a[176:179], %[vH], %[srdH], s63 offen\n"
"v_mfma_f32_16x16x32_bf16 v[60:63], v[208:211], a[124:127], v[60:63]\n"
"s_waitcnt lgkmcnt(2)\n"
"v_mfma_f32_16x16x32_bf16 v[64:67], v[212:215], a[112:115], v[64:67]\n"
"ds_read_b64_tr_b16 v[220:221], v225 offset:58368\n"
"v_mfma_f32_16x16x32_bf16 v[68:71], v[212:215], a[116:119], v[68:71]\n"
"ds_read_b64_tr_b16 v[222:223], v225 offset:58880\n"
"v_mfma_f32_16x16x32_bf16 v[72:75], v[212:215], a[120:123], v[72:75]\n"
"s_add_u32 s63, %[soffH], 29696\n"
"buffer_load_dwordx4 a[180:183], %[vH], %[srdH], s63 offen\n"
"v_mfma_f32_16x16x32_bf16 v[76:79], v[212:215], a[124:127], v[76:79]\n"
"s_waitcnt lgkmcnt(2)\n"
"v_mfma_f32_16x16x32_bf16 v[80:83], v[216:219], a[112:115], v[80:83]\n"
"ds_read_b64_tr_b16 v[208:209], v226 offset:57344\n"
"v_mfma_f32_16x16x32_bf16 v[84:87], v[216:219], a[116:119], v[84:87]\n"
"ds_read_b64_tr_b16 v[210:211], v226 offset:57856\n"
"v_mfma_f32_16x16x32_bf16 v[88:91], v[216:219], a[120:123], v[88:91]\n"
"s_add_u32 s63, %[soffH], 48128\n"
"buffer_load_dwordx4 a[184:187], %[vH], %[srdH], s63 offen\n"
"v_mfma_f32_16x16x32_bf16 v[92:95], v[216:219], a[124:127], v[92:95]\n"
"s_waitcnt lgkmcnt(2)\n"
"v_mfma_f32_16x16x32_bf16 v[96:99], v[220:223], a[112:115], v[96:99]\n"
"ds_read_b64_tr_b16 v[212:213], v226 offset:58368\n"
"v_mfma_f32_16x16x32_bf16 v[100:103], v[220:223], a[116:119], v[100:103]\n"
"ds_read_b64_tr_b16 v[214:215], v226 offset:58880\n"
"v_mfma_f32_16x16x32_bf16 v[104:107], v[220:223], a[120:123], v[104:107]\n"
"v_mfma_f32_16x16x32_bf16 v[108:111], v[220:223], a[124:127], v[108:111]\n"
"s_add_u32 s63, %[soffH], 66560\n"
"buffer_load_dwordx4 a[188:191], %[vH], %[srdH], s63 offen\n"
"s_waitcnt lgkmcnt(2)\n"
"v_mfma_f32_16x16x32_bf16 v[112:115], v[208:211], a[112:115], v[112:115]\n"
"ds_read_b64_tr_b16 v[216:217], v227 offset:57344\n"
"v_mfma_f32_16x16x32_bf16 v[116:119], v[208:211], a[116:119], v[116:119]\n"
"ds_read_b64_tr_b16 v[218:219], v227 offset:57856\n"
"v_mfma_f32_16x16x32_bf16 v[120:123], v[208:211], a[120:123], v[120:123]\n"
"v_mfma_f32_16x16x32_bf16 v[124:127], v[208:211], a[124:127], v[124:127]\n"
"s_add_u32 m0, %[m0W], 98304\n"
"s_add_u32 s60, %[soffW], 3538944\n"
"buffer_load_dwordx4 %[vW], %[srdG], s60 offen lds\n"
"s_waitcnt lgkmcnt(2)\n"
"v_mfma_f32_16x16x32_bf16 v[128:131], v[212:215], a[112:115], v[128:131]\n"
"ds_read_b64_tr_b16 v[220:221], v227 offset:58368\n"
"v_mfma_f32_16x16x32_bf16 v[132:135], v[212:215], a[116:119], v[132:135]\n"
"ds_read_b64_tr_b16 v[222:223], v227 offset:58880\n"
"v_mfma_f32_16x16x32_bf16 v[136:139], v[212:215], a[120:123], v[136:139]\n"
"v_mfma_f32_16x16x32_bf16 v[140:143], v[212:215], a[124:127], v[140:143]\n"
"s_waitcnt vmcnt(19)\n"
"s_barrier\n"
"s_waitcnt lgkmcnt(2)\n"
"v_mfma_f32_16x16x32_bf16 v[144:147], v[216:219], a[112:115], v[144:147]\n"
"v_add_u32 v224, 65536, v224\n"
"ds_read_b64_tr_b16 v[208:209], v224 offset:0\n"
"v_mfma_f32_16x16x32_bf16 v[148:151], v[216:219], a[116:119], v[148:151]\n"
"ds_read_b64_tr_b16 v[210:211], v224 offset:512\n"
"v_mfma_f32_16x16x32_bf16 v[152:155], v[216:219], a[120:123], v[152:155]\n"
"buffer_load_dwordx4 %[vW], %[srdU], s60 offen offset:1024 lds\n"
"v_mfma_f32_16x16x32_bf16 v[156:159], v[216:219], a[124:127], v[156:159]\n"
"s_waitcnt lgkmcnt(2)\n"
"v_mfma_f32_16x16x32_bf16 v[160:163], v[220:223], a[112:115], v[160:163]\n"
"ds_read_b64_tr_b16 v[212:213], v224 offset:1024\n"
"v_mfma_f32_16x16x32_bf16 v[164:167], v[220:223], a[116:119], v[164:167]\n"
"ds_read_b64_tr_b16 v[214:215], v224 offset:1536\n"
"v_mfma_f32_16x16x32_bf16 v[168:171], v[220:223], a[120:123], v[168:171]\n"
"s_add_u32 s63, %[soffH], 12288\n"
"buffer_load_dwordx4 a[192:195], %[vH], %[srdH], s63 offen\n"
"v_mfma_f32_16x16x32_bf16 v[172:175], v[220:223], a[124:127], v[172:175]\n"
"s_waitcnt lgkmcnt(2)\n"
"v_mfma_f32_16x16x32_bf16 v[48:51], v[208:211], a[128:131], v[48:51]\n"
"v_add_u32 v225, 65536, v225\n"
"ds_read_b64_tr_b16 v[216:217], v225 offset:0\n"
"v_mfma_f32_16x16x32_bf16 v[52:55], v[208:211], a[132:135], v[52:55]\n"
"ds_read_b64_tr_b16 v[218:219], v225 offset:512\n"
"v_mfma_f32_16x16x32_bf16 v[56:59], v[208:211], a[136:139], v[56:59]\n"
"s_add_u32 s63, %[soffH], 30720\n"
"buffer_load_dwordx4 a[196:199], %[vH], %[srdH], s63 offen\n"
"v_mfma_f32_16x16x32_bf16 v[60:63], v[208:211], a[140:143], v[60:63]\n"
"s_waitcnt lgkmcnt(2)\n"
"v_mfma_f32_16x16x32_bf16 v[64:67], v[212:215], a[128:131], v[64:67]\n"
"ds_read_b64_tr_b16 v[220:221], v225 offset:1024\n"
"v_mfma_f32_16x16x32_bf16 v[68:71], v[212:215], a[132:135], v[68:71]\n"
"ds_read_b64_tr_b16 v[222:223], v225 offset:1536\n"
"v_mfma_f32_16x16x32_bf16 v[72:75], v[212:215], a[136:139], v[72:75]\n"
"s_add_u32 s63, %[soffH], 49152\n"
"buffer_load_dwordx4 a[200:203], %[vH], %[srdH], s63 offen\n"
"v_mfma_f32_16x16x32_bf16 v[76:79], v[212:215], a[140:143], v[76:79]\n"
"s_waitcnt lgkmcnt(2)\n"
"v_mfma_f32_16x16x32_bf16 v[80:83], v[216:219], a[128:131], v[80:83]\n"
"v_add_u32 v226, 65536, v226\n"
"ds_read_b64_tr_b16 v[208:209], v226 offset:0\n"
"v_mfma_f32_16x16x32_bf16 v[84:87], v[216:219], a[132:135], v[84:87]\n"
"ds_read_b64_tr_b16 v[210:211], v226 offset:512\n"
"v_mfma_f32_16x16x32_bf16 v[88:91], v[216:219], a[136:139], v[88:91]\n"
"s_add_u32 s63, %[soffH], 67584\n"
"buffer_load_dwordx4 a[204:207], %[vH], %[srdH], s63 offen\n"
"v_mfma_f32_16x16x32_bf16 v[92:95], v[216:219], a[140:143], v[92:95]\n"
"s_waitcnt lgkmcnt(2)\n"
"v_mfma_f32_16x16x32_bf16 v[96:99], v[220:223], a[128:131], v[96:99]\n"
"ds_read_b64_tr_b16 v[212:213], v226 offset:1024\n"
"v_mfma_f32_16x16x32_bf16 v[100:103], v[220:223], a[132:135], v[100:103]\n"
"ds_read_b64_tr_b16 v[214:215], v226 offset:1536\n"
"v_mfma_f32_16x16x32_bf16 v[104:107], v[220:223], a[136:139], v[104:107]\n"
"s_add_u32 m0, %[m0W], 106496\n"
"s_add_u32 s60, %[soffW], 3833856\n"
"buffer_load_dwordx4 %[vW], %[srdG], s60 offen lds\n"
"v_mfma_f32_16x16x32_bf16 v[108:111], v[220:223], a[140:143], v[108:111]\n"
"s_waitcnt lgkmcnt(2)\n"
"v_mfma_f32_16x16x32_bf16 v[112:115], v[208:211], a[128:131], v[112:115]\n"
"v_add_u32 v227, 65536, v227\n"
"ds_read_b64_tr_b16 v[216:217], v227 offset:0\n"
"v_mfma_f32_16x16x32_bf16 v[116:119], v[208:211], a[132:135], v[116:119]\n"
"ds_read_b64_tr_b16 v[218:219], v227 offset:512\n"
"v_mfma_f32_16x16x32_bf16 v[120:123], v[208:211], a[136:139], v[120:123]\n"
"v_mfma_f32_16x16x32_bf16 v[124:127], v[208:211], a[140:143], v[124:127]\n"
"buffer_load_dwordx4 %[vW], %[srdU], s60 offen offset:1024 lds\n"
"s_waitcnt lgkmcnt(2)\n"
"v_mfma_f32_16x16x32_bf16 v[128:131], v[212:215], a[128:131], v[128:131]\n"
"ds_read_b64_tr_b16 v[220:221], v227 offset:1024\n"
"v_mfma_f32_16x16x32_bf16 v[132:135], v[212:215], a[132:135], v[132:135]\n"
"ds_read_b64_tr_b16 v[222:223], v227 offset:1536\n"
"v_mfma_f32_16x16x32_bf16 v[136:139], v[212:215], a[136:139], v[136:139]\n"
"v_mfma_f32_16x16x32_bf16 v[140:143], v[212:215], a[140:143], v[140:143]\n"
"s_add_u32 s63, %[soffH], 13312\n"
"buffer_load_dwordx4 a[208:211], %[vH], %[srdH], s63 offen\n"
"s_waitcnt vmcnt(21)\n"
"s_barrier\n"
"s_waitcnt lgkmcnt(2)\n"
"v_mfma_f32_16x16x32_bf16 v[144:147], v[216:219], a[128:131], v[144:147]\n"
"ds_read_b64_tr_b16 v[208:209], v224 offset:8192\n"
"v_mfma_f32_16x16x32_bf16 v[148:151], v[216:219], a[132:135], v[148:151]\n"
"ds_read_b64_tr_b16 v[210:211], v224 offset:8704\n"
"v_mfma_f32_16x16x32_bf16 v[152:155], v[216:219], a[136:139], v[152:155]\n"
"v_mfma_f32_16x16x32_bf16 v[156:159], v[216:219], a[140:143], v[156:159]\n"
"s_waitcnt lgkmcnt(2)\n"
"v_mfma_f32_16x16x32_bf16 v[160:163], v[220:223], a[128:131], v[160:163]\n"
"ds_read_b64_tr_b16 v[212:213], v224 offset:9216\n"
"v_mfma_f32_16x16x32_bf16 v[164:167], v[220:223], a[132:135], v[164:167]\n"
"ds_read_b64_tr_b16 v[214:215], v224 offset:9728\n"
"v_mfma_f32_16x16x32_bf16 v[168:171], v[220:223], a[136:139], v[168:171]\n"
"s_add_u32 s63, %[soffH], 31744\n"
"buffer_load_dwordx4 a[212:215], %[vH], %[srdH], s63 offen\n"
"v_mfma_f32_16x16x32_bf16 v[172:175], v[220:223], a[140:143], v[172:175]\n"
"s_waitcnt lgkmcnt(2)\n"
"v_mfma_f32_16x16x32_bf16 v[48:51], v[208:211], a[144:147], v[48:51]\n"
"ds_read_b64_tr_b16 v[216:217], v225 offset:8192\n"
"v_mfma_f32_16x16x32_bf16 v[52:55], v[208:211], a[148:151], v[52:55]\n"
"ds_read_b64_tr_b16 v[218:219], v225 offset:8704\n"
"v_mfma_f32_16x16x32_bf16 v[56:59], v[208:211], a[152:155], v[56:59]\n"
"s_add_u32 s63, %[soffH], 50176\n"
"buffer_load_dwordx4 a[216:219], %[vH], %[srdH], s63 offen\n"
"v_mfma_f32_16x16x32_bf16 v[60:63], v[208:211], a[156:159], v[60:63]\n"
"s_waitcnt lgkmcnt(2)\n"
"v_mfma_f32_16x16x32_bf16 v[64:67], v[212:215], a[144:147], v[64:67]\n"
"ds_read_b64_tr_b16 v[220:221], v225 offset:9216\n"
"v_mfma_f32_16x16x32_bf16 v[68:71], v[212:215], a[148:151], v[68:71]\n"
"ds_read_b64_tr_b16 v[222:223], v225 offset:9728\n"
"v_mfma_f32_16x16x32_bf16 v[72:75], v[212:215], a[152:155], v[72:75]\n"
"s_add_u32 s63, %[soffH], 68608\n"
"buffer_load_dwordx4 a[220:223], %[vH], %[srdH], s63 offen\n"
"v_mfma_f32_16x16x32_bf16 v[76:79], v[212:215], a[156:159], v[76:79]\n"
"s_waitcnt lgkmcnt(2)\n"
"v_mfma_f32_16x16x32_bf16 v[80:83], v[216:219], a[144:147], v[80:83]\n"
"ds_read_b64_tr_b16 v[208:209], v226 offset:8192\n"
"v_mfma_f32_16x16x32_bf16 v[84:87], v[216:219], a[148:151], v[84:87]\n"
"ds_read_b64_tr_b16 v[210:211], v226 offset:8704\n"
"v_mfma_f32_16x16x32_bf16 v[88:91], v[216:219], a[152:155], v[88:91]\n"
"s_add_u32 m0, %[m0W], 114688\n"
"s_add_u32 s60, %[soffW], 4128768\n"
"buffer_load_dwordx4 %[vW], %[srdG], s60 offen lds\n"
"v_mfma_f32_16x16x32_bf16 v[92:95], v[216:219], a[156:159], v[92:95]\n"
"s_waitcnt lgkmcnt(2)\n"
"v_mfma_f32_16x16x32_bf16 v[96:99], v[220:223], a[144:147], v[96:99]\n"
"ds_read_b64_tr_b16 v[212:213], v226 offset:9216\n"
"v_mfma_f32_16x16x32_bf16 v[100:103], v[220:223], a[148:151], v[100:103]\n"
"ds_read_b64_tr_b16 v[214:215], v226 offset:9728\n"
"v_mfma_f32_16x16x32_bf16 v[104:107], v[220:223], a[152:155], v[104:107]\n"
"buffer_load_dwordx4 %[vW], %[srdU], s60 offen offset:1024 lds\n"
"v_mfma_f32_16x16x32_bf16 v[108:111], v[220:223], a[156:159], v[108:111]\n"
"s_waitcnt lgkmcnt(2)\n"
"v_mfma_f32_16x16x32_bf16 v[112:115], v[208:211], a[144:147], v[112:115]\n"
"ds_read_b64_tr_b16 v[216:217], v227 offset:8192\n"
"v_mfma_f32_16x16x32_bf16 v[116:119], v[208:211], a[148:151], v[116:119]\n"
"ds_read_b64_tr_b16 v[218:219], v227 offset:8704\n"
"v_mfma_f32_16x16x32_bf16 v[120:123], v[208:211], a[152:155], v[120:123]\n"
"s_add_u32 s63, %[soffH], 14336\n"
"buffer_load_dwordx4 a[224:227], %[vH], %[srdH], s63 offen\n"
"v_mfma_f32_16x16x32_bf16 v[124:127], v[208:211], a[156:159], v[124:127]\n"
"s_waitcnt lgkmcnt(2)\n"
"v_mfma_f32_16x16x32_bf16 v[128:131], v[212:215], a[144:147], v[128:131]\n"
"ds_read_b64_tr_b16 v[220:221], v227 offset:9216\n"
"v_mfma_f32_16x16x32_bf16 v[132:135], v[212:215], a[148:151], v[132:135]\n"
"ds_read_b64_tr_b16 v[222:223], v227 offset:9728\n"
"v_mfma_f32_16x16x32_bf16 v[136:139], v[212:215], a[152:155], v[136:139]\n"
"v_mfma_f32_16x16x32_bf16 v[140:143], v[212:215], a[156:159], v[140:143]\n"
"s_add_u32 s63, %[soffH], 32768\n"
"buffer_load_dwordx4 a[228:231], %[vH], %[srdH], s63 offen\n"
"s_waitcnt vmcnt(22)\n"
"s_barrier\n"
"s_waitcnt lgkmcnt(2)\n"
"v_mfma_f32_16x16x32_bf16 v[144:147], v[216:219], a[144:147], v[144:147]\n"
"ds_read_b64_tr_b16 v[208:209], v224 offset:16384\n"
"v_mfma_f32_16x16x32_bf16 v[148:151], v[216:219], a[148:151], v[148:151]\n"
"ds_read_b64_tr_b16 v[210:211], v224 offset:16896\n"
"v_mfma_f32_16x16x32_bf16 v[152:155], v[216:219], a[152:155], v[152:155]\n"
"v_mfma_f32_16x16x32_bf16 v[156:159], v[216:219], a[156:159], v[156:159]\n"
"s_add_u32 s63, %[soffH], 51200\n"
"buffer_load_dwordx4 a[232:235], %[vH], %[srdH], s63 offen\n"
"s_waitcnt lgkmcnt(2)\n"
"v_mfma_f32_16x16x32_bf16 v[160:163], v[220:223], a[144:147], v[160:163]\n"
"ds_read_b64_tr_b16 v[212:213], v224 offset:17408\n"
"v_mfma_f32_16x16x32_bf16 v[164:167], v[220:223], a[148:151], v[164:167]\n"
"ds_read_b64_tr_b16 v[214:215], v224 offset:17920\n"
"v_mfma_f32_16x16x32_bf16 v[168:171], v[220:223], a[152:155], v[168:171]\n"
"v_mfma_f32_16x16x32_bf16 v[172:175], v[220:223], a[156:159], v[172:175]\n"
"s_waitcnt lgkmcnt(2)\n"
"v_mfma_f32_16x16x32_bf16 v[48:51], v[208:211], a[160:163], v[48:51]\n"
"ds_read_b64_tr_b16 v[216:217], v225 offset:16384\n"
"v_mfma_f32_16x16x32_bf16 v[52:55], v[208:211], a[164:167], v[52:55]\n"
"ds_read_b64_tr_b16 v[218:219], v225 offset:16896\n"
"v_mfma_f32_16x16x32_bf16 v[56:59], v[208:211], a[168:171], v[56:59]\n"
"s_add_u32 s63, %[soffH], 69632\n"
"buffer_load_dwordx4 a[236:239], %[vH], %[srdH], s63 offen\n"
"v_mfma_f32_16x16x32_bf16 v[60:63], v[208:211], a[172:175], v[60:63]\n"
"s_waitcnt lgkmcnt(2)\n"
"v_mfma_f32_16x16x32_bf16 v[64:67], v[212:215], a[160:163], v[64:67]\n"
"ds_read_b64_tr_b16 v[220:221], v225 offset:17408\n"
"v_mfma_f32_16x16x32_bf16 v[68:71], v[212:215], a[164:167], v[68:71]\n"
"ds_read_b64_tr_b16 v[222:223], v225 offset:17920\n"
"v_mfma_f32_16x16x32_bf16 v[72:75], v[212:215], a[168:171], v[72:75]\n"
"s_add_u32 m0, %[m0W], 122880\n"
"s_add_u32 s60, %[soffW], 4423680\n"
"buffer_load_dwordx4 %[vW], %[srdG], s60 offen lds\n"
"v_mfma_f32_16x16x32_bf16 v[76:79], v[212:215], a[172:175], v[76:79]\n"
"s_waitcnt lgkmcnt(2)\n"
"v_mfma_f32_16x16x32_bf16 v[80:83], v[216:219], a[160:163], v[80:83]\n"
"ds_read_b64_tr_b16 v[208:209], v226 offset:16384\n"
"v_mfma_f32_16x16x32_bf16 v[84:87], v[216:219], a[164:167], v[84:87]\n"
"ds_read_b64_tr_b16 v[210:211], v226 offset:16896\n"
"v_mfma_f32_16x16x32_bf16 v[88:91], v[216:219], a[168:171], v[88:91]\n"
"buffer_load_dwordx4 %[vW], %[srdU], s60 offen offset:1024 lds\n"
"v_mfma_f32_16x16x32_bf16 v[92:95], v[216:219], a[172:175], v[92:95]\n"
"s_waitcnt lgkmcnt(2)\n"
"v_mfma_f32_16x16x32_bf16 v[96:99], v[220:223], a[160:163], v[96:99]\n"
"ds_read_b64_tr_b16 v[212:213], v226 offset:17408\n"
"v_mfma_f32_16x16x32_bf16 v[100:103], v[220:223], a[164:167], v[100:103]\n"
"ds_read_b64_tr_b16 v[214:215], v226 offset:17920\n"
"v_mfma_f32_16x16x32_bf16 v[104:107], v[220:223], a[168:171], v[104:107]\n"
"s_add_u32 s63, %[soffH], 15360\n"
"buffer_load_dwordx4 a[240:243], %[vH], %[srdH], s63 offen\n"
"v_mfma_f32_16x16x32_bf16 v[108:111], v[220:223], a[172:175], v[108:111]\n"
"s_waitcnt lgkmcnt(2)\n"
"v_mfma_f32_16x16x32_bf16 v[112:115], v[208:211], a[160:163], v[112:115]\n"
"ds_read_b64_tr_b16 v[216:217], v227 offset:16384\n"
"v_mfma_f32_16x16x32_bf16 v[116:119], v[208:211], a[164:167], v[116:119]\n"
"ds_read_b64_tr_b16 v[218:219], v227 offset:16896\n"
"v_mfma_f32_16x16x32_bf16 v[120:123], v[208:211], a[168:171], v[120:123]\n"
"s_add_u32 s63, %[soffH], 33792\n"
"buffer_load_dwordx4 a[244:247], %[vH], %[srdH], s63 offen\n"
"v_mfma_f32_16x16x32_bf16 v[124:127], v[208:211], a[172:175], v[124:127]\n"
"s_waitcnt lgkmcnt(2)\n"
"v_mfma_f32_16x16x32_bf16 v[128:131], v[212:215], a[160:163], v[128:131]\n"
"ds_read_b64_tr_b16 v[220:221], v227 offset:17408\n"
"v_mfma_f32_16x16x32_bf16 v[132:135], v[212:215], a[164:167], v[132:135]\n"
"ds_read_b64_tr_b16 v[222:223], v227 offset:17920\n"
"v_mfma_f32_16x16x32_bf16 v[136:139], v[212:215], a[168:171], v[136:139]\n"
"s_add_u32 s63, %[soffH], 52224\n"
"buffer_load_dwordx4 a[248:251], %[vH], %[srdH], s63 offen\n"
"v_mfma_f32_16x16x32_bf16 v[140:143], v[212:215], a[172:175], v[140:143]\n"
"s_waitcnt vmcnt(23)\n"
"s_barrier\n"
"s_waitcnt lgkmcnt(2)\n"
"v_mfma_f32_16x16x32_bf16 v[144:147], v[216:219], a[160:163], v[144:147]\n"
"ds_read_b64_tr_b16 v[208:209], v224 offset:24576\n"
"v_mfma_f32_16x16x32_bf16 v[148:151], v[216:219], a[164:167], v[148:151]\n"
"ds_read_b64_tr_b16 v[210:211], v224 offset:25088\n"
"v_mfma_f32_16x16x32_bf16 v[152:155], v[216:219], a[168:171], v[152:155]\n"
"v_mfma_f32_16x16x32_bf16 v[156:159], v[216:219], a[172:175], v[156:159]\n"
"s_add_u32 s63, %[soffH], 70656\n"
"buffer_load_dwordx4 a[252:255], %[vH], %[srdH], s63 offen\n"
"s_waitcnt lgkmcnt(2)\n"
"v_mfma_f32_16x16x32_bf16 v[160:163], v[220:223], a[160:163], v[160:163]\n"
"ds_read_b64_tr_b16 v[212:213], v224 offset:25600\n"
"v_mfma_f32_16x16x32_bf16 v[164:167], v[220:223], a[164:167], v[164:167]\n"
"ds_read_b64_tr_b16 v[214:215], v224 offset:26112\n"
"v_mfma_f32_16x16x32_bf16 v[168:171], v[220:223], a[168:171], v[168:171]\n"
"v_mfma_f32_16x16x32_bf16 v[172:175], v[220:223], a[172:175], v[172:175]\n"
"s_add_u32 m0, %[m0W], 131072\n"
"s_add_u32 s60, %[soffW], 4718592\n"
"buffer_load_dwordx4 %[vW], %[srdG], s60 offen lds\n"
"s_waitcnt lgkmcnt(2)\n"
"v_mfma_f32_16x16x32_bf16 v[48:51], v[208:211], a[176:179], v[48:51]\n"
"ds_read_b64_tr_b16 v[216:217], v225 offset:24576\n"
"v_mfma_f32_16x16x32_bf16 v[52:55], v[208:211], a[180:183], v[52:55]\n"
"ds_read_b64_tr_b16 v[218:219], v225 offset:25088\n"
"v_mfma_f32_16x16x32_bf16 v[56:59], v[208:211], a[184:187], v[56:59]\n"
"v_mfma_f32_16x16x32_bf16 v[60:63], v[208:211], a[188:191], v[60:63]\n"
"s_waitcnt lgkmcnt(2)\n"
"v_mfma_f32_16x16x32_bf16 v[64:67], v[212:215], a[176:179], v[64:67]\n"
"ds_read_b64_tr_b16 v[220:221], v225 offset:25600\n"
"v_mfma_f32_16x16x32_bf16 v[68:71], v[212:215], a[180:183], v[68:71]\n"
"ds_read_b64_tr_b16 v[222:223], v225 offset:26112\n"
"v_mfma_f32_16x16x32_bf16 v[72:75], v[212:215], a[184:187], v[72:75]\n"
"buffer_load_dwordx4 %[vW], %[srdU], s60 offen offset:1024 lds\n"
"v_mfma_f32_16x16x32_bf16 v[76:79], v[212:215], a[188:191], v[76:79]\n"
"s_waitcnt lgkmcnt(2)\n"
"v_mfma_f32_16x16x32_bf16 v[80:83], v[216:219], a[176:179], v[80:83]\n"
"ds_read_b64_tr_b16 v[208:209], v226 offset:24576\n"
"v_mfma_f32_16x16x32_bf16 v[84:87], v[216:219], a[180:183], v[84:87]\n"
"ds_read_b64_tr_b16 v[210:211], v226 offset:25088\n"
"v_mfma_f32_16x16x32_bf16 v[88:91], v[216:219], a[184:187], v[88:91]\n"
"s_add_u32 s63, %[soffH], 16384\n"
"buffer_load_dwordx4 v[16:19], %[vH], %[srdH], s63 offen\n"
"v_mfma_f32_16x16x32_bf16 v[92:95], v[216:219], a[188:191], v[92:95]\n"
"s_waitcnt lgkmcnt(2)\n"
"v_mfma_f32_16x16x32_bf16 v[96:99], v[220:223], a[176:179], v[96:99]\n"
"ds_read_b64_tr_b16 v[212:213], v226 offset:25600\n"
"v_mfma_f32_16x16x32_bf16 v[100:103], v[220:223], a[180:183], v[100:103]\n"
"ds_read_b64_tr_b16 v[214:215], v226 offset:26112\n"
"v_mfma_f32_16x16x32_bf16 v[104:107], v[220:223], a[184:187], v[104:107]\n"
"s_add_u32 s63, %[soffH], 34816\n"
"buffer_load_dwordx4 v[20:23], %[vH], %[srdH], s63 offen\n"
"v_mfma_f32_16x16x32_bf16 v[108:111], v[220:223], a[188:191], v[108:111]\n"
"s_waitcnt lgkmcnt(2)\n"
"v_mfma_f32_16x16x32_bf16 v[112:115], v[208:211], a[176:179], v[112:115]\n"
"ds_read_b64_tr_b16 v[216:217], v227 offset:24576\n"
"v_mfma_f32_16x16x32_bf16 v[116:119], v[208:211], a[180:183], v[116:119]\n"
"ds_read_b64_tr_b16 v[218:219], v227 offset:25088\n"
"v_mfma_f32_16x16x32_bf16 v[120:123], v[208:211], a[184:187], v[120:123]\n"
"s_add_u32 s63, %[soffH], 53248\n"
"buffer_load_dwordx4 v[24:27], %[vH], %[srdH], s63 offen\n"
"v_mfma_f32_16x16x32_bf16 v[124:127], v[208:211], a[188:191], v[124:127]\n"
"s_waitcnt lgkmcnt(2)\n"
"v_mfma_f32_16x16x32_bf16 v[128:131], v[212:215], a[176:179], v[128:131]\n"
"ds_read_b64_tr_b16 v[220:221], v227 offset:25600\n"
"v_mfma_f32_16x16x32_bf16 v[132:135], v[212:215], a[180:183], v[132:135]\n"
"ds_read_b64_tr_b16 v[222:223], v227 offset:26112\n"
"v_mfma_f32_16x16x32_bf16 v[136:139], v[212:215], a[184:187], v[136:139]\n"
"s_add_u32 s63, %[soffH], 71680\n"
"buffer_load_dwordx4 v[28:31], %[vH], %[srdH], s63 offen\n"
"v_mfma_f32_16x16x32_bf16 v[140:143], v[212:215], a[188:191], v[140:143]\n"
"s_waitcnt vmcnt(24)\n"
"s_barrier\n"
"s_waitcnt lgkmcnt(2)\n"
"v_mfma_f32_16x16x32_bf16 v[144:147], v[216:219], a[176:179], v[144:147]\n"
"ds_read_b64_tr_b16 v[208:209], v224 offset:32768\n"
"v_mfma_f32_16x16x32_bf16 v[148:151], v[216:219], a[180:183], v[148:151]\n"
"ds_read_b64_tr_b16 v[210:211], v224 offset:33280\n"
"v_mfma_f32_16x16x32_bf16 v[152:155], v[216:219], a[184:187], v[152:155]\n"
"s_add_u32 m0, %[m0W], 139264\n"
"s_add_u32 s60, %[soffW], 5013504\n"
"buffer_load_dwordx4 %[vW], %[srdG], s60 offen lds\n"
"v_mfma_f32_16x16x32_bf16 v[156:159], v[216:219], a[188:191], v[156:159]\n"
"s_waitcnt lgkmcnt(2)\n"
"v_mfma_f32_16x16x32_bf16 v[160:163], v[220:223], a[176:179], v[160:163]\n"
"ds_read_b64_tr_b16 v[212:213], v224 offset:33792\n"
"v_mfma_f32_16x16x32_bf16 v[164:167], v[220:223], a[180:183], v[164:167]\n"
"ds_read_b64_tr_b16 v[214:215], v224 offset:34304\n"
"v_mfma_f32_16x16x32_bf16 v[168:171], v[220:223], a[184:187], v[168:171]\n"
"v_mfma_f32_16x16x32_bf16 v[172:175], v[220:223], a[188:191], v[172:175]\n"
"buffer_load_dwordx4 %[vW], %[srdU], s60 offen offset:1024 lds\n"
"s_waitcnt lgkmcnt(2)\n"
"v_mfma_f32_16x16x32_bf16 v[48:51], v[208:211], a[192:195], v[48:51]\n"
"ds_read_b64_tr_b16 v[216:217], v225 offset:32768\n"
"v_mfma_f32_16x16x32_bf16 v[52:55], v[208:211], a[196:199], v[52:55]\n"
"ds_read_b64_tr_b16 v[218:219], v225 offset:33280\n"
"v_mfma_f32_16x16x32_bf16 v[56:59], v[208:211], a[200:203], v[56:59]\n"
"v_mfma_f32_16x16x32_bf16 v[60:63], v[208:211], a[204:207], v[60:63]\n"
"s_add_u32 s63, %[soffH], 17408\n"
"buffer_load_dwordx4 v[32:35], %[vH], %[srdH], s63 offen\n"
"s_waitcnt lgkmcnt(2)\n"
"v_mfma_f32_16x16x32_bf16 v[64:67], v[212:215], a[192:195], v[64:67]\n"
"ds_read_b64_tr_b16 v[220:221], v225 offset:33792\n"
"v_mfma_f32_16x16x32_bf16 v[68:71], v[212:215], a[196:199], v[68:71]\n"
"ds_read_b64_tr_b16 v[222:223], v225 offset:34304\n"
"v_mfma_f32_16x16x32_bf16 v[72:75], v[212:215], a[200:203], v[72:75]\n"
"v_mfma_f32_16x16x32_bf16 v[76:79], v[212:215], a[204:207], v[76:79]\n"
"s_waitcnt lgkmcnt(2)\n"
"v_mfma_f32_16x16x32_bf16 v[80:83], v[216:219], a[192:195], v[80:83]\n"
"ds_read_b64_tr_b16 v[208:209], v226 offset:32768\n"
"v_mfma_f32_16x16x32_bf16 v[84:87], v[216:219], a[196:199], v[84:87]\n"
"ds_read_b64_tr_b16 v[210:211], v226 offset:33280\n"
"v_mfma_f32_16x16x32_bf16 v[88:91], v[216:219], a[200:203], v[88:91]\n"
"s_add_u32 s63, %[soffH], 35840\n"
"buffer_load_dwordx4 v[36:39], %[vH], %[srdH], s63 offen\n"
"v_mfma_f32_16x16x32_bf16 v[92:95], v[216:219], a[204:207], v[92:95]\n"
"s_waitcnt lgkmcnt(2)\n"
"v_mfma_f32_16x16x32_bf16 v[96:99], v[220:223], a[192:195], v[96:99]\n"
"ds_read_b64_tr_b16 v[212:213], v226 offset:33792\n"
"v_mfma_f32_16x16x32_bf16 v[100:103], v[220:223], a[196:199], v[100:103]\n"
"ds_read_b64_tr_b16 v[214:215], v226 offset:34304\n"
"v_mfma_f32_16x16x32_bf16 v[104:107], v[220:223], a[200:203], v[104:107]\n"
"s_add_u32 s63, %[soffH], 54272\n"
"buffer_load_dwordx4 v[40:43], %[vH], %[srdH], s63 offen\n"
"v_mfma_f32_16x16x32_bf16 v[108:111], v[220:223], a[204:207], v[108:111]\n"
"s_waitcnt lgkmcnt(2)\n"
"v_mfma_f32_16x16x32_bf16 v[112:115], v[208:211], a[192:195], v[112:115]\n"
"ds_read_b64_tr_b16 v[216:217], v227 offset:32768\n"
"v_mfma_f32_16x16x32_bf16 v[116:119], v[208:211], a[196:199], v[116:119]\n"
"ds_read_b64_tr_b16 v[218:219], v227 offset:33280\n"
"v_mfma_f32_16x16x32_bf16 v[120:123], v[208:211], a[200:203], v[120:123]\n"
"s_add_u32 s63, %[soffH], 72704\n"
"buffer_load_dwordx4 v[44:47], %[vH], %[srdH], s63 offen\n"
"v_mfma_f32_16x16x32_bf16 v[124:127], v[208:211], a[204:207], v[124:127]\n"
"s_waitcnt lgkmcnt(2)\n"
"v_mfma_f32_16x16x32_bf16 v[128:131], v[212:215], a[192:195], v[128:131]\n"
"ds_read_b64_tr_b16 v[220:221], v227 offset:33792\n"
"v_mfma_f32_16x16x32_bf16 v[132:135], v[212:215], a[196:199], v[132:135]\n"
"ds_read_b64_tr_b16 v[222:223], v227 offset:34304\n"
"v_mfma_f32_16x16x32_bf16 v[136:139], v[212:215], a[200:203], v[136:139]\n"
"s_add_u32 m0, %[m0W], 0\n"
"s_add_u32 s60, %[soffW], 128\n"
"buffer_load_dwordx4 %[vW], %[srdG], s60 offen lds\n"
"v_mfma_f32_16x16x32_bf16 v[140:143], v[212:215], a[204:207], v[140:143]\n"
"s_waitcnt vmcnt(1)\n"
"s_barrier\n"
"s_waitcnt lgkmcnt(2)\n"
"v_mfma_f32_16x16x32_bf16 v[144:147], v[216:219], a[192:195], v[144:147]\n"
"ds_read_b64_tr_b16 v[208:209], v224 offset:40960\n"
"v_mfma_f32_16x16x32_bf16 v[148:151], v[216:219], a[196:199], v[148:151]\n"
"ds_read_b64_tr_b16 v[210:211], v224 offset:41472\n"
"v_mfma_f32_16x16x32_bf16 v[152:155], v[216:219], a[200:203], v[152:155]\n"
"buffer_load_dwordx4 %[vW], %[srdU], s60 offen offset:1024 lds\n"
"v_mfma_f32_16x16x32_bf16 v[156:159], v[216:219], a[204:207], v[156:159]\n"
"s_waitcnt lgkmcnt(2)\n"
"v_mfma_f32_16x16x32_bf16 v[160:163], v[220:223], a[192:195], v[160:163]\n"
"ds_read_b64_tr_b16 v[212:213], v224 offset:41984\n"
"v_mfma_f32_16x16x32_bf16 v[164:167], v[220:223], a[196:199], v[164:167]\n"
"ds_read_b64_tr_b16 v[214:215], v224 offset:42496\n"
"v_mfma_f32_16x16x32_bf16 v[168:171], v[220:223], a[200:203], v[168:171]\n"
"s_add_u32 m0, %[m0W], 8192\n"
"s_add_u32 s60, %[soffW], 295040\n"
"buffer_load_dwordx4 %[vW], %[srdG], s60 offen lds\n"
"v_mfma_f32_16x16x32_bf16 v[172:175], v[220:223], a[204:207], v[172:175]\n"
"s_waitcnt lgkmcnt(2)\n"
"v_mfma_f32_16x16x32_bf16 v[48:51], v[208:211], a[208:211], v[48:51]\n"
"ds_read_b64_tr_b16 v[216:217], v224 offset:49152\n"
"v_mfma_f32_16x16x32_bf16 v[52:55], v[208:211], a[212:215], v[52:55]\n"
"ds_read_b64_tr_b16 v[218:219], v224 offset:49664\n"
"v_mfma_f32_16x16x32_bf16 v[56:59], v[208:211], a[216:219], v[56:59]\n"
"v_mfma_f32_16x16x32_bf16 v[60:63], v[208:211], a[220:223], v[60:63]\n"
"buffer_load_dwordx4 %[vW], %[srdU], s60 offen offset:1024 lds\n"
"s_waitcnt lgkmcnt(2)\n"
"v_mfma_f32_16x16x32_bf16 v[64:67], v[212:215], a[208:211], v[64:67]\n"
"ds_read_b64_tr_b16 v[220:221], v224 offset:50176\n"
"v_mfma_f32_16x16x32_bf16 v[68:71], v[212:215], a[212:215], v[68:71]\n"
"ds_read_b64_tr_b16 v[222:223], v224 offset:50688\n"
"v_mfma_f32_16x16x32_bf16 v[72:75], v[212:215], a[216:219], v[72:75]\n"
"v_mfma_f32_16x16x32_bf16 v[76:79], v[212:215], a[220:223], v[76:79]\n"
"s_add_u32 m0, %[m0W], 16384\n"
"s_add_u32 s60, %[soffW], 589952\n"
"buffer_load_dwordx4 %[vW], %[srdG], s60 offen lds\n"
"s_waitcnt lgkmcnt(2)\n"
"v_mfma_f32_16x16x32_bf16 v[48:51], v[216:219], a[224:227], v[48:51]\n"
"ds_read_b64_tr_b16 v[208:209], v224 offset:57344\n"
"v_mfma_f32_16x16x32_bf16 v[52:55], v[216:219], a[228:231], v[52:55]\n"
"ds_read_b64_tr_b16 v[210:211], v224 offset:57856\n"
"v_mfma_f32_16x16x32_bf16 v[56:59], v[216:219], a[232:235], v[56:59]\n"
"v_mfma_f32_16x16x32_bf16 v[60:63], v[216:219], a[236:239], v[60:63]\n"
"s_waitcnt lgkmcnt(2)\n"
"v_mfma_f32_16x16x32_bf16 v[64:67], v[220:223], a[224:227], v[64:67]\n"
"ds_read_b64_tr_b16 v[212:213], v224 offset:58368\n"
"v_mfma_f32_16x16x32_bf16 v[68:71], v[220:223], a[228:231], v[68:71]\n"
"ds_read_b64_tr_b16 v[214:215], v224 offset:58880\n"
"v_mfma_f32_16x16x32_bf16 v[72:75], v[220:223], a[232:235], v[72:75]\n"
"buffer_load_dwordx4 %[vW], %[srdU], s60 offen offset:1024 lds\n"
"v_mfma_f32_16x16x32_bf16 v[76:79], v[220:223], a[236:239], v[76:79]\n"
"s_waitcnt lgkmcnt(2)\n"
"v_mfma_f32_16x16x32_bf16 v[48:51], v[208:211], a[240:243], v[48:51]\n"
"v_add_u32 v224, 65536, v224\n"
"ds_read_b64_tr_b16 v[216:217], v224 offset:0\n"
"v_mfma_f32_16x16x32_bf16 v[52:55], v[208:211], a[244:247], v[52:55]\n"
"ds_read_b64_tr_b16 v[218:219], v224 offset:512\n"
"v_mfma_f32_16x16x32_bf16 v[56:59], v[208:211], a[248:251], v[56:59]\n"
"s_add_u32 m0, %[m0W], 24576\n"
"s_add_u32 s60, %[soffW], 884864\n"
"buffer_load_dwordx4 %[vW], %[srdG], s60 offen lds\n"
"v_mfma_f32_16x16x32_bf16 v[60:63], v[208:211], a[252:255], v[60:63]\n"
"s_waitcnt lgkmcnt(2)\n"
"v_mfma_f32_16x16x32_bf16 v[64:67], v[212:215], a[240:243], v[64:67]\n"
"ds_read_b64_tr_b16 v[220:221], v224 offset:1024\n"
"v_mfma_f32_16x16x32_bf16 v[68:71], v[212:215], a[244:247], v[68:71]\n"
"ds_read_b64_tr_b16 v[222:223], v224 offset:1536\n"
"v_mfma_f32_16x16x32_bf16 v[72:75], v[212:215], a[248:251], v[72:75]\n"
"buffer_load_dwordx4 %[vW], %[srdU], s60 offen offset:1024 lds\n"
"v_mfma_f32_16x16x32_bf16 v[76:79], v[212:215], a[252:255], v[76:79]\n"
"s_waitcnt lgkmcnt(2)\n"
"v_mfma_f32_16x16x32_bf16 v[48:51], v[216:219], v[16:19], v[48:51]\n"
"ds_read_b64_tr_b16 v[208:209], v224 offset:8192\n"
"v_mfma_f32_16x16x32_bf16 v[52:55], v[216:219], v[20:23], v[52:55]\n"
"ds_read_b64_tr_b16 v[210:211], v224 offset:8704\n"
"v_mfma_f32_16x16x32_bf16 v[56:59], v[216:219], v[24:27], v[56:59]\n"
"s_add_u32 m0, %[m0W], 32768\n"
"s_add_u32 s60, %[soffW], 1179776\n"
"buffer_load_dwordx4 %[vW], %[srdG], s60 offen lds\n"
"v_mfma_f32_16x16x32_bf16 v[60:63], v[216:219], v[28:31], v[60:63]\n"
"s_waitcnt lgkmcnt(2)\n"
"v_mfma_f32_16x16x32_bf16 v[64:67], v[220:223], v[16:19], v[64:67]\n"
"ds_read_b64_tr_b16 v[212:213], v224 offset:9216\n"
"v_mfma_f32_16x16x32_bf16 v[68:71], v[220:223], v[20:23], v[68:71]\n"
"ds_read_b64_tr_b16 v[214:215], v224 offset:9728\n"
"v_mfma_f32_16x16x32_bf16 v[72:75], v[220:223], v[24:27], v[72:75]\n"
"buffer_load_dwordx4 %[vW], %[srdU], s60 offen offset:1024 lds\n"
"v_mfma_f32_16x16x32_bf16 v[76:79], v[220:223], v[28:31], v[76:79]\n"
"s_waitcnt lgkmcnt(2)\n"
"v_mfma_f32_16x16x32_bf16 v[48:51], v[208:211], v[32:35], v[48:51]\n"
"ds_read_b64_tr_b16 v[216:217], v225 offset:40960\n"
"v_mfma_f32_16x16x32_bf16 v[52:55], v[208:211], v[36:39], v[52:55]\n"
"ds_read_b64_tr_b16 v[218:219], v225 offset:41472\n"
"v_mfma_f32_16x16x32_bf16 v[56:59], v[208:211], v[40:43], v[56:59]\n"
"s_add_u32 m0, %[m0W], 40960\n"
"s_add_u32 s60, %[soffW], 1474688\n"
"buffer_load_dwordx4 %[vW], %[srdG], s60 offen lds\n"
"v_mfma_f32_16x16x32_bf16 v[60:63], v[208:211], v[44:47], v[60:63]\n"
"s_waitcnt lgkmcnt(2)\n"
"v_mfma_f32_16x16x32_bf16 v[64:67], v[212:215], v[32:35], v[64:67]\n"
"ds_read_b64_tr_b16 v[220:221], v225 offset:41984\n"
"v_mfma_f32_16x16x32_bf16 v[68:71], v[212:215], v[36:39], v[68:71]\n"
"ds_read_b64_tr_b16 v[222:223], v225 offset:42496\n"
"v_mfma_f32_16x16x32_bf16 v[72:75], v[212:215], v[40:43], v[72:75]\n"
"v_mfma_f32_16x16x32_bf16 v[76:79], v[212:215], v[44:47], v[76:79]\n"
"buffer_load_dwordx4 %[vW], %[srdU], s60 offen offset:1024 lds\n"
"s_waitcnt lgkmcnt(2)\n"
"v_mfma_f32_16x16x32_bf16 v[80:83], v[216:219], a[208:211], v[80:83]\n"
"ds_read_b64_tr_b16 v[208:209], v225 offset:49152\n"
"v_mfma_f32_16x16x32_bf16 v[84:87], v[216:219], a[212:215], v[84:87]\n"
"ds_read_b64_tr_b16 v[210:211], v225 offset:49664\n"
"v_mul_f32 v236, s61, v48\n"
"v_mfma_f32_16x16x32_bf16 v[88:91], v[216:219], a[216:219], v[88:91]\n"
"v_exp_f32 v236, v236\n"
"v_mfma_f32_16x16x32_bf16 v[92:95], v[216:219], a[220:223], v[92:95]\n"
"s_add_u32 m0, %[m0W], 49152\n"
"s_add_u32 s60, %[soffW], 1769600\n"
"buffer_load_dwordx4 %[vW], %[srdG], s60 offen lds\n"
"s_waitcnt lgkmcnt(2)\n"
"v_mfma_f32_16x16x32_bf16 v[96:99], v[220:223], a[208:211], v[96:99]\n"
"ds_read_b64_tr_b16 v[212:213], v225 offset:50176\n"
"v_add_f32 v236, 1.0, v236\n"
"v_mfma_f32_16x16x32_bf16 v[100:103], v[220:223], a[212:215], v[100:103]\n"
"ds_read_b64_tr_b16 v[214:215], v225 offset:50688\n"
"v_mul_f32 v64, v48, v64\n"
"v_mfma_f32_16x16x32_bf16 v[104:107], v[220:223], a[216:219], v[104:107]\n"
"v_rcp_f32 v236, v236\n"
"v_mfma_f32_16x16x32_bf16 v[108:111], v[220:223], a[220:223], v[108:111]\n"
"v_mul_f32 v64, v64, v236\n"
"v_mul_f32 v237, s61, v49\n"
"s_waitcnt lgkmcnt(2)\n"
"v_mfma_f32_16x16x32_bf16 v[80:83], v[208:211], a[224:227], v[80:83]\n"
"ds_read_b64_tr_b16 v[216:217], v225 offset:57344\n"
"v_mul_f32 v65, v49, v65\n"
"v_mfma_f32_16x16x32_bf16 v[84:87], v[208:211], a[228:231], v[84:87]\n"
"ds_read_b64_tr_b16 v[218:219], v225 offset:57856\n"
"v_mul_f32 v238, s61, v50\n"
"v_mfma_f32_16x16x32_bf16 v[88:91], v[208:211], a[232:235], v[88:91]\n"
"buffer_load_dwordx4 %[vW], %[srdU], s60 offen offset:1024 lds\n"
"v_mfma_f32_16x16x32_bf16 v[92:95], v[208:211], a[236:239], v[92:95]\n"
"v_exp_f32 v237, v237\n"
"s_waitcnt lgkmcnt(2)\n"
"v_mfma_f32_16x16x32_bf16 v[96:99], v[212:215], a[224:227], v[96:99]\n"
"ds_read_b64_tr_b16 v[220:221], v225 offset:58368\n"
"v_add_f32 v237, 1.0, v237\n"
"v_mfma_f32_16x16x32_bf16 v[100:103], v[212:215], a[228:231], v[100:103]\n"
"ds_read_b64_tr_b16 v[222:223], v225 offset:58880\n"
"v_mul_f32 v66, v50, v66\n"
"v_mfma_f32_16x16x32_bf16 v[104:107], v[212:215], a[232:235], v[104:107]\n"
"s_add_u32 m0, %[m0W], 57344\n"
"s_add_u32 s60, %[soffW], 2064512\n"
"buffer_load_dwordx4 %[vW], %[srdG], s60 offen lds\n"
"v_mfma_f32_16x16x32_bf16 v[108:111], v[212:215], a[236:239], v[108:111]\n"
"v_rcp_f32 v237, v237\n"
"s_waitcnt lgkmcnt(2)\n"
"v_mfma_f32_16x16x32_bf16 v[80:83], v[216:219], a[240:243], v[80:83]\n"
"v_add_u32 v225, 65536, v225\n"
"ds_read_b64_tr_b16 v[208:209], v225 offset:0\n"
"v_mfma_f32_16x16x32_bf16 v[84:87], v[216:219], a[244:247], v[84:87]\n"
"ds_read_b64_tr_b16 v[210:211], v225 offset:512\n"
"v_mul_f32 v65, v65, v237\n"
"v_mfma_f32_16x16x32_bf16 v[88:91], v[216:219], a[248:251], v[88:91]\n"
"buffer_load_dwordx4 %[vW], %[srdU], s60 offen offset:1024 lds\n"
"v_mfma_f32_16x16x32_bf16 v[92:95], v[216:219], a[252:255], v[92:95]\n"
"v_exp_f32 v238, v238\n"
"s_waitcnt lgkmcnt(2)\n"
"v_mfma_f32_16x16x32_bf16 v[96:99], v[220:223], a[240:243], v[96:99]\n"
"ds_read_b64_tr_b16 v[212:213], v225 offset:1024\n"
"v_add_f32 v238, 1.0, v238\n"
"v_mfma_f32_16x16x32_bf16 v[100:103], v[220:223], a[244:247], v[100:103]\n"
"ds_read_b64_tr_b16 v[214:215], v225 offset:1536\n"
"v_mul_f32 v239, s61, v51\n"
"v_mfma_f32_16x16x32_bf16 v[104:107], v[220:223], a[248:251], v[104:107]\n"
"s_add_u32 m0, %[m0W], 65536\n"
"s_add_u32 s60, %[soffW], 2359424\n"
"buffer_load_dwordx4 %[vW], %[srdG], s60 offen lds\n"
"v_mfma_f32_16x16x32_bf16 v[108:111], v[220:223], a[252:255], v[108:111]\n"
"v_rcp_f32 v238, v238\n"
"s_waitcnt lgkmcnt(2)\n"
"v_mfma_f32_16x16x32_bf16 v[80:83], v[208:211], v[16:19], v[80:83]\n"
"ds_read_b64_tr_b16 v[216:217], v225 offset:8192\n"
"v_mul_f32 v66, v66, v238\n"
"v_mfma_f32_16x16x32_bf16 v[84:87], v[208:211], v[20:23], v[84:87]\n"
"ds_read_b64_tr_b16 v[218:219], v225 offset:8704\n"
"v_mul_f32 v67, v51, v67\n"
"v_mfma_f32_16x16x32_bf16 v[88:91], v[208:211], v[24:27], v[88:91]\n"
"buffer_load_dwordx4 %[vW], %[srdU], s60 offen offset:1024 lds\n"
"v_mfma_f32_16x16x32_bf16 v[92:95], v[208:211], v[28:31], v[92:95]\n"
"v_exp_f32 v239, v239\n"
"s_waitcnt lgkmcnt(2)\n"
"v_mfma_f32_16x16x32_bf16 v[96:99], v[212:215], v[16:19], v[96:99]\n"
"ds_read_b64_tr_b16 v[220:221], v225 offset:9216\n"
"v_add_f32 v239, 1.0, v239\n"
"v_mfma_f32_16x16x32_bf16 v[100:103], v[212:215], v[20:23], v[100:103]\n"
"ds_read_b64_tr_b16 v[222:223], v225 offset:9728\n"
"v_cvt_pk_bf16_f32 v228, v64, v65\n"
"v_mfma_f32_16x16x32_bf16 v[104:107], v[212:215], v[24:27], v[104:107]\n"
"v_rcp_f32 v239, v239\n"
"v_mfma_f32_16x16x32_bf16 v[108:111], v[212:215], v[28:31], v[108:111]\n"
"v_mul_f32 v67, v67, v239\n"
"v_mul_f32 v240, s61, v52\n"
"s_waitcnt lgkmcnt(2)\n"
"v_mfma_f32_16x16x32_bf16 v[80:83], v[216:219], v[32:35], v[80:83]\n"
"ds_read_b64_tr_b16 v[208:209], v226 offset:40960\n"
"v_cvt_pk_bf16_f32 v229, v66, v67\n"
"v_mfma_f32_16x16x32_bf16 v[84:87], v[216:219], v[36:39], v[84:87]\n"
"ds_read_b64_tr_b16 v[210:211], v226 offset:41472\n"
"v_mul_f32 v68, v52, v68\n"
"v_mfma_f32_16x16x32_bf16 v[88:91], v[216:219], v[40:43], v[88:91]\n"
"v_exp_f32 v240, v240\n"
"v_mfma_f32_16x16x32_bf16 v[92:95], v[216:219], v[44:47], v[92:95]\n"
"v_add_f32 v240, 1.0, v240\n"
"v_mul_f32 v241, s61, v53\n"
"s_waitcnt lgkmcnt(2)\n"
"v_mfma_f32_16x16x32_bf16 v[96:99], v[220:223], v[32:35], v[96:99]\n"
"ds_read_b64_tr_b16 v[212:213], v226 offset:41984\n"
"v_mul_f32 v69, v53, v69\n"
"v_mfma_f32_16x16x32_bf16 v[100:103], v[220:223], v[36:39], v[100:103]\n"
"ds_read_b64_tr_b16 v[214:215], v226 offset:42496\n"
"v_mul_f32 v242, s61, v54\n"
"v_mfma_f32_16x16x32_bf16 v[104:107], v[220:223], v[40:43], v[104:107]\n"
"v_rcp_f32 v240, v240\n"
"v_mfma_f32_16x16x32_bf16 v[108:111], v[220:223], v[44:47], v[108:111]\n"
"v_exp_f32 v241, v241\n"
"s_waitcnt lgkmcnt(2)\n"
"v_mfma_f32_16x16x32_bf16 v[112:115], v[208:211], a[208:211], v[112:115]\n"
"ds_read_b64_tr_b16 v[216:217], v226 offset:49152\n"
"v_mul_f32 v68, v68, v240\n"
"v_mfma_f32_16x16x32_bf16 v[116:119], v[208:211], a[212:215], v[116:119]\n"
"ds_read_b64_tr_b16 v[218:219], v226 offset:49664\n"
"v_add_f32 v241, 1.0, v241\n"
"v_mfma_f32_16x16x32_bf16 v[120:123], v[208:211], a[216:219], v[120:123]\n"
"v_rcp_f32 v241, v241\n"
"v_mfma_f32_16x16x32_bf16 v[124:127], v[208:211], a[220:223], v[124:127]\n"
"v_exp_f32 v242, v242\n"
"s_waitcnt lgkmcnt(2)\n"
"v_mfma_f32_16x16x32_bf16 v[128:131], v[212:215], a[208:211], v[128:131]\n"
"ds_read_b64_tr_b16 v[220:221], v226 offset:50176\n"
"v_mul_f32 v69, v69, v241\n"
"v_mfma_f32_16x16x32_bf16 v[132:135], v[212:215], a[212:215], v[132:135]\n"
"ds_read_b64_tr_b16 v[222:223], v226 offset:50688\n"
"v_add_f32 v242, 1.0, v242\n"
"v_mfma_f32_16x16x32_bf16 v[136:139], v[212:215], a[216:219], v[136:139]\n"
"v_rcp_f32 v242, v242\n"
"v_mfma_f32_16x16x32_bf16 v[140:143], v[212:215], a[220:223], v[140:143]\n"
"v_mul_f32 v70, v54, v70\n"
"v_mul_f32 v243, s61, v55\n"
"s_waitcnt lgkmcnt(2)\n"
"v_mfma_f32_16x16x32_bf16 v[112:115], v[216:219], a[224:227], v[112:115]\n"
"ds_read_b64_tr_b16 v[208:209], v226 offset:57344\n"
"v_mul_f32 v70, v70, v242\n"
"v_mfma_f32_16x16x32_bf16 v[116:119], v[216:219], a[228:231], v[116:119]\n"
"ds_read_b64_tr_b16 v[210:211], v226 offset:57856\n"
"v_mul_f32 v71, v55, v71\n"
"v_mfma_f32_16x16x32_bf16 v[120:123], v[216:219], a[232:235], v[120:123]\n"
"v_exp_f32 v243, v243\n"
"v_mfma_f32_16x16x32_bf16 v[124:127], v[216:219], a[236:239], v[124:127]\n"
"v_add_f32 v243, 1.0, v243\n"
"v_cvt_pk_bf16_f32 v230, v68, v69\n"
"s_waitcnt lgkmcnt(2)\n"
"v_mfma_f32_16x16x32_bf16 v[128:131], v[220:223], a[224:227], v[128:131]\n"
"ds_read_b64_tr_b16 v[212:213], v226 offset:58368\n"
"v_mul_f32 v236, s61, v56\n"
"v_mfma_f32_16x16x32_bf16 v[132:135], v[220:223], a[228:231], v[132:135]\n"
"ds_read_b64_tr_b16 v[214:215], v226 offset:58880\n"
"v_mul_f32 v72, v56, v72\n"
"v_mfma_f32_16x16x32_bf16 v[136:139], v[220:223], a[232:235], v[136:139]\n"
"v_rcp_f32 v243, v243\n"
"v_mfma_f32_16x16x32_bf16 v[140:143], v[220:223], a[236:239], v[140:143]\n"
"v_exp_f32 v236, v236\n"
"s_waitcnt lgkmcnt(2)\n"
"v_mfma_f32_16x16x32_bf16 v[112:115], v[208:211], a[240:243], v[112:115]\n"
"v_add_u32 v226, 65536, v226\n"
"ds_read_b64_tr_b16 v[216:217], v226 offset:0\n"
"v_mfma_f32_16x16x32_bf16 v[116:119], v[208:211], a[244:247], v[116:119]\n"
"ds_read_b64_tr_b16 v[218:219], v226 offset:512\n"
"v_mul_f32 v71, v71, v243\n"
"v_mfma_f32_16x16x32_bf16 v[120:123], v[208:211], a[248:251], v[120:123]\n"
"v_cvt_pk_bf16_f32 v231, v70, v71\n"
"v_add_f32 v236, 1.0, v236\n"
"v_mfma_f32_16x16x32_bf16 v[124:127], v[208:211], a[252:255], v[124:127]\n"
"v_rcp_f32 v236, v236\n"
"s_waitcnt lgkmcnt(2)\n"
"v_mfma_f32_16x16x32_bf16 v[128:131], v[212:215], a[240:243], v[128:131]\n"
"ds_read_b64_tr_b16 v[220:221], v226 offset:1024\n"
"v_permlane16_swap_b32 v228, v230\n"
"v_mfma_f32_16x16x32_bf16 v[132:135], v[212:215], a[244:247], v[132:135]\n"
"ds_read_b64_tr_b16 v[222:223], v226 offset:1536\n"
"v_permlane16_swap_b32 v229, v231\n"
"v_mfma_f32_16x16x32_bf16 v[136:139], v[212:215], a[248:251], v[136:139]\n"
"s_add_u32 s62, %[soffA], 0\n"
"buffer_store_dwordx4 v[228:231], %[vS], %[srdA], s62 offen sc1\n"
"v_mfma_f32_16x16x32_bf16 v[140:143], v[212:215], a[252:255], v[140:143]\n"
"v_mul_f32 v72, v72, v236\n"
"v_mul_f32 v237, s61, v57\n"
"s_waitcnt lgkmcnt(2)\n"
"v_mfma_f32_16x16x32_bf16 v[112:115], v[216:219], v[16:19], v[112:115]\n"
"ds_read_b64_tr_b16 v[208:209], v226 offset:8192\n"
"v_mul_f32 v73, v57, v73\n"
"v_mfma_f32_16x16x32_bf16 v[116:119], v[216:219], v[20:23], v[116:119]\n"
"ds_read_b64_tr_b16 v[210:211], v226 offset:8704\n"
"v_mul_f32 v238, s61, v58\n"
"v_mfma_f32_16x16x32_bf16 v[120:123], v[216:219], v[24:27], v[120:123]\n"
"v_exp_f32 v237, v237\n"
"v_mfma_f32_16x16x32_bf16 v[124:127], v[216:219], v[28:31], v[124:127]\n"
"v_exp_f32 v238, v238\n"
"s_waitcnt lgkmcnt(2)\n"
"v_mfma_f32_16x16x32_bf16 v[128:131], v[220:223], v[16:19], v[128:131]\n"
"ds_read_b64_tr_b16 v[212:213], v226 offset:9216\n"
"v_add_f32 v237, 1.0, v237\n"
"v_mfma_f32_16x16x32_bf16 v[132:135], v[220:223], v[20:23], v[132:135]\n"
"ds_read_b64_tr_b16 v[214:215], v226 offset:9728\n"
"v_add_f32 v238, 1.0, v238\n"
"v_mfma_f32_16x16x32_bf16 v[136:139], v[220:223], v[24:27], v[136:139]\n"
"v_rcp_f32 v237, v237\n"
"v_mfma_f32_16x16x32_bf16 v[140:143], v[220:223], v[28:31], v[140:143]\n"
"v_rcp_f32 v238, v238\n"
"s_waitcnt lgkmcnt(2)\n"
"v_mfma_f32_16x16x32_bf16 v[112:115], v[208:211], v[32:35], v[112:115]\n"
"ds_read_b64_tr_b16 v[216:217], v227 offset:40960\n"
"v_mul_f32 v73, v73, v237\n"
"v_mfma_f32_16x16x32_bf16 v[116:119], v[208:211], v[36:39], v[116:119]\n"
"ds_read_b64_tr_b16 v[218:219], v227 offset:41472\n"
"v_mul_f32 v74, v58, v74\n"
"v_mfma_f32_16x16x32_bf16 v[120:123], v[208:211], v[40:43], v[120:123]\n"
"v_mul_f32 v74, v74, v238\n"
"v_mul_f32 v239, s61, v59\n"
"v_mfma_f32_16x16x32_bf16 v[124:127], v[208:211], v[44:47], v[124:127]\n"
"v_exp_f32 v239, v239\n"
"s_waitcnt lgkmcnt(2)\n"
"v_mfma_f32_16x16x32_bf16 v[128:131], v[212:215], v[32:35], v[128:131]\n"
"ds_read_b64_tr_b16 v[220:221], v227 offset:41984\n"
"v_add_f32 v239, 1.0, v239\n"
"v_mfma_f32_16x16x32_bf16 v[132:135], v[212:215], v[36:39], v[132:135]\n"
"ds_read_b64_tr_b16 v[222:223], v227 offset:42496\n"
"v_mul_f32 v75, v59, v75\n"
"v_mfma_f32_16x16x32_bf16 v[136:139], v[212:215], v[40:43], v[136:139]\n"
"v_rcp_f32 v239, v239\n"
"v_mfma_f32_16x16x32_bf16 v[140:143], v[212:215], v[44:47], v[140:143]\n"
"v_mul_f32 v75, v75, v239\n"
"v_cvt_pk_bf16_f32 v232, v72, v73\n"
"s_waitcnt lgkmcnt(2)\n"
"v_mfma_f32_16x16x32_bf16 v[144:147], v[216:219], a[208:211], v[144:147]\n"
"ds_read_b64_tr_b16 v[208:209], v227 offset:49152\n"
"v_cvt_pk_bf16_f32 v233, v74, v75\n"
"v_mfma_f32_16x16x32_bf16 v[148:151], v[216:219], a[212:215], v[148:151]\n"
"ds_read_b64_tr_b16 v[210:211], v227 offset:49664\n"
"v_mul_f32 v240, s61, v60\n"
"v_mfma_f32_16x16x32_bf16 v[152:155], v[216:219], a[216:219], v[152:155]\n"
"v_exp_f32 v240, v240\n"
"v_mfma_f32_16x16x32_bf16 v[156:159], v[216:219], a[220:223], v[156:159]\n"
"v_add_f32 v240, 1.0, v240\n"
"v_mul_f32 v76, v60, v76\n"
"s_waitcnt lgkmcnt(2)\n"
"v_mfma_f32_16x16x32_bf16 v[160:163], v[220:223], a[208:211], v[160:163]\n"
"ds_read_b64_tr_b16 v[212:213], v227 offset:50176\n"
"v_mul_f32 v241, s61, v61\n"
"v_mfma_f32_16x16x32_bf16 v[164:167], v[220:223], a[212:215], v[164:167]\n"
"ds_read_b64_tr_b16 v[214:215], v227 offset:50688\n"
"v_mul_f32 v77, v61, v77\n"
"v_mfma_f32_16x16x32_bf16 v[168:171], v[220:223], a[216:219], v[168:171]\n"
"v_rcp_f32 v240, v240\n"
"v_mfma_f32_16x16x32_bf16 v[172:175], v[220:223], a[220:223], v[172:175]\n"
"v_exp_f32 v241, v241\n"
"s_waitcnt lgkmcnt(2)\n"
"v_mfma_f32_16x16x32_bf16 v[144:147], v[208:211], a[224:227], v[144:147]\n"
"ds_read_b64_tr_b16 v[216:217], v227 offset:57344\n"
"v_mul_f32 v76, v76, v240\n"
"v_mfma_f32_16x16x32_bf16 v[148:151], v[208:211], a[228:231], v[148:151]\n"
"ds_read_b64_tr_b16 v[218:219], v227 offset:57856\n"
"v_add_f32 v241, 1.0, v241\n"
"v_mfma_f32_16x16x32_bf16 v[152:155], v[208:211], a[232:235], v[152:155]\n"
"v_rcp_f32 v241, v241\n"
"v_mfma_f32_16x16x32_bf16 v[156:159], v[208:211], a[236:239], v[156:159]\n"
"v_mul_f32 v77, v77, v241\n"
"v_mul_f32 v242, s61, v62\n"
"s_waitcnt lgkmcnt(2)\n"
"v_mfma_f32_16x16x32_bf16 v[160:163], v[212:215], a[224:227], v[160:163]\n"
"ds_read_b64_tr_b16 v[220:221], v227 offset:58368\n"
"v_mul_f32 v78, v62, v78\n"
"v_mfma_f32_16x16x32_bf16 v[164:167], v[212:215], a[228:231], v[164:167]\n"
"ds_read_b64_tr_b16 v[222:223], v227 offset:58880\n"
"v_mul_f32 v243, s61, v63\n"
"v_mfma_f32_16x16x32_bf16 v[168:171], v[212:215], a[232:235], v[168:171]\n"
"v_exp_f32 v242, v242\n"
"v_mfma_f32_16x16x32_bf16 v[172:175], v[212:215], a[236:239], v[172:175]\n"
"v_exp_f32 v243, v243\n"
"s_waitcnt lgkmcnt(2)\n"
"v_mfma_f32_16x16x32_bf16 v[144:147], v[216:219], a[240:243], v[144:147]\n"
"v_add_u32 v227, 65536, v227\n"
"ds_read_b64_tr_b16 v[208:209], v227 offset:0\n"
"v_mfma_f32_16x16x32_bf16 v[148:151], v[216:219], a[244:247], v[148:151]\n"
"ds_read_b64_tr_b16 v[210:211], v227 offset:512\n"
"v_add_f32 v242, 1.0, v242\n"
"v_mfma_f32_16x16x32_bf16 v[152:155], v[216:219], a[248:251], v[152:155]\n"
"v_rcp_f32 v242, v242\n"
"v_mfma_f32_16x16x32_bf16 v[156:159], v[216:219], a[252:255], v[156:159]\n"
"v_mul_f32 v78, v78, v242\n"
"v_add_f32 v243, 1.0, v243\n"
"s_waitcnt lgkmcnt(2)\n"
"v_mfma_f32_16x16x32_bf16 v[160:163], v[220:223], a[240:243], v[160:163]\n"
"ds_read_b64_tr_b16 v[212:213], v227 offset:1024\n"
"v_mul_f32 v79, v63, v79\n"
"v_mfma_f32_16x16x32_bf16 v[164:167], v[220:223], a[244:247], v[164:167]\n"
"ds_read_b64_tr_b16 v[214:215], v227 offset:1536\n"
"v_cvt_pk_bf16_f32 v234, v76, v77\n"
"v_mfma_f32_16x16x32_bf16 v[168:171], v[220:223], a[248:251], v[168:171]\n"
"v_rcp_f32 v243, v243\n"
"v_mfma_f32_16x16x32_bf16 v[172:175], v[220:223], a[252:255], v[172:175]\n"
"v_mul_f32 v79, v79, v243\n"
"v_mul_f32 v236, s61, v80\n"
"s_waitcnt lgkmcnt(2)\n"
"v_mfma_f32_16x16x32_bf16 v[144:147], v[208:211], v[16:19], v[144:147]\n"
"ds_read_b64_tr_b16 v[216:217], v227 offset:8192\n"
"v_cvt_pk_bf16_f32 v235, v78, v79\n"
"v_mfma_f32_16x16x32_bf16 v[148:151], v[208:211], v[20:23], v[148:151]\n"
"ds_read_b64_tr_b16 v[218:219], v227 offset:8704\n"
"v_permlane16_swap_b32 v232, v234\n"
"v_mfma_f32_16x16x32_bf16 v[152:155], v[208:211], v[24:27], v[152:155]\n"
"v_exp_f32 v236, v236\n"
"v_mfma_f32_16x16x32_bf16 v[156:159], v[208:211], v[28:31], v[156:159]\n"
"v_permlane16_swap_b32 v233, v235\n"
"v_add_f32 v236, 1.0, v236\n"
"s_waitcnt lgkmcnt(2)\n"
"v_mfma_f32_16x16x32_bf16 v[160:163], v[212:215], v[16:19], v[160:163]\n"
"ds_read_b64_tr_b16 v[220:221], v227 offset:9216\n"
"v_mul_f32 v96, v80, v96\n"
"v_mfma_f32_16x16x32_bf16 v[164:167], v[212:215], v[20:23], v[164:167]\n"
"ds_read_b64_tr_b16 v[222:223], v227 offset:9728\n"
"v_mul_f32 v237, s61, v81\n"
"v_mfma_f32_16x16x32_bf16 v[168:171], v[212:215], v[24:27], v[168:171]\n"
"s_add_u32 s62, %[soffA], 294912\n"
"buffer_store_dwordx4 v[232:235], %[vS], %[srdA], s62 offen sc1\n"
"v_mfma_f32_16x16x32_bf16 v[172:175], v[212:215], v[28:31], v[172:175]\n"
"v_rcp_f32 v236, v236\n"
"s_waitcnt vmcnt(2)\n"
"s_barrier\n"
"s_waitcnt lgkmcnt(2)\n"
"v_mfma_f32_16x16x32_bf16 v[144:147], v[216:219], v[32:35], v[144:147]\n"
"v_add_u32 v224, 0xfffe0000, v224\n"
"ds_read_b64_tr_b16 v[208:209], v224 offset:0\n"
"v_mfma_f32_16x16x32_bf16 v[148:151], v[216:219], v[36:39], v[148:151]\n"
"ds_read_b64_tr_b16 v[210:211], v224 offset:512\n"
"v_mul_f32 v96, v96, v236\n"
"v_mfma_f32_16x16x32_bf16 v[152:155], v[216:219], v[40:43], v[152:155]\n"
"v_exp_f32 v237, v237\n"
"v_mfma_f32_16x16x32_bf16 v[156:159], v[216:219], v[44:47], v[156:159]\n"
"v_add_f32 v237, 1.0, v237\n"
"v_mul_f32 v97, v81, v97\n"
"s_waitcnt lgkmcnt(2)\n"
"v_mfma_f32_16x16x32_bf16 v[160:163], v[220:223], v[32:35], v[160:163]\n"
"ds_read_b64_tr_b16 v[212:213], v224 offset:1024\n"
"v_mul_f32 v238, s61, v82\n"
"v_mfma_f32_16x16x32_bf16 v[164:167], v[220:223], v[36:39], v[164:167]\n"
"ds_read_b64_tr_b16 v[214:215], v224 offset:1536\n"
"v_mul_f32 v98, v82, v98\n"
"v_mfma_f32_16x16x32_bf16 v[168:171], v[220:223], v[40:43], v[168:171]\n"
"v_rcp_f32 v237, v237\n"
"v_mfma_f32_16x16x32_bf16 v[172:175], v[220:223], v[44:47], v[172:175]\n"
"v_exp_f32 v238, v238\n"
"s_waitcnt lgkmcnt(2)\n"
"v_mfma_f32_16x16x32_bf16 v[176:179], v[208:211], a[0:3], 0\n"
"ds_read_b64_tr_b16 v[216:217], v224 offset:8192\n"
"v_mul_f32 v97, v97, v237\n"
"v_mfma_f32_16x16x32_bf16 v[180:183], v[208:211], a[4:7], 0\n"
"ds_read_b64_tr_b16 v[218:219], v224 offset:8704\n"
"v_add_f32 v238, 1.0, v238\n"
"v_mfma_f32_16x16x32_bf16 v[184:187], v[208:211], a[8:11], 0\n"
"v_rcp_f32 v238, v238\n"
"v_mfma_f32_16x16x32_bf16 v[188:191], v[208:211], a[12:15], 0\n"
"v_mul_f32 v98, v98, v238\n"
"v_mul_f32 v239, s61, v83\n"
"s_waitcnt lgkmcnt(2)\n"
"v_mfma_f32_16x16x32_bf16 v[192:195], v[212:215], a[0:3], 0\n"
"ds_read_b64_tr_b16 v[220:221], v224 offset:9216\n"
"v_mul_f32 v99, v83, v99\n"
"v_mfma_f32_16x16x32_bf16 v[196:199], v[212:215], a[4:7], 0\n"
"ds_read_b64_tr_b16 v[222:223], v224 offset:9728\n"
"v_cvt_pk_bf16_f32 v228, v96, v97\n"
"v_mfma_f32_16x16x32_bf16 v[200:203], v[212:215], a[8:11], 0\n"
"v_exp_f32 v239, v239\n"
"v_mfma_f32_16x16x32_bf16 v[204:207], v[212:215], a[12:15], 0\n"
"v_add_f32 v239, 1.0, v239\n"
"v_mul_f32 v240, s61, v84\n"
"s_barrier\n"
"s_waitcnt lgkmcnt(2)\n"
"v_mfma_f32_16x16x32_bf16 v[176:179], v[216:219], a[16:19], v[176:179]\n"
"ds_read_b64_tr_b16 v[208:209], v224 offset:16384\n"
"v_mul_f32 v100, v84, v100\n"
"v_mfma_f32_16x16x32_bf16 v[180:183], v[216:219], a[20:23], v[180:183]\n"
"ds_read_b64_tr_b16 v[210:211], v224 offset:16896\n"
"v_mul_f32 v241, s61, v85\n"
"v_mfma_f32_16x16x32_bf16 v[184:187], v[216:219], a[24:27], v[184:187]\n"
"v_rcp_f32 v239, v239\n"
"v_mfma_f32_16x16x32_bf16 v[188:191], v[216:219], a[28:31], v[188:191]\n"
"v_exp_f32 v240, v240\n"
"s_waitcnt lgkmcnt(2)\n"
"v_mfma_f32_16x16x32_bf16 v[192:195], v[220:223], a[16:19], v[192:195]\n"
"ds_read_b64_tr_b16 v[212:213], v224 offset:17408\n"
"v_mul_f32 v99, v99, v239\n"
"v_mfma_f32_16x16x32_bf16 v[196:199], v[220:223], a[20:23], v[196:199]\n"
"ds_read_b64_tr_b16 v[214:215], v224 offset:17920\n"
"v_cvt_pk_bf16_f32 v229, v98, v99\n"
"v_mfma_f32_16x16x32_bf16 v[200:203], v[220:223], a[24:27], v[200:203]\n"
"s_add_u32 m0, %[m0W], 73728\n"
"s_add_u32 s60, %[soffW], 2654336\n"
"buffer_load_dwordx4 %[vW], %[srdG], s60 offen lds\n"
"v_mfma_f32_16x16x32_bf16 v[204:207], v[220:223], a[28:31], v[204:207]\n"
"v_exp_f32 v241, v241\n"
"s_waitcnt lgkmcnt(2)\n"
"v_mfma_f32_16x16x32_bf16 v[176:179], v[208:211], a[32:35], v[176:179]\n"
"ds_read_b64_tr_b16 v[216:217], v224 offset:24576\n"
"v_add_f32 v240, 1.0, v240\n"
"v_mfma_f32_16x16x32_bf16 v[180:183], v[208:211], a[36:39], v[180:183]\n"
"ds_read_b64_tr_b16 v[218:219], v224 offset:25088\n"
"v_add_f32 v241, 1.0, v241\n"
"v_mfma_f32_16x16x32_bf16 v[184:187], v[208:211], a[40:43], v[184:187]\n"
"v_rcp_f32 v240, v240\n"
"v_mfma_f32_16x16x32_bf16 v[188:191], v[208:211], a[44:47], v[188:191]\n"
"v_rcp_f32 v241, v241\n"
"s_waitcnt lgkmcnt(2)\n"
"v_mfma_f32_16x16x32_bf16 v[192:195], v[212:215], a[32:35], v[192:195]\n"
"ds_read_b64_tr_b16 v[220:221], v224 offset:25600\n"
"v_mul_f32 v100, v100, v240\n"
"v_mfma_f32_16x16x32_bf16 v[196:199], v[212:215], a[36:39], v[196:199]\n"
"ds_read_b64_tr_b16 v[222:223], v224 offset:26112\n"
"v_mul_f32 v101, v85, v101\n"
"v_mfma_f32_16x16x32_bf16 v[200:203], v[212:215], a[40:43], v[200:203]\n"
"buffer_load_dwordx4 %[vW], %[srdU], s60 offen offset:1024 lds\n"
"v_mfma_f32_16x16x32_bf16 v[204:207], v[212:215], a[44:47], v[204:207]\n"
"v_mul_f32 v101, v101, v241\n"
"v_mul_f32 v242, s61, v86\n"
"s_waitcnt lgkmcnt(2)\n"
"v_mfma_f32_16x16x32_bf16 v[176:179], v[216:219], a[48:51], v[176:179]\n"
"ds_read_b64_tr_b16 v[208:209], v224 offset:32768\n"
"v_mul_f32 v102, v86, v102\n"
"v_mfma_f32_16x16x32_bf16 v[180:183], v[216:219], a[52:55], v[180:183]\n"
"ds_read_b64_tr_b16 v[210:211], v224 offset:33280\n"
"v_mul_f32 v243, s61, v87\n"
"v_mfma_f32_16x16x32_bf16 v[184:187], v[216:219], a[56:59], v[184:187]\n"
"v_exp_f32 v242, v242\n"
"v_mfma_f32_16x16x32_bf16 v[188:191], v[216:219], a[60:63], v[188:191]\n"
"v_exp_f32 v243, v243\n"
"s_waitcnt lgkmcnt(2)\n"
"v_mfma_f32_16x16x32_bf16 v[192:195], v[220:223], a[48:51], v[192:195]\n"
"ds_read_b64_tr_b16 v[212:213], v224 offset:33792\n"
"v_add_f32 v242, 1.0, v242\n"
"v_mfma_f32_16x16x32_bf16 v[196:199], v[220:223], a[52:55], v[196:199]\n"
"ds_read_b64_tr_b16 v[214:215], v224 offset:34304\n"
"v_add_f32 v243, 1.0, v243\n"
"v_mfma_f32_16x16x32_bf16 v[200:203], v[220:223], a[56:59], v[200:203]\n"
"s_add_u32 m0, %[m0W], 81920\n"
"s_add_u32 s60, %[soffW], 2949248\n"
"buffer_load_dwordx4 %[vW], %[srdG], s60 offen lds\n"
"v_mfma_f32_16x16x32_bf16 v[204:207], v[220:223], a[60:63], v[204:207]\n"
"v_rcp_f32 v242, v242\n"
"s_waitcnt lgkmcnt(2)\n"
"v_mfma_f32_16x16x32_bf16 v[176:179], v[208:211], a[64:67], v[176:179]\n"
"ds_read_b64_tr_b16 v[216:217], v224 offset:40960\n"
"v_mul_f32 v102, v102, v242\n"
"v_mfma_f32_16x16x32_bf16 v[180:183], v[208:211], a[68:71], v[180:183]\n"
"ds_read_b64_tr_b16 v[218:219], v224 offset:41472\n"
"v_mul_f32 v103, v87, v103\n"
"v_mfma_f32_16x16x32_bf16 v[184:187], v[208:211], a[72:75], v[184:187]\n"
"v_rcp_f32 v243, v243\n"
"v_mfma_f32_16x16x32_bf16 v[188:191], v[208:211], a[76:79], v[188:191]\n"
"v_mul_f32 v103, v103, v243\n"
"v_cvt_pk_bf16_f32 v230, v100, v101\n"
"s_waitcnt lgkmcnt(2)\n"
"v_mfma_f32_16x16x32_bf16 v[192:195], v[212:215], a[64:67], v[192:195]\n"
"ds_read_b64_tr_b16 v[220:221], v224 offset:41984\n"
"v_cvt_pk_bf16_f32 v231, v102, v103\n"
"v_mfma_f32_16x16x32_bf16 v[196:199], v[212:215], a[68:71], v[196:199]\n"
"ds_read_b64_tr_b16 v[222:223], v224 offset:42496\n"
"v_permlane16_swap_b32 v228, v230\n"
"v_mfma_f32_16x16x32_bf16 v[200:203], v[212:215], a[72:75], v[200:203]\n"
"v_permlane16_swap_b32 v229, v231\n"
"v_mul_f32 v236, s61, v88\n"
"v_mfma_f32_16x16x32_bf16 v[204:207], v[212:215], a[76:79], v[204:207]\n"
"buffer_load_dwordx4 %[vW], %[srdU], s60 offen offset:1024 lds\n"
"s_waitcnt lgkmcnt(2)\n"
"v_mfma_f32_16x16x32_bf16 v[176:179], v[216:219], a[80:83], v[176:179]\n"
"ds_read_b64_tr_b16 v[208:209], v224 offset:49152\n"
"v_mul_f32 v104, v88, v104\n"
"v_mfma_f32_16x16x32_bf16 v[180:183], v[216:219], a[84:87], v[180:183]\n"
"ds_read_b64_tr_b16 v[210:211], v224 offset:49664\n"
"v_mul_f32 v237, s61, v89\n"
"v_mfma_f32_16x16x32_bf16 v[184:187], v[216:219], a[88:91], v[184:187]\n"
"s_add_u32 s62, %[soffA], 512\n"
"buffer_store_dwordx4 v[228:231], %[vS], %[srdA], s62 offen sc1\n"
"v_mfma_f32_16x16x32_bf16 v[188:191], v[216:219], a[92:95], v[188:191]\n"
"v_exp_f32 v236, v236\n"
"s_waitcnt lgkmcnt(2)\n"
"v_mfma_f32_16x16x32_bf16 v[192:195], v[220:223], a[80:83], v[192:195]\n"
"ds_read_b64_tr_b16 v[212:213], v224 offset:50176\n"
"v_add_f32 v236, 1.0, v236\n"
"v_mfma_f32_16x16x32_bf16 v[196:199], v[220:223], a[84:87], v[196:199]\n"
"ds_read_b64_tr_b16 v[214:215], v224 offset:50688\n"
"v_mul_f32 v105, v89, v105\n"
"v_mfma_f32_16x16x32_bf16 v[200:203], v[220:223], a[88:91], v[200:203]\n"
"v_rcp_f32 v236, v236\n"
"v_mfma_f32_16x16x32_bf16 v[204:207], v[220:223], a[92:95], v[204:207]\n"
"v_exp_f32 v237, v237\n"
"s_waitcnt lgkmcnt(2)\n"
"v_mfma_f32_16x16x32_bf16 v[176:179], v[208:211], a[96:99], v[176:179]\n"
"ds_read_b64_tr_b16 v[216:217], v224 offset:57344\n"
"v_mul_f32 v104, v104, v236\n"
"v_mfma_f32_16x16x32_bf16 v[180:183], v[208:211], a[100:103], v[180:183]\n"
"ds_read_b64_tr_b16 v[218:219], v224 offset:57856\n"
"v_add_f32 v237, 1.0, v237\n"
"v_mfma_f32_16x16x32_bf16 v[184:187], v[208:211], a[104:107], v[184:187]\n"
"s_add_u32 m0, %[m0W], 90112\n"
"s_add_u32 s60, %[soffW], 3244160\n"
"buffer_load_dwordx4 %[vW], %[srdG], s60 offen lds\n"
"v_mfma_f32_16x16x32_bf16 v[188:191], v[208:211], a[108:111], v[188:191]\n"
"v_rcp_f32 v237, v237\n"
"s_waitcnt lgkmcnt(2)\n"
"v_mfma_f32_16x16x32_bf16 v[192:195], v[212:215], a[96:99], v[192:195]\n"
"ds_read_b64_tr_b16 v[220:221], v224 offset:58368\n"
"v_mul_f32 v105, v105, v237\n"
"v_mfma_f32_16x16x32_bf16 v[196:199], v[212:215], a[100:103], v[196:199]\n"
"ds_read_b64_tr_b16 v[222:223], v224 offset:58880\n"
"v_mul_f32 v238, s61, v90\n"
"v_mfma_f32_16x16x32_bf16 v[200:203], v[212:215], a[104:107], v[200:203]\n"
"v_exp_f32 v238, v238\n"
"v_mfma_f32_16x16x32_bf16 v[204:207], v[212:215], a[108:111], v[204:207]\n"
"v_add_f32 v238, 1.0, v238\n"
"v_mul_f32 v106, v90, v106\n"
"s_waitcnt lgkmcnt(2)\n"
"v_mfma_f32_16x16x32_bf16 v[176:179], v[216:219], a[112:115], v[176:179]\n"
"v_add_u32 v224, 65536, v224\n"
"ds_read_b64_tr_b16 v[208:209], v224 offset:0\n"
"v_mfma_f32_16x16x32_bf16 v[180:183], v[216:219], a[116:119], v[180:183]\n"
"ds_read_b64_tr_b16 v[210:211], v224 offset:512\n"
"v_mul_f32 v239, s61, v91\n"
"v_mfma_f32_16x16x32_bf16 v[184:187], v[216:219], a[120:123], v[184:187]\n"
"buffer_load_dwordx4 %[vW], %[srdU], s60 offen offset:1024 lds\n"
"v_mfma_f32_16x16x32_bf16 v[188:191], v[216:219], a[124:127], v[188:191]\n"
"v_rcp_f32 v238, v238\n"
"s_waitcnt lgkmcnt(2)\n"
"v_mfma_f32_16x16x32_bf16 v[192:195], v[220:223], a[112:115], v[192:195]\n"
"ds_read_b64_tr_b16 v[212:213], v224 offset:1024\n"
"v_mul_f32 v106, v106, v238\n"
"v_mfma_f32_16x16x32_bf16 v[196:199], v[220:223], a[116:119], v[196:199]\n"
"ds_read_b64_tr_b16 v[214:215], v224 offset:1536\n"
"v_mul_f32 v107, v91, v107\n"
"v_mfma_f32_16x16x32_bf16 v[200:203], v[220:223], a[120:123], v[200:203]\n"
"v_exp_f32 v239, v239\n"
"v_mfma_f32_16x16x32_bf16 v[204:207], v[220:223], a[124:127], v[204:207]\n"
"v_add_f32 v239, 1.0, v239\n"
"v_cvt_pk_bf16_f32 v232, v104, v105\n"
"s_waitcnt lgkmcnt(2)\n"
"v_mfma_f32_16x16x32_bf16 v[176:179], v[208:211], a[128:131], v[176:179]\n"
"v_add_u32 v225, 0xfffe0000, v225\n"
"ds_read_b64_tr_b16 v[216:217], v225 offset:0\n"
"v_mfma_f32_16x16x32_bf16 v[180:183], v[208:211], a[132:135], v[180:183]\n"
"ds_read_b64_tr_b16 v[218:219], v225 offset:512\n"
"v_mul_f32 v240, s61, v92\n"
"v_mfma_f32_16x16x32_bf16 v[184:187], v[208:211], a[136:139], v[184:187]\n"
"s_add_u32 m0, %[m0W], 98304\n"
"s_add_u32 s60, %[soffW], 3539072\n"
"buffer_load_dwordx4 %[vW], %[srdG], s60 offen lds\n"
"v_mfma_f32_16x16x32_bf16 v[188:191], v[208:211], a[140:143], v[188:191]\n"
"v_rcp_f32 v239, v239\n"
"s_waitcnt lgkmcnt(2)\n"
"v_mfma_f32_16x16x32_bf16 v[192:195], v[212:215], a[128:131], v[192:195]\n"
"ds_read_b64_tr_b16 v[220:221], v225 offset:1024\n"
"v_mul_f32 v107, v107, v239\n"
"v_mfma_f32_16x16x32_bf16 v[196:199], v[212:215], a[132:135], v[196:199]\n"
"ds_read_b64_tr_b16 v[222:223], v225 offset:1536\n"
"v_cvt_pk_bf16_f32 v233, v106, v107\n"
"v_mfma_f32_16x16x32_bf16 v[200:203], v[212:215], a[136:139], v[200:203]\n"
"v_exp_f32 v240, v240\n"
"v_mfma_f32_16x16x32_bf16 v[204:207], v[212:215], a[140:143], v[204:207]\n"
"v_add_f32 v240, 1.0, v240\n"
"v_mul_f32 v108, v92, v108\n"
"s_waitcnt lgkmcnt(2)\n"
"v_mfma_f32_16x16x32_bf16 v[48:51], v[216:219], a[0:3], 0\n"
"ds_read_b64_tr_b16 v[208:209], v225 offset:8192\n"
"v_mul_f32 v241, s61, v93\n"
"v_mfma_f32_16x16x32_bf16 v[52:55], v[216:219], a[4:7], 0\n"
"ds_read_b64_tr_b16 v[210:211], v225 offset:8704\n"
"v_mul_f32 v109, v93, v109\n"
"v_mfma_f32_16x16x32_bf16 v[56:59], v[216:219], a[8:11], 0\n"
"v_rcp_f32 v240, v240\n"
"v_mfma_f32_16x16x32_bf16 v[60:63], v[216:219], a[12:15], 0\n"
"buffer_load_dwordx4 %[vW], %[srdU], s60 offen offset:1024 lds\n"
"s_waitcnt lgkmcnt(2)\n"
"v_mfma_f32_16x16x32_bf16 v[64:67], v[220:223], a[0:3], 0\n"
"ds_read_b64_tr_b16 v[212:213], v225 offset:9216\n"
"v_mul_f32 v108, v108, v240\n"
"v_mfma_f32_16x16x32_bf16 v[68:71], v[220:223], a[4:7], 0\n"
"ds_read_b64_tr_b16 v[214:215], v225 offset:9728\n"
"v_mul_f32 v242, s61, v94\n"
"v_mfma_f32_16x16x32_bf16 v[72:75], v[220:223], a[8:11], 0\n"
"v_exp_f32 v241, v241\n"
"v_mfma_f32_16x16x32_bf16 v[76:79], v[220:223], a[12:15], 0\n"
"v_exp_f32 v242, v242\n"
"s_waitcnt lgkmcnt(2)\n"
"v_mfma_f32_16x16x32_bf16 v[48:51], v[208:211], a[16:19], v[48:51]\n"
"ds_read_b64_tr_b16 v[216:217], v225 offset:16384\n"
"v_add_f32 v241, 1.0, v241\n"
"v_mfma_f32_16x16x32_bf16 v[52:55], v[208:211], a[20:23], v[52:55]\n"
"ds_read_b64_tr_b16 v[218:219], v225 offset:16896\n"
"v_add_f32 v242, 1.0, v242\n"
"v_mfma_f32_16x16x32_bf16 v[56:59], v[208:211], a[24:27], v[56:59]\n"
"v_rcp_f32 v241, v241\n"
"v_mfma_f32_16x16x32_bf16 v[60:63], v[208:211], a[28:31], v[60:63]\n"
"v_rcp_f32 v242, v242\n"
"s_waitcnt lgkmcnt(2)\n"
"v_mfma_f32_16x16x32_bf16 v[64:67], v[212:215], a[16:19], v[64:67]\n"
"ds_read_b64_tr_b16 v[220:221], v225 offset:17408\n"
"v_mul_f32 v109, v109, v241\n"
"v_mfma_f32_16x16x32_bf16 v[68:71], v[212:215], a[20:23], v[68:71]\n"
"ds_read_b64_tr_b16 v[222:223], v225 offset:17920\n"
"v_mul_f32 v110, v94, v110\n"
"v_mfma_f32_16x16x32_bf16 v[72:75], v[212:215], a[24:27], v[72:75]\n"
"s_add_u32 m0, %[m0W], 106496\n"
"s_add_u32 s60, %[soffW], 3833984\n"
"buffer_load_dwordx4 %[vW], %[srdG], s60 offen lds\n"
"v_mfma_f32_16x16x32_bf16 v[76:79], v[212:215], a[28:31], v[76:79]\n"
"v_mul_f32 v110, v110, v242\n"
"v_mul_f32 v243, s61, v95\n"
"s_waitcnt lgkmcnt(2)\n"
"v_mfma_f32_16x16x32_bf16 v[48:51], v[216:219], a[32:35], v[48:51]\n"
"ds_read_b64_tr_b16 v[208:209], v225 offset:24576\n"
"v_mul_f32 v111, v95, v111\n"
"v_mfma_f32_16x16x32_bf16 v[52:55], v[216:219], a[36:39], v[52:55]\n"
"ds_read_b64_tr_b16 v[210:211], v225 offset:25088\n"
"v_cvt_pk_bf16_f32 v234, v108, v109\n"
"v_mfma_f32_16x16x32_bf16 v[56:59], v[216:219], a[40:43], v[56:59]\n"
"v_exp_f32 v243, v243\n"
"v_mfma_f32_16x16x32_bf16 v[60:63], v[216:219], a[44:47], v[60:63]\n"
"v_add_f32 v243, 1.0, v243\n"
"v_mul_f32 v236, s61, v112\n"
"s_waitcnt lgkmcnt(2)\n"
"v_mfma_f32_16x16x32_bf16 v[64:67], v[220:223], a[32:35], v[64:67]\n"
"ds_read_b64_tr_b16 v[212:213], v225 offset:25600\n"
"v_mul_f32 v128, v112, v128\n"
"v_mfma_f32_16x16x32_bf16 v[68:71], v[220:223], a[36:39], v[68:71]\n"
"ds_read_b64_tr_b16 v[214:215], v225 offset:26112\n"
"v_mul_f32 v237, s61, v113\n"
"v_mfma_f32_16x16x32_bf16 v[72:75], v[220:223], a[40:43], v[72:75]\n"
"buffer_load_dwordx4 %[vW], %[srdU], s60 offen offset:1024 lds\n"
"v_mfma_f32_16x16x32_bf16 v[76:79], v[220:223], a[44:47], v[76:79]\n"
"v_rcp_f32 v243, v243\n"
"s_waitcnt lgkmcnt(2)\n"
"v_mfma_f32_16x16x32_bf16 v[48:51], v[208:211], a[48:51], v[48:51]\n"
"ds_read_b64_tr_b16 v[216:217], v225 offset:32768\n"
"v_mul_f32 v111, v111, v243\n"
"v_mfma_f32_16x16x32_bf16 v[52:55], v[208:211], a[52:55], v[52:55]\n"
"ds_read_b64_tr_b16 v[218:219], v225 offset:33280\n"
"v_cvt_pk_bf16_f32 v235, v110, v111\n"
"v_mfma_f32_16x16x32_bf16 v[56:59], v[208:211], a[56:59], v[56:59]\n"
"v_exp_f32 v236, v236\n"
"v_mfma_f32_16x16x32_bf16 v[60:63], v[208:211], a[60:63], v[60:63]\n"
"v_exp_f32 v237, v237\n"
"s_waitcnt lgkmcnt(2)\n"
"v_mfma_f32_16x16x32_bf16 v[64:67], v[212:215], a[48:51], v[64:67]\n"
"ds_read_b64_tr_b16 v[220:221], v225 offset:33792\n"
"v_permlane16_swap_b32 v232, v234\n"
"v_mfma_f32_16x16x32_bf16 v[68:71], v[212:215], a[52:55], v[68:71]\n"
"ds_read_b64_tr_b16 v[222:223], v225 offset:34304\n"
"v_permlane16_swap_b32 v233, v235\n"
"v_mfma_f32_16x16x32_bf16 v[72:75], v[212:215], a[56:59], v[72:75]\n"
"s_add_u32 m0, %[m0W], 114688\n"
"s_add_u32 s60, %[soffW], 4128896\n"
"buffer_load_dwordx4 %[vW], %[srdG], s60 offen lds\n"
"v_mfma_f32_16x16x32_bf16 v[76:79], v[212:215], a[60:63], v[76:79]\n"
"s_add_u32 s62, %[soffA], 295424\n"
"buffer_store_dwordx4 v[232:235], %[vS], %[srdA], s62 offen sc1\n"
"s_waitcnt lgkmcnt(2)\n"
"v_mfma_f32_16x16x32_bf16 v[48:51], v[216:219], a[64:67], v[48:51]\n"
"ds_read_b64_tr_b16 v[208:209], v225 offset:40960\n"
"v_add_f32 v236, 1.0, v236\n"
"v_mfma_f32_16x16x32_bf16 v[52:55], v[216:219], a[68:71], v[52:55]\n"
"ds_read_b64_tr_b16 v[210:211], v225 offset:41472\n"
"v_add_f32 v237, 1.0, v237\n"
"v_mfma_f32_16x16x32_bf16 v[56:59], v[216:219], a[72:75], v[56:59]\n"
"v_rcp_f32 v236, v236\n"
"v_mfma_f32_16x16x32_bf16 v[60:63], v[216:219], a[76:79], v[60:63]\n"
"v_rcp_f32 v237, v237\n"
"s_waitcnt lgkmcnt(2)\n"
"v_mfma_f32_16x16x32_bf16 v[64:67], v[220:223], a[64:67], v[64:67]\n"
"ds_read_b64_tr_b16 v[212:213], v225 offset:41984\n"
"v_mul_f32 v128, v128, v236\n"
"v_mfma_f32_16x16x32_bf16 v[68:71], v[220:223], a[68:71], v[68:71]\n"
"ds_read_b64_tr_b16 v[214:215], v225 offset:42496\n"
"v_mul_f32 v129, v113, v129\n"
"v_mfma_f32_16x16x32_bf16 v[72:75], v[220:223], a[72:75], v[72:75]\n"
"v_mul_f32 v129, v129, v237\n"
"v_mul_f32 v238, s61, v114\n"
"v_mfma_f32_16x16x32_bf16 v[76:79], v[220:223], a[76:79], v[76:79]\n"
"buffer_load_dwordx4 %[vW], %[srdU], s60 offen offset:1024 lds\n"
"s_waitcnt lgkmcnt(2)\n"
"v_mfma_f32_16x16x32_bf16 v[48:51], v[208:211], a[80:83], v[48:51]\n"
"ds_read_b64_tr_b16 v[216:217], v225 offset:49152\n"
"v_mul_f32 v130, v114, v130\n"
"v_mfma_f32_16x16x32_bf16 v[52:55], v[208:211], a[84:87], v[52:55]\n"
"ds_read_b64_tr_b16 v[218:219], v225 offset:49664\n"
"v_mul_f32 v239, s61, v115\n"
"v_mfma_f32_16x16x32_bf16 v[56:59], v[208:211], a[88:91], v[56:59]\n"
"v_exp_f32 v238, v238\n"
"v_mfma_f32_16x16x32_bf16 v[60:63], v[208:211], a[92:95], v[60:63]\n"
"v_exp_f32 v239, v239\n"
"s_waitcnt lgkmcnt(2)\n"
"v_mfma_f32_16x16x32_bf16 v[64:67], v[212:215], a[80:83], v[64:67]\n"
"ds_read_b64_tr_b16 v[220:221], v225 offset:50176\n"
"v_add_f32 v238, 1.0, v238\n"
"v_mfma_f32_16x16x32_bf16 v[68:71], v[212:215], a[84:87], v[68:71]\n"
"ds_read_b64_tr_b16 v[222:223], v225 offset:50688\n"
"v_add_f32 v239, 1.0, v239\n"
"v_mfma_f32_16x16x32_bf16 v[72:75], v[212:215], a[88:91], v[72:75]\n"
"v_rcp_f32 v238, v238\n"
"v_mfma_f32_16x16x32_bf16 v[76:79], v[212:215], a[92:95], v[76:79]\n"
"v_rcp_f32 v239, v239\n"
"s_waitcnt lgkmcnt(2)\n"
"v_mfma_f32_16x16x32_bf16 v[48:51], v[216:219], a[96:99], v[48:51]\n"
"ds_read_b64_tr_b16 v[208:209], v225 offset:57344\n"
"v_mul_f32 v130, v130, v238\n"
"v_mfma_f32_16x16x32_bf16 v[52:55], v[216:219], a[100:103], v[52:55]\n"
"ds_read_b64_tr_b16 v[210:211], v225 offset:57856\n"
"v_mul_f32 v131, v115, v131\n"
"v_mfma_f32_16x16x32_bf16 v[56:59], v[216:219], a[104:107], v[56:59]\n"
"s_add_u32 m0, %[m0W], 122880\n"
"s_add_u32 s60, %[soffW], 4423808\n"
"buffer_load_dwordx4 %[vW], %[srdG], s60 offen lds\n"
"v_mfma_f32_16x16x32_bf16 v[60:63], v[216:219], a[108:111], v[60:63]\n"
"v_mul_f32 v131, v131, v239\n"
"v_cvt_pk_bf16_f32 v228, v128, v129\n"
"s_waitcnt lgkmcnt(2)\n"
"v_mfma_f32_16x16x32_bf16 v[64:67], v[220:223], a[96:99], v[64:67]\n"
"ds_read_b64_tr_b16 v[212:213], v225 offset:58368\n"
"v_cvt_pk_bf16_f32 v229, v130, v131\n"
"v_mfma_f32_16x16x32_bf16 v[68:71], v[220:223], a[100:103], v[68:71]\n"
"ds_read_b64_tr_b16 v[214:215], v225 offset:58880\n"
"v_mul_f32 v240, s61, v116\n"
"v_mfma_f32_16x16x32_bf16 v[72:75], v[220:223], a[104:107], v[72:75]\n"
"v_exp_f32 v240, v240\n"
"v_mfma_f32_16x16x32_bf16 v[76:79], v[220:223], a[108:111], v[76:79]\n"
"v_add_f32 v240, 1.0, v240\n"
"v_mul_f32 v132, v116, v132\n"
"s_waitcnt lgkmcnt(2)\n"
"v_mfma_f32_16x16x32_bf16 v[48:51], v[208:211], a[112:115], v[48:51]\n"
"v_add_u32 v225, 65536, v225\n"
"ds_read_b64_tr_b16 v[216:217], v225 offset:0\n"
"v_mfma_f32_16x16x32_bf16 v[52:55], v[208:211], a[116:119], v[52:55]\n"
"ds_read_b64_tr_b16 v[218:219], v225 offset:512\n"
"v_mul_f32 v241, s61, v117\n"
"v_mfma_f32_16x16x32_bf16 v[56:59], v[208:211], a[120:123], v[56:59]\n"
"buffer_load_dwordx4 %[vW], %[srdU], s60 offen offset:1024 lds\n"
"v_mfma_f32_16x16x32_bf16 v[60:63], v[208:211], a[124:127], v[60:63]\n"
"v_rcp_f32 v240, v240\n"
"s_waitcnt lgkmcnt(2)\n"
"v_mfma_f32_16x16x32_bf16 v[64:67], v[212:215], a[112:115], v[64:67]\n"
"ds_read_b64_tr_b16 v[220:221], v225 offset:1024\n"
"v_mul_f32 v132, v132, v240\n"
"v_mfma_f32_16x16x32_bf16 v[68:71], v[212:215], a[116:119], v[68:71]\n"
"ds_read_b64_tr_b16 v[222:223], v225 offset:1536\n"
"v_mul_f32 v133, v117, v133\n"
"v_mfma_f32_16x16x32_bf16 v[72:75], v[212:215], a[120:123], v[72:75]\n"
"v_exp_f32 v241, v241\n"
"v_mfma_f32_16x16x32_bf16 v[76:79], v[212:215], a[124:127], v[76:79]\n"
"v_add_f32 v241, 1.0, v241\n"
"v_mul_f32 v242, s61, v118\n"
"s_waitcnt lgkmcnt(2)\n"
"v_mfma_f32_16x16x32_bf16 v[48:51], v[216:219], a[128:131], v[48:51]\n"
"v_add_u32 v226, 0xfffe0000, v226\n"
"ds_read_b64_tr_b16 v[208:209], v226 offset:0\n"
"v_mfma_f32_16x16x32_bf16 v[52:55], v[216:219], a[132:135], v[52:55]\n"
"ds_read_b64_tr_b16 v[210:211], v226 offset:512\n"
"v_mul_f32 v134, v118, v134\n"
"v_mfma_f32_16x16x32_bf16 v[56:59], v[216:219], a[136:139], v[56:59]\n"
"s_add_u32 m0, %[m0W], 131072\n"
"s_add_u32 s60, %[soffW], 4718720\n"
"buffer_load_dwordx4 %[vW], %[srdG], s60 offen lds\n"
"v_mfma_f32_16x16x32_bf16 v[60:63], v[216:219], a[140:143], v[60:63]\n"
"v_rcp_f32 v241, v241\n"
"s_waitcnt lgkmcnt(2)\n"
"v_mfma_f32_16x16x32_bf16 v[64:67], v[220:223], a[128:131], v[64:67]\n"
"ds_read_b64_tr_b16 v[212:213], v226 offset:1024\n"
"v_mul_f32 v133, v133, v241\n"
"v_mfma_f32_16x16x32_bf16 v[68:71], v[220:223], a[132:135], v[68:71]\n"
"ds_read_b64_tr_b16 v[214:215], v226 offset:1536\n"
"v_mul_f32 v243, s61, v119\n"
"v_mfma_f32_16x16x32_bf16 v[72:75], v[220:223], a[136:139], v[72:75]\n"
"v_exp_f32 v242, v242\n"
"v_mfma_f32_16x16x32_bf16 v[76:79], v[220:223], a[140:143], v[76:79]\n"
"v_exp_f32 v243, v243\n"
"s_waitcnt lgkmcnt(2)\n"
"v_mfma_f32_16x16x32_bf16 v[80:83], v[208:211], a[0:3], 0\n"
"ds_read_b64_tr_b16 v[216:217], v226 offset:8192\n"
"v_add_f32 v242, 1.0, v242\n"
"v_mfma_f32_16x16x32_bf16 v[84:87], v[208:211], a[4:7], 0\n"
"ds_read_b64_tr_b16 v[218:219], v226 offset:8704\n"
"v_add_f32 v243, 1.0, v243\n"
"v_mfma_f32_16x16x32_bf16 v[88:91], v[208:211], a[8:11], 0\n"
"v_rcp_f32 v242, v242\n"
"v_mfma_f32_16x16x32_bf16 v[92:95], v[208:211], a[12:15], 0\n"
"buffer_load_dwordx4 %[vW], %[srdU], s60 offen offset:1024 lds\n"
"s_waitcnt lgkmcnt(2)\n"
"v_mfma_f32_16x16x32_bf16 v[96:99], v[212:215], a[0:3], 0\n"
"ds_read_b64_tr_b16 v[220:221], v226 offset:9216\n"
"v_mul_f32 v134, v134, v242\n"
"v_mfma_f32_16x16x32_bf16 v[100:103], v[212:215], a[4:7], 0\n"
"ds_read_b64_tr_b16 v[222:223], v226 offset:9728\n"
"v_mul_f32 v135, v119, v135\n"
"v_mfma_f32_16x16x32_bf16 v[104:107], v[212:215], a[8:11], 0\n"
"v_rcp_f32 v243, v243\n"
"v_mfma_f32_16x16x32_bf16 v[108:111], v[212:215], a[12:15], 0\n"
"v_mul_f32 v135, v135, v243\n"
"v_cvt_pk_bf16_f32 v230, v132, v133\n"
"s_waitcnt lgkmcnt(2)\n"
"v_mfma_f32_16x16x32_bf16 v[80:83], v[216:219], a[16:19], v[80:83]\n"
"ds_read_b64_tr_b16 v[208:209], v226 offset:16384\n"
"v_cvt_pk_bf16_f32 v231, v134, v135\n"
"v_mfma_f32_16x16x32_bf16 v[84:87], v[216:219], a[20:23], v[84:87]\n"
"ds_read_b64_tr_b16 v[210:211], v226 offset:16896\n"
"v_permlane16_swap_b32 v228, v230\n"
"v_mfma_f32_16x16x32_bf16 v[88:91], v[216:219], a[24:27], v[88:91]\n"
"v_permlane16_swap_b32 v229, v231\n"
"v_mul_f32 v236, s61, v120\n"
"v_mfma_f32_16x16x32_bf16 v[92:95], v[216:219], a[28:31], v[92:95]\n"
"s_add_u32 s62, %[soffA], 1024\n"
"buffer_store_dwordx4 v[228:231], %[vS], %[srdA], s62 offen sc1\n"
"s_waitcnt lgkmcnt(2)\n"
"v_mfma_f32_16x16x32_bf16 v[96:99], v[220:223], a[16:19], v[96:99]\n"
"ds_read_b64_tr_b16 v[212:213], v226 offset:17408\n"
"v_mul_f32 v136, v120, v136\n"
"v_mfma_f32_16x16x32_bf16 v[100:103], v[220:223], a[20:23], v[100:103]\n"
"ds_read_b64_tr_b16 v[214:215], v226 offset:17920\n"
"v_mul_f32 v237, s61, v121\n"
"v_mfma_f32_16x16x32_bf16 v[104:107], v[220:223], a[24:27], v[104:107]\n"
"s_add_u32 m0, %[m0W], 139264\n"
"s_add_u32 s60, %[soffW], 5013632\n"
"buffer_load_dwordx4 %[vW], %[srdG], s60 offen lds\n"
"v_mfma_f32_16x16x32_bf16 v[108:111], v[220:223], a[28:31], v[108:111]\n"
"v_exp_f32 v236, v236\n"
"s_waitcnt lgkmcnt(2)\n"
"v_mfma_f32_16x16x32_bf16 v[80:83], v[208:211], a[32:35], v[80:83]\n"
"ds_read_b64_tr_b16 v[216:217], v226 offset:24576\n"
"v_add_f32 v236, 1.0, v236\n"
"v_mfma_f32_16x16x32_bf16 v[84:87], v[208:211], a[36:39], v[84:87]\n"
"ds_read_b64_tr_b16 v[218:219], v226 offset:25088\n"
"v_mul_f32 v137, v121, v137\n"
"v_mfma_f32_16x16x32_bf16 v[88:91], v[208:211], a[40:43], v[88:91]\n"
"v_rcp_f32 v236, v236\n"
"v_mfma_f32_16x16x32_bf16 v[92:95], v[208:211], a[44:47], v[92:95]\n"
"v_exp_f32 v237, v237\n"
"s_waitcnt lgkmcnt(2)\n"
"v_mfma_f32_16x16x32_bf16 v[96:99], v[212:215], a[32:35], v[96:99]\n"
"ds_read_b64_tr_b16 v[220:221], v226 offset:25600\n"
"v_mul_f32 v136, v136, v236\n"
"v_mfma_f32_16x16x32_bf16 v[100:103], v[212:215], a[36:39], v[100:103]\n"
"ds_read_b64_tr_b16 v[222:223], v226 offset:26112\n"
"v_add_f32 v237, 1.0, v237\n"
"v_mfma_f32_16x16x32_bf16 v[104:107], v[212:215], a[40:43], v[104:107]\n"
"buffer_load_dwordx4 %[vW], %[srdU], s60 offen offset:1024 lds\n"
"v_mfma_f32_16x16x32_bf16 v[108:111], v[212:215], a[44:47], v[108:111]\n"
"v_rcp_f32 v237, v237\n"
"s_waitcnt lgkmcnt(2)\n"
"v_mfma_f32_16x16x32_bf16 v[80:83], v[216:219], a[48:51], v[80:83]\n"
"ds_read_b64_tr_b16 v[208:209], v226 offset:32768\n"
"v_mul_f32 v137, v137, v237\n"
"v_mfma_f32_16x16x32_bf16 v[84:87], v[216:219], a[52:55], v[84:87]\n"
"ds_read_b64_tr_b16 v[210:211], v226 offset:33280\n"
"v_mul_f32 v238, s61, v122\n"
"v_mfma_f32_16x16x32_bf16 v[88:91], v[216:219], a[56:59], v[88:91]\n"
"v_exp_f32 v238, v238\n"
"v_mfma_f32_16x16x32_bf16 v[92:95], v[216:219], a[60:63], v[92:95]\n"
"v_add_f32 v238, 1.0, v238\n"
"v_mul_f32 v138, v122, v138\n"
"s_waitcnt lgkmcnt(2)\n"
"v_mfma_f32_16x16x32_bf16 v[96:99], v[220:223], a[48:51], v[96:99]\n"
"ds_read_b64_tr_b16 v[212:213], v226 offset:33792\n"
"v_mul_f32 v239, s61, v123\n"
"v_mfma_f32_16x16x32_bf16 v[100:103], v[220:223], a[52:55], v[100:103]\n"
"ds_read_b64_tr_b16 v[214:215], v226 offset:34304\n"
"v_mul_f32 v139, v123, v139\n"
"v_mfma_f32_16x16x32_bf16 v[104:107], v[220:223], a[56:59], v[104:107]\n"
"v_rcp_f32 v238, v238\n"
"v_mfma_f32_16x16x32_bf16 v[108:111], v[220:223], a[60:63], v[108:111]\n"
"v_exp_f32 v239, v239\n"
"s_waitcnt lgkmcnt(2)\n"
"v_mfma_f32_16x16x32_bf16 v[80:83], v[208:211], a[64:67], v[80:83]\n"
"ds_read_b64_tr_b16 v[216:217], v226 offset:40960\n"
"v_mul_f32 v138, v138, v238\n"
"v_mfma_f32_16x16x32_bf16 v[84:87], v[208:211], a[68:71], v[84:87]\n"
"ds_read_b64_tr_b16 v[218:219], v226 offset:41472\n"
"v_add_f32 v239, 1.0, v239\n"
"v_mfma_f32_16x16x32_bf16 v[88:91], v[208:211], a[72:75], v[88:91]\n"
"v_rcp_f32 v239, v239\n"
"v_mfma_f32_16x16x32_bf16 v[92:95], v[208:211], a[76:79], v[92:95]\n"
"v_mul_f32 v139, v139, v239\n"
"v_cvt_pk_bf16_f32 v232, v136, v137\n"
"s_waitcnt lgkmcnt(2)\n"
"v_mfma_f32_16x16x32_bf16 v[96:99], v[212:215], a[64:67], v[96:99]\n"
"ds_read_b64_tr_b16 v[220:221], v226 offset:41984\n"
"v_cvt_pk_bf16_f32 v233, v138, v139\n"
"v_mfma_f32_16x16x32_bf16 v[100:103], v[212:215], a[68:71], v[100:103]\n"
"ds_read_b64_tr_b16 v[222:223], v226 offset:42496\n"
"v_mul_f32 v240, s61, v124\n"
"v_mfma_f32_16x16x32_bf16 v[104:107], v[212:215], a[72:75], v[104:107]\n"
"v_exp_f32 v240, v240\n"
"v_mfma_f32_16x16x32_bf16 v[108:111], v[212:215], a[76:79], v[108:111]\n"
"v_add_f32 v240, 1.0, v240\n"
"v_mul_f32 v140, v124, v140\n"
"s_waitcnt lgkmcnt(2)\n"
"v_mfma_f32_16x16x32_bf16 v[80:83], v[216:219], a[80:83], v[80:83]\n"
"ds_read_b64_tr_b16 v[208:209], v226 offset:49152\n"
"v_mul_f32 v241, s61, v125\n"
"v_mfma_f32_16x16x32_bf16 v[84:87], v[216:219], a[84:87], v[84:87]\n"
"ds_read_b64_tr_b16 v[210:211], v226 offset:49664\n"
"v_mul_f32 v141, v125, v141\n"
"v_mfma_f32_16x16x32_bf16 v[88:91], v[216:219], a[88:91], v[88:91]\n"
"v_rcp_f32 v240, v240\n"
"v_mfma_f32_16x16x32_bf16 v[92:95], v[216:219], a[92:95], v[92:95]\n"
"v_exp_f32 v241, v241\n"
"s_waitcnt lgkmcnt(2)\n"
"v_mfma_f32_16x16x32_bf16 v[96:99], v[220:223], a[80:83], v[96:99]\n"
"ds_read_b64_tr_b16 v[212:213], v226 offset:50176\n"
"v_mul_f32 v140, v140, v240\n"
"v_mfma_f32_16x16x32_bf16 v[100:103], v[220:223], a[84:87], v[100:103]\n"
"ds_read_b64_tr_b16 v[214:215], v226 offset:50688\n"
"v_add_f32 v241, 1.0, v241\n"
"v_mfma_f32_16x16x32_bf16 v[104:107], v[220:223], a[88:91], v[104:107]\n"
"v_rcp_f32 v241, v241\n"
"v_mfma_f32_16x16x32_bf16 v[108:111], v[220:223], a[92:95], v[108:111]\n"
"v_mul_f32 v141, v141, v241\n"
"v_mul_f32 v242, s61, v126\n"
"s_waitcnt lgkmcnt(2)\n"
"v_mfma_f32_16x16x32_bf16 v[80:83], v[208:211], a[96:99], v[80:83]\n"
"ds_read_b64_tr_b16 v[216:217], v226 offset:57344\n"
"v_mul_f32 v142, v126, v142\n"
"v_mfma_f32_16x16x32_bf16 v[84:87], v[208:211], a[100:103], v[84:87]\n"
"ds_read_b64_tr_b16 v[218:219], v226 offset:57856\n"
"v_mul_f32 v243, s61, v127\n"
"v_mfma_f32_16x16x32_bf16 v[88:91], v[208:211], a[104:107], v[88:91]\n"
"v_exp_f32 v242, v242\n"
"v_mfma_f32_16x16x32_bf16 v[92:95], v[208:211], a[108:111], v[92:95]\n"
"v_exp_f32 v243, v243\n"
"s_waitcnt lgkmcnt(2)\n"
"v_mfma_f32_16x16x32_bf16 v[96:99], v[212:215], a[96:99], v[96:99]\n"
"ds_read_b64_tr_b16 v[220:221], v226 offset:58368\n"
"v_add_f32 v242, 1.0, v242\n"
"v_mfma_f32_16x16x32_bf16 v[100:103], v[212:215], a[100:103], v[100:103]\n"
"ds_read_b64_tr_b16 v[222:223], v226 offset:58880\n"
"v_add_f32 v243, 1.0, v243\n"
"v_mfma_f32_16x16x32_bf16 v[104:107], v[212:215], a[104:107], v[104:107]\n"
"v_rcp_f32 v242, v242\n"
"v_mfma_f32_16x16x32_bf16 v[108:111], v[212:215], a[108:111], v[108:111]\n"
"v_rcp_f32 v243, v243\n"
"s_waitcnt lgkmcnt(2)\n"
"v_mfma_f32_16x16x32_bf16 v[80:83], v[216:219], a[112:115], v[80:83]\n"
"v_add_u32 v226, 65536, v226\n"
"ds_read_b64_tr_b16 v[208:209], v226 offset:0\n"
"v_mfma_f32_16x16x32_bf16 v[84:87], v[216:219], a[116:119], v[84:87]\n"
"ds_read_b64_tr_b16 v[210:211], v226 offset:512\n"
"v_mul_f32 v142, v142, v242\n"
"v_mfma_f32_16x16x32_bf16 v[88:91], v[216:219], a[120:123], v[88:91]\n"
"v_mul_f32 v143, v127, v143\n"
"v_cvt_pk_bf16_f32 v234, v140, v141\n"
"v_mfma_f32_16x16x32_bf16 v[92:95], v[216:219], a[124:127], v[92:95]\n"
"v_mul_f32 v143, v143, v243\n"
"v_mul_f32 v236, s61, v144\n"
"s_waitcnt lgkmcnt(2)\n"
"v_mfma_f32_16x16x32_bf16 v[96:99], v[220:223], a[112:115], v[96:99]\n"
"ds_read_b64_tr_b16 v[212:213], v226 offset:1024\n"
"v_cvt_pk_bf16_f32 v235, v142, v143\n"
"v_mfma_f32_16x16x32_bf16 v[100:103], v[220:223], a[116:119], v[100:103]\n"
"ds_read_b64_tr_b16 v[214:215], v226 offset:1536\n"
"v_permlane16_swap_b32 v232, v234\n"
"v_mfma_f32_16x16x32_bf16 v[104:107], v[220:223], a[120:123], v[104:107]\n"
"v_exp_f32 v236, v236\n"
"v_mfma_f32_16x16x32_bf16 v[108:111], v[220:223], a[124:127], v[108:111]\n"
"v_permlane16_swap_b32 v233, v235\n"
"v_add_f32 v236, 1.0, v236\n"
"s_waitcnt lgkmcnt(2)\n"
"v_mfma_f32_16x16x32_bf16 v[80:83], v[208:211], a[128:131], v[80:83]\n"
"v_add_u32 v227, 0xfffe0000, v227\n"
"ds_read_b64_tr_b16 v[216:217], v227 offset:0\n"
"v_mfma_f32_16x16x32_bf16 v[84:87], v[208:211], a[132:135], v[84:87]\n"
"ds_read_b64_tr_b16 v[218:219], v227 offset:512\n"
"v_mul_f32 v160, v144, v160\n"
"v_mfma_f32_16x16x32_bf16 v[88:91], v[208:211], a[136:139], v[88:91]\n"
"s_add_u32 s62, %[soffA], 295936\n"
"buffer_store_dwordx4 v[232:235], %[vS], %[srdA], s62 offen sc1\n"
"v_mfma_f32_16x16x32_bf16 v[92:95], v[208:211], a[140:143], v[92:95]\n"
"v_rcp_f32 v236, v236\n"
"s_waitcnt lgkmcnt(2)\n"
"v_mfma_f32_16x16x32_bf16 v[96:99], v[212:215], a[128:131], v[96:99]\n"
"ds_read_b64_tr_b16 v[220:221], v227 offset:1024\n"
"v_mul_f32 v160, v160, v236\n"
"v_mfma_f32_16x16x32_bf16 v[100:103], v[212:215], a[132:135], v[100:103]\n"
"ds_read_b64_tr_b16 v[222:223], v227 offset:1536\n"
"v_mul_f32 v237, s61, v145\n"
"v_mfma_f32_16x16x32_bf16 v[104:107], v[212:215], a[136:139], v[104:107]\n"
"v_exp_f32 v237, v237\n"
"v_mfma_f32_16x16x32_bf16 v[108:111], v[212:215], a[140:143], v[108:111]\n"
"v_add_f32 v237, 1.0, v237\n"
"v_mul_f32 v161, v145, v161\n"
"s_waitcnt lgkmcnt(2)\n"
"v_mfma_f32_16x16x32_bf16 v[112:115], v[216:219], a[0:3], 0\n"
"ds_read_b64_tr_b16 v[208:209], v227 offset:8192\n"
"v_mul_f32 v238, s61, v146\n"
"v_mfma_f32_16x16x32_bf16 v[116:119], v[216:219], a[4:7], 0\n"
"ds_read_b64_tr_b16 v[210:211], v227 offset:8704\n"
"v_mul_f32 v162, v146, v162\n"
"v_mfma_f32_16x16x32_bf16 v[120:123], v[216:219], a[8:11], 0\n"
"v_rcp_f32 v237, v237\n"
"v_mfma_f32_16x16x32_bf16 v[124:127], v[216:219], a[12:15], 0\n"
"v_exp_f32 v238, v238\n"
"s_waitcnt lgkmcnt(2)\n"
"v_mfma_f32_16x16x32_bf16 v[128:131], v[220:223], a[0:3], 0\n"
"ds_read_b64_tr_b16 v[212:213], v227 offset:9216\n"
"v_mul_f32 v161, v161, v237\n"
"v_mfma_f32_16x16x32_bf16 v[132:135], v[220:223], a[4:7], 0\n"
"ds_read_b64_tr_b16 v[214:215], v227 offset:9728\n"
"v_add_f32 v238, 1.0, v238\n"
"v_mfma_f32_16x16x32_bf16 v[136:139], v[220:223], a[8:11], 0\n"
"v_rcp_f32 v238, v238\n"
"v_mfma_f32_16x16x32_bf16 v[140:143], v[220:223], a[12:15], 0\n"
"v_mul_f32 v162, v162, v238\n"
"v_mul_f32 v239, s61, v147\n"
"s_waitcnt lgkmcnt(2)\n"
"v_mfma_f32_16x16x32_bf16 v[112:115], v[208:211], a[16:19], v[112:115]\n"
"ds_read_b64_tr_b16 v[216:217], v227 offset:16384\n"
"v_mul_f32 v163, v147, v163\n"
"v_mfma_f32_16x16x32_bf16 v[116:119], v[208:211], a[20:23], v[116:119]\n"
"ds_read_b64_tr_b16 v[218:219], v227 offset:16896\n"
"v_cvt_pk_bf16_f32 v228, v160, v161\n"
"v_mfma_f32_16x16x32_bf16 v[120:123], v[208:211], a[24:27], v[120:123]\n"
"v_exp_f32 v239, v239\n"
"v_mfma_f32_16x16x32_bf16 v[124:127], v[208:211], a[28:31], v[124:127]\n"
"v_add_f32 v239, 1.0, v239\n"
"v_mul_f32 v240, s61, v148\n"
"s_waitcnt lgkmcnt(2)\n"
"v_mfma_f32_16x16x32_bf16 v[128:131], v[212:215], a[16:19], v[128:131]\n"
"ds_read_b64_tr_b16 v[220:221], v227 offset:17408\n"
"v_mul_f32 v164, v148, v164\n"
"v_mfma_f32_16x16x32_bf16 v[132:135], v[212:215], a[20:23], v[132:135]\n"
"ds_read_b64_tr_b16 v[222:223], v227 offset:17920\n"
"v_mul_f32 v241, s61, v149\n"
"v_mfma_f32_16x16x32_bf16 v[136:139], v[212:215], a[24:27], v[136:139]\n"
"v_rcp_f32 v239, v239\n"
"v_mfma_f32_16x16x32_bf16 v[140:143], v[212:215], a[28:31], v[140:143]\n"
"v_exp_f32 v240, v240\n"
"s_waitcnt lgkmcnt(2)\n"
"v_mfma_f32_16x16x32_bf16 v[112:115], v[216:219], a[32:35], v[112:115]\n"
"ds_read_b64_tr_b16 v[208:209], v227 offset:24576\n"
"v_mul_f32 v163, v163, v239\n"
"v_mfma_f32_16x16x32_bf16 v[116:119], v[216:219], a[36:39], v[116:119]\n"
"ds_read_b64_tr_b16 v[210:211], v227 offset:25088\n"
"v_cvt_pk_bf16_f32 v229, v162, v163\n"
"v_mfma_f32_16x16x32_bf16 v[120:123], v[216:219], a[40:43], v[120:123]\n"
"v_exp_f32 v241, v241\n"
"v_mfma_f32_16x16x32_bf16 v[124:127], v[216:219], a[44:47], v[124:127]\n"
"v_add_f32 v240, 1.0, v240\n"
"v_add_f32 v241, 1.0, v241\n"
"s_waitcnt lgkmcnt(2)\n"
"v_mfma_f32_16x16x32_bf16 v[128:131], v[220:223], a[32:35], v[128:131]\n"
"ds_read_b64_tr_b16 v[212:213], v227 offset:25600\n"
"v_mul_f32 v165, v149, v165\n"
"v_mfma_f32_16x16x32_bf16 v[132:135], v[220:223], a[36:39], v[132:135]\n"
"ds_read_b64_tr_b16 v[214:215], v227 offset:26112\n"
"v_mul_f32 v242, s61, v150\n"
"v_mfma_f32_16x16x32_bf16 v[136:139], v[220:223], a[40:43], v[136:139]\n"
"v_rcp_f32 v240, v240\n"
"v_mfma_f32_16x16x32_bf16 v[140:143], v[220:223], a[44:47], v[140:143]\n"
"v_rcp_f32 v241, v241\n"
"s_waitcnt lgkmcnt(2)\n"
"v_mfma_f32_16x16x32_bf16 v[112:115], v[208:211], a[48:51], v[112:115]\n"
"ds_read_b64_tr_b16 v[216:217], v227 offset:32768\n"
"v_mul_f32 v164, v164, v240\n"
"v_mfma_f32_16x16x32_bf16 v[116:119], v[208:211], a[52:55], v[116:119]\n"
"ds_read_b64_tr_b16 v[218:219], v227 offset:33280\n"
"v_mul_f32 v165, v165, v241\n"
"v_mfma_f32_16x16x32_bf16 v[120:123], v[208:211], a[56:59], v[120:123]\n"
"v_exp_f32 v242, v242\n"
"v_mfma_f32_16x16x32_bf16 v[124:127], v[208:211], a[60:63], v[124:127]\n"
"v_add_f32 v242, 1.0, v242\n"
"v_mul_f32 v166, v150, v166\n"
"s_waitcnt lgkmcnt(2)\n"
"v_mfma_f32_16x16x32_bf16 v[128:131], v[212:215], a[48:51], v[128:131]\n"
"ds_read_b64_tr_b16 v[220:221], v227 offset:33792\n"
"v_mul_f32 v243, s61, v151\n"
"v_mfma_f32_16x16x32_bf16 v[132:135], v[212:215], a[52:55], v[132:135]\n"
"ds_read_b64_tr_b16 v[222:223], v227 offset:34304\n"
"v_mul_f32 v167, v151, v167\n"
"v_mfma_f32_16x16x32_bf16 v[136:139], v[212:215], a[56:59], v[136:139]\n"
"v_rcp_f32 v242, v242\n"
"v_mfma_f32_16x16x32_bf16 v[140:143], v[212:215], a[60:63], v[140:143]\n"
"v_exp_f32 v243, v243\n"
"s_waitcnt lgkmcnt(2)\n"
"v_mfma_f32_16x16x32_bf16 v[112:115], v[216:219], a[64:67], v[112:115]\n"
"ds_read_b64_tr_b16 v[208:209], v227 offset:40960\n"
"v_mul_f32 v166, v166, v242\n"
"v_mfma_f32_16x16x32_bf16 v[116:119], v[216:219], a[68:71], v[116:119]\n"
"ds_read_b64_tr_b16 v[210:211], v227 offset:41472\n"
"v_add_f32 v243, 1.0, v243\n"
"v_mfma_f32_16x16x32_bf16 v[120:123], v[216:219], a[72:75], v[120:123]\n"
"v_rcp_f32 v243, v243\n"
"v_mfma_f32_16x16x32_bf16 v[124:127], v[216:219], a[76:79], v[124:127]\n"
"v_mul_f32 v167, v167, v243\n"
"v_cvt_pk_bf16_f32 v230, v164, v165\n"
"s_waitcnt lgkmcnt(2)\n"
"v_mfma_f32_16x16x32_bf16 v[128:131], v[220:223], a[64:67], v[128:131]\n"
"ds_read_b64_tr_b16 v[212:213], v227 offset:41984\n"
"v_cvt_pk_bf16_f32 v231, v166, v167\n"
"v_mfma_f32_16x16x32_bf16 v[132:135], v[220:223], a[68:71], v[132:135]\n"
"ds_read_b64_tr_b16 v[214:215], v227 offset:42496\n"
"v_permlane16_swap_b32 v228, v230\n"
"v_mfma_f32_16x16x32_bf16 v[136:139], v[220:223], a[72:75], v[136:139]\n"
"v_permlane16_swap_b32 v229, v231\n"
"v_mul_f32 v236, s61, v152\n"
"v_mfma_f32_16x16x32_bf16 v[140:143], v[220:223], a[76:79], v[140:143]\n"
"s_add_u32 s62, %[soffA], 1536\n"
"buffer_store_dwordx4 v[228:231], %[vS], %[srdA], s62 offen sc1\n"
"s_waitcnt lgkmcnt(2)\n"
"v_mfma_f32_16x16x32_bf16 v[112:115], v[208:211], a[80:83], v[112:115]\n"
"ds_read_b64_tr_b16 v[216:217], v227 offset:49152\n"
"v_mul_f32 v168, v152, v168\n"
"v_mfma_f32_16x16x32_bf16 v[116:119], v[208:211], a[84:87], v[116:119]\n"
"ds_read_b64_tr_b16 v[218:219], v227 offset:49664\n"
"v_mul_f32 v237, s61, v153\n"
"v_mfma_f32_16x16x32_bf16 v[120:123], v[208:211], a[88:91], v[120:123]\n"
"v_exp_f32 v236, v236\n"
"v_mfma_f32_16x16x32_bf16 v[124:127], v[208:211], a[92:95], v[124:127]\n"
"v_exp_f32 v237, v237\n"
"s_waitcnt lgkmcnt(2)\n"
"v_mfma_f32_16x16x32_bf16 v[128:131], v[212:215], a[80:83], v[128:131]\n"
"ds_read_b64_tr_b16 v[220:221], v227 offset:50176\n"
"v_add_f32 v236, 1.0, v236\n"
"v_mfma_f32_16x16x32_bf16 v[132:135], v[212:215], a[84:87], v[132:135]\n"
"ds_read_b64_tr_b16 v[222:223], v227 offset:50688\n"
"v_add_f32 v237, 1.0, v237\n"
"v_mfma_f32_16x16x32_bf16 v[136:139], v[212:215], a[88:91], v[136:139]\n"
"v_rcp_f32 v236, v236\n"
"v_mfma_f32_16x16x32_bf16 v[140:143], v[212:215], a[92:95], v[140:143]\n"
"v_rcp_f32 v237, v237\n"
"s_waitcnt lgkmcnt(2)\n"
"v_mfma_f32_16x16x32_bf16 v[112:115], v[216:219], a[96:99], v[112:115]\n"
"ds_read_b64_tr_b16 v[208:209], v227 offset:57344\n"
"v_mul_f32 v168, v168, v236\n"
"v_mfma_f32_16x16x32_bf16 v[116:119], v[216:219], a[100:103], v[116:119]\n"
"ds_read_b64_tr_b16 v[210:211], v227 offset:57856\n"
"v_mul_f32 v169, v153, v169\n"
"v_mfma_f32_16x16x32_bf16 v[120:123], v[216:219], a[104:107], v[120:123]\n"
"v_mul_f32 v169, v169, v237\n"
"v_mul_f32 v238, s61, v154\n"
"v_mfma_f32_16x16x32_bf16 v[124:127], v[216:219], a[108:111], v[124:127]\n"
"v_exp_f32 v238, v238\n"
"s_waitcnt lgkmcnt(2)\n"
"v_mfma_f32_16x16x32_bf16 v[128:131], v[220:223], a[96:99], v[128:131]\n"
"ds_read_b64_tr_b16 v[212:213], v227 offset:58368\n"
"v_add_f32 v238, 1.0, v238\n"
"v_mfma_f32_16x16x32_bf16 v[132:135], v[220:223], a[100:103], v[132:135]\n"
"ds_read_b64_tr_b16 v[214:215], v227 offset:58880\n"
"v_mul_f32 v170, v154, v170\n"
"v_mfma_f32_16x16x32_bf16 v[136:139], v[220:223], a[104:107], v[136:139]\n"
"v_rcp_f32 v238, v238\n"
"v_mfma_f32_16x16x32_bf16 v[140:143], v[220:223], a[108:111], v[140:143]\n"
"v_mul_f32 v170, v170, v238\n"
"v_mul_f32 v239, s61, v155\n"
"s_waitcnt lgkmcnt(2)\n"
"v_mfma_f32_16x16x32_bf16 v[112:115], v[208:211], a[112:115], v[112:115]\n"
"v_add_u32 v227, 65536, v227\n"
"ds_read_b64_tr_b16 v[216:217], v227 offset:0\n"
"v_mfma_f32_16x16x32_bf16 v[116:119], v[208:211], a[116:119], v[116:119]\n"
"ds_read_b64_tr_b16 v[218:219], v227 offset:512\n"
"v_mul_f32 v171, v155, v171\n"
"v_mfma_f32_16x16x32_bf16 v[120:123], v[208:211], a[120:123], v[120:123]\n"
"v_exp_f32 v239, v239\n"
"v_mfma_f32_16x16x32_bf16 v[124:127], v[208:211], a[124:127], v[124:127]\n"
"v_add_f32 v239, 1.0, v239\n"
"v_cvt_pk_bf16_f32 v232, v168, v169\n"
"s_waitcnt lgkmcnt(2)\n"
"v_mfma_f32_16x16x32_bf16 v[128:131], v[212:215], a[112:115], v[128:131]\n"
"ds_read_b64_tr_b16 v[220:221], v227 offset:1024\n"
"v_mul_f32 v240, s61, v156\n"
"v_mfma_f32_16x16x32_bf16 v[132:135], v[212:215], a[116:119], v[132:135]\n"
"ds_read_b64_tr_b16 v[222:223], v227 offset:1536\n"
"v_mul_f32 v172, v156, v172\n"
"v_mfma_f32_16x16x32_bf16 v[136:139], v[212:215], a[120:123], v[136:139]\n"
"v_rcp_f32 v239, v239\n"
"v_mfma_f32_16x16x32_bf16 v[140:143], v[212:215], a[124:127], v[140:143]\n"
"v_exp_f32 v240, v240\n"
"s_waitcnt vmcnt(2)\n"
"s_barrier\n"
"s_waitcnt lgkmcnt(2)\n"
"v_mfma_f32_16x16x32_bf16 v[112:115], v[216:219], a[128:131], v[112:115]\n"
"ds_read_b64_tr_b16 v[208:209], v224 offset:8192\n"
"v_mul_f32 v171, v171, v239\n"
"v_mfma_f32_16x16x32_bf16 v[116:119], v[216:219], a[132:135], v[116:119]\n"
"ds_read_b64_tr_b16 v[210:211], v224 offset:8704\n"
"v_cvt_pk_bf16_f32 v233, v170, v171\n"
"v_mfma_f32_16x16x32_bf16 v[120:123], v[216:219], a[136:139], v[120:123]\n"
"v_add_f32 v240, 1.0, v240\n"
"v_mul_f32 v241, s61, v157\n"
"v_mfma_f32_16x16x32_bf16 v[124:127], v[216:219], a[140:143], v[124:127]\n"
"v_rcp_f32 v240, v240\n"
"s_waitcnt lgkmcnt(2)\n"
"v_mfma_f32_16x16x32_bf16 v[128:131], v[220:223], a[128:131], v[128:131]\n"
"ds_read_b64_tr_b16 v[212:213], v224 offset:9216\n"
"v_mul_f32 v172, v172, v240\n"
"v_mfma_f32_16x16x32_bf16 v[132:135], v[220:223], a[132:135], v[132:135]\n"
"ds_read_b64_tr_b16 v[214:215], v224 offset:9728\n"
"v_mul_f32 v173, v157, v173\n"
"v_mfma_f32_16x16x32_bf16 v[136:139], v[220:223], a[136:139], v[136:139]\n"
"v_exp_f32 v241, v241\n"
"v_mfma_f32_16x16x32_bf16 v[140:143], v[220:223], a[140:143], v[140:143]\n"
"v_add_f32 v241, 1.0, v241\n"
"v_mul_f32 v242, s61, v158\n"
"s_waitcnt lgkmcnt(2)\n"
"v_mfma_f32_16x16x32_bf16 v[176:179], v[208:211], a[144:147], v[176:179]\n"
"ds_read_b64_tr_b16 v[216:217], v224 offset:16384\n"
"v_mul_f32 v174, v158, v174\n"
"v_mfma_f32_16x16x32_bf16 v[180:183], v[208:211], a[148:151], v[180:183]\n"
"ds_read_b64_tr_b16 v[218:219], v224 offset:16896\n"
"v_mul_f32 v243, s61, v159\n"
"v_mfma_f32_16x16x32_bf16 v[184:187], v[208:211], a[152:155], v[184:187]\n"
"s_add_u32 m0, %[m0R], 0\n"
"s_add_u32 s60, %[soffR], 0\n"
"buffer_load_dwordx4 %[vR], %[srdR], s60 offen lds\n"
"v_mfma_f32_16x16x32_bf16 v[188:191], v[208:211], a[156:159], v[188:191]\n"
"s_add_u32 m0, %[m0R], 4096\n"
"s_add_u32 s60, %[soffR], 4096\n"
"buffer_load_dwordx4 %[vR], %[srdR], s60 offen lds\n"
"s_waitcnt lgkmcnt(2)\n"
"v_mfma_f32_16x16x32_bf16 v[192:195], v[212:215], a[144:147], v[192:195]\n"
"ds_read_b64_tr_b16 v[220:221], v224 offset:17408\n"
"v_mul_f32 v175, v159, v175\n"
"v_mfma_f32_16x16x32_bf16 v[196:199], v[212:215], a[148:151], v[196:199]\n"
"ds_read_b64_tr_b16 v[222:223], v224 offset:17920\n"
"v_mfma_f32_16x16x32_bf16 v[200:203], v[212:215], a[152:155], v[200:203]\n"
"s_add_u32 m0, %[m0R], 8192\n"
"s_add_u32 s60, %[soffR], 8192\n"
"buffer_load_dwordx4 %[vR], %[srdR], s60 offen lds\n"
"v_mfma_f32_16x16x32_bf16 v[204:207], v[212:215], a[156:159], v[204:207]\n"
"v_rcp_f32 v241, v241\n"
"s_waitcnt lgkmcnt(2)\n"
"v_mfma_f32_16x16x32_bf16 v[176:179], v[216:219], a[160:163], v[176:179]\n"
"ds_read_b64_tr_b16 v[208:209], v224 offset:24576\n"
"v_mul_f32 v173, v173, v241\n"
"v_mfma_f32_16x16x32_bf16 v[180:183], v[216:219], a[164:167], v[180:183]\n"
"ds_read_b64_tr_b16 v[210:211], v224 offset:25088\n"
"v_cvt_pk_bf16_f32 v234, v172, v173\n"
"v_mfma_f32_16x16x32_bf16 v[184:187], v[216:219], a[168:171], v[184:187]\n"
"s_add_u32 m0, %[m0R], 12288\n"
"s_add_u32 s60, %[soffR], 12288\n"
"buffer_load_dwordx4 %[vR], %[srdR], s60 offen lds\n"
"v_mfma_f32_16x16x32_bf16 v[188:191], v[216:219], a[172:175], v[188:191]\n"
"s_add_u32 m0, %[m0R], 16384\n"
"s_add_u32 s60, %[soffR], 16384\n"
"buffer_load_dwordx4 %[vR], %[srdR], s60 offen lds\n"
"s_waitcnt lgkmcnt(2)\n"
"v_mfma_f32_16x16x32_bf16 v[192:195], v[220:223], a[160:163], v[192:195]\n"
"ds_read_b64_tr_b16 v[212:213], v224 offset:25600\n"
"v_mfma_f32_16x16x32_bf16 v[196:199], v[220:223], a[164:167], v[196:199]\n"
"ds_read_b64_tr_b16 v[214:215], v224 offset:26112\n"
"v_mfma_f32_16x16x32_bf16 v[200:203], v[220:223], a[168:171], v[200:203]\n"
"s_add_u32 m0, %[m0R], 20480\n"
"s_add_u32 s60, %[soffR], 20480\n"
"buffer_load_dwordx4 %[vR], %[srdR], s60 offen lds\n"
"v_mfma_f32_16x16x32_bf16 v[204:207], v[220:223], a[172:175], v[204:207]\n"
"s_add_u32 m0, %[m0R], 24576\n"
"s_add_u32 s60, %[soffR], 24576\n"
"buffer_load_dwordx4 %[vR], %[srdR], s60 offen lds\n"
"s_waitcnt lgkmcnt(2)\n"
"v_mfma_f32_16x16x32_bf16 v[176:179], v[208:211], a[176:179], v[176:179]\n"
"ds_read_b64_tr_b16 v[216:217], v224 offset:32768\n"
"v_mfma_f32_16x16x32_bf16 v[180:183], v[208:211], a[180:183], v[180:183]\n"
"ds_read_b64_tr_b16 v[218:219], v224 offset:33280\n"
"v_mfma_f32_16x16x32_bf16 v[184:187], v[208:211], a[184:187], v[184:187]\n"
"s_add_u32 m0, %[m0R], 28672\n"
"s_add_u32 s60, %[soffR], 28672\n"
"buffer_load_dwordx4 %[vR], %[srdR], s60 offen lds\n"
"v_mfma_f32_16x16x32_bf16 v[188:191], v[208:211], a[188:191], v[188:191]\n"
"v_exp_f32 v242, v242\n"
"s_waitcnt lgkmcnt(2)\n"
"v_mfma_f32_16x16x32_bf16 v[192:195], v[212:215], a[176:179], v[192:195]\n"
"ds_read_b64_tr_b16 v[220:221], v224 offset:33792\n"
"v_add_f32 v242, 1.0, v242\n"
"v_mfma_f32_16x16x32_bf16 v[196:199], v[212:215], a[180:183], v[196:199]\n"
"ds_read_b64_tr_b16 v[222:223], v224 offset:34304\n"
"v_mfma_f32_16x16x32_bf16 v[200:203], v[212:215], a[184:187], v[200:203]\n"
"s_add_u32 m0, %[m0R], 32768\n"
"s_add_u32 s60, %[soffR], 32768\n"
"buffer_load_dwordx4 %[vR], %[srdR], s60 offen lds\n"
"v_mfma_f32_16x16x32_bf16 v[204:207], v[212:215], a[188:191], v[204:207]\n"
"v_rcp_f32 v242, v242\n"
"s_waitcnt lgkmcnt(2)\n"
"v_mfma_f32_16x16x32_bf16 v[176:179], v[216:219], a[192:195], v[176:179]\n"
"ds_read_b64_tr_b16 v[208:209], v224 offset:40960\n"
"v_mul_f32 v174, v174, v242\n"
"v_mfma_f32_16x16x32_bf16 v[180:183], v[216:219], a[196:199], v[180:183]\n"
"ds_read_b64_tr_b16 v[210:211], v224 offset:41472\n"
"v_mfma_f32_16x16x32_bf16 v[184:187], v[216:219], a[200:203], v[184:187]\n"
"v_exp_f32 v243, v243\n"
"v_mfma_f32_16x16x32_bf16 v[188:191], v[216:219], a[204:207], v[188:191]\n"
"v_add_f32 v243, 1.0, v243\n"
"s_waitcnt lgkmcnt(2)\n"
"v_mfma_f32_16x16x32_bf16 v[192:195], v[220:223], a[192:195], v[192:195]\n"
"ds_read_b64_tr_b16 v[212:213], v224 offset:41984\n"
"v_mfma_f32_16x16x32_bf16 v[196:199], v[220:223], a[196:199], v[196:199]\n"
"ds_read_b64_tr_b16 v[214:215], v224 offset:42496\n"
"v_mfma_f32_16x16x32_bf16 v[200:203], v[220:223], a[200:203], v[200:203]\n"
"v_rcp_f32 v243, v243\n"
"v_mfma_f32_16x16x32_bf16 v[204:207], v[220:223], a[204:207], v[204:207]\n"
"buffer_load_dword v244, %[vPF], %[srdPF], 0 offen\n"
"s_waitcnt lgkmcnt(2)\n"
"v_mfma_f32_16x16x32_bf16 v[176:179], v[208:211], a[208:211], v[176:179]\n"
"ds_read_b64_tr_b16 v[216:217], v224 offset:49152\n"
"v_mul_f32 v175, v175, v243\n"
"v_mfma_f32_16x16x32_bf16 v[180:183], v[208:211], a[212:215], v[180:183]\n"
"ds_read_b64_tr_b16 v[218:219], v224 offset:49664\n"
"v_cvt_pk_bf16_f32 v235, v174, v175\n"
"v_mfma_f32_16x16x32_bf16 v[184:187], v[208:211], a[216:219], v[184:187]\n"
"v_mfma_f32_16x16x32_bf16 v[188:191], v[208:211], a[220:223], v[188:191]\n"
"v_permlane16_swap_b32 v232, v234\n"
"v_permlane16_swap_b32 v233, v235\n"
"s_waitcnt lgkmcnt(2)\n"
"v_mfma_f32_16x16x32_bf16 v[192:195], v[212:215], a[208:211], v[192:195]\n"
"ds_read_b64_tr_b16 v[220:221], v224 offset:50176\n"
"v_mfma_f32_16x16x32_bf16 v[196:199], v[212:215], a[212:215], v[196:199]\n"
"ds_read_b64_tr_b16 v[222:223], v224 offset:50688\n"
"v_mfma_f32_16x16x32_bf16 v[200:203], v[212:215], a[216:219], v[200:203]\n"
"buffer_load_dword v244, %[vC], %[srdC], 0 offen\n"
"v_mfma_f32_16x16x32_bf16 v[204:207], v[212:215], a[220:223], v[204:207]\n"
"s_add_u32 s62, %[soffA], 296448\n"
"buffer_store_dwordx4 v[232:235], %[vS], %[srdA], s62 offen sc1\n"
"s_waitcnt lgkmcnt(2)\n"
"v_mfma_f32_16x16x32_bf16 v[176:179], v[216:219], a[224:227], v[176:179]\n"
"ds_read_b64_tr_b16 v[208:209], v224 offset:57344\n"
"v_mfma_f32_16x16x32_bf16 v[180:183], v[216:219], a[228:231], v[180:183]\n"
"ds_read_b64_tr_b16 v[210:211], v224 offset:57856\n"
"v_mfma_f32_16x16x32_bf16 v[184:187], v[216:219], a[232:235], v[184:187]\n"
"v_mfma_f32_16x16x32_bf16 v[188:191], v[216:219], a[236:239], v[188:191]\n"
"s_waitcnt lgkmcnt(2)\n"
"v_mfma_f32_16x16x32_bf16 v[192:195], v[220:223], a[224:227], v[192:195]\n"
"ds_read_b64_tr_b16 v[212:213], v224 offset:58368\n"
"v_mfma_f32_16x16x32_bf16 v[196:199], v[220:223], a[228:231], v[196:199]\n"
"ds_read_b64_tr_b16 v[214:215], v224 offset:58880\n"
"v_mfma_f32_16x16x32_bf16 v[200:203], v[220:223], a[232:235], v[200:203]\n"
"v_mfma_f32_16x16x32_bf16 v[204:207], v[220:223], a[236:239], v[204:207]\n"
"s_waitcnt lgkmcnt(2)\n"
"v_mfma_f32_16x16x32_bf16 v[176:179], v[208:211], a[240:243], v[176:179]\n"
"v_add_u32 v224, 65536, v224\n"
"ds_read_b64_tr_b16 v[216:217], v224 offset:0\n"
"v_mfma_f32_16x16x32_bf16 v[180:183], v[208:211], a[244:247], v[180:183]\n"
"ds_read_b64_tr_b16 v[218:219], v224 offset:512\n"
"v_mfma_f32_16x16x32_bf16 v[184:187], v[208:211], a[248:251], v[184:187]\n"
"v_mfma_f32_16x16x32_bf16 v[188:191], v[208:211], a[252:255], v[188:191]\n"
"s_waitcnt lgkmcnt(2)\n"
"v_mfma_f32_16x16x32_bf16 v[192:195], v[212:215], a[240:243], v[192:195]\n"
"ds_read_b64_tr_b16 v[220:221], v224 offset:1024\n"
"v_mfma_f32_16x16x32_bf16 v[196:199], v[212:215], a[244:247], v[196:199]\n"
"ds_read_b64_tr_b16 v[222:223], v224 offset:1536\n"
"v_mfma_f32_16x16x32_bf16 v[200:203], v[212:215], a[248:251], v[200:203]\n"
"v_mfma_f32_16x16x32_bf16 v[204:207], v[212:215], a[252:255], v[204:207]\n"
"s_waitcnt lgkmcnt(2)\n"
"v_mfma_f32_16x16x32_bf16 v[176:179], v[216:219], v[16:19], v[176:179]\n"
"ds_read_b64_tr_b16 v[208:209], v224 offset:8192\n"
"v_mfma_f32_16x16x32_bf16 v[180:183], v[216:219], v[20:23], v[180:183]\n"
"ds_read_b64_tr_b16 v[210:211], v224 offset:8704\n"
"v_mfma_f32_16x16x32_bf16 v[184:187], v[216:219], v[24:27], v[184:187]\n"
"v_mfma_f32_16x16x32_bf16 v[188:191], v[216:219], v[28:31], v[188:191]\n"
"s_waitcnt lgkmcnt(2)\n"
"v_mfma_f32_16x16x32_bf16 v[192:195], v[220:223], v[16:19], v[192:195]\n"
"ds_read_b64_tr_b16 v[212:213], v224 offset:9216\n"
"v_mfma_f32_16x16x32_bf16 v[196:199], v[220:223], v[20:23], v[196:199]\n"
"ds_read_b64_tr_b16 v[214:215], v224 offset:9728\n"
"v_mfma_f32_16x16x32_bf16 v[200:203], v[220:223], v[24:27], v[200:203]\n"
"v_mfma_f32_16x16x32_bf16 v[204:207], v[220:223], v[28:31], v[204:207]\n"
"s_waitcnt lgkmcnt(2)\n"
"v_mfma_f32_16x16x32_bf16 v[176:179], v[208:211], v[32:35], v[176:179]\n"
"ds_read_b64_tr_b16 v[216:217], v225 offset:8192\n"
"v_mfma_f32_16x16x32_bf16 v[180:183], v[208:211], v[36:39], v[180:183]\n"
"ds_read_b64_tr_b16 v[218:219], v225 offset:8704\n"
"v_mfma_f32_16x16x32_bf16 v[184:187], v[208:211], v[40:43], v[184:187]\n"
"v_mfma_f32_16x16x32_bf16 v[188:191], v[208:211], v[44:47], v[188:191]\n"
"s_waitcnt lgkmcnt(2)\n"
"v_mfma_f32_16x16x32_bf16 v[192:195], v[212:215], v[32:35], v[192:195]\n"
"ds_read_b64_tr_b16 v[220:221], v225 offset:9216\n"
"v_mfma_f32_16x16x32_bf16 v[196:199], v[212:215], v[36:39], v[196:199]\n"
"ds_read_b64_tr_b16 v[222:223], v225 offset:9728\n"
"v_mfma_f32_16x16x32_bf16 v[200:203], v[212:215], v[40:43], v[200:203]\n"
"v_mfma_f32_16x16x32_bf16 v[204:207], v[212:215], v[44:47], v[204:207]\n"
"s_waitcnt lgkmcnt(2)\n"
"v_mfma_f32_16x16x32_bf16 v[48:51], v[216:219], a[144:147], v[48:51]\n"
"ds_read_b64_tr_b16 v[208:209], v225 offset:16384\n"
"v_mfma_f32_16x16x32_bf16 v[52:55], v[216:219], a[148:151], v[52:55]\n"
"ds_read_b64_tr_b16 v[210:211], v225 offset:16896\n"
"v_mul_f32 v236, s61, v176\n"
"v_mfma_f32_16x16x32_bf16 v[56:59], v[216:219], a[152:155], v[56:59]\n"
"v_exp_f32 v236, v236\n"
"v_mfma_f32_16x16x32_bf16 v[60:63], v[216:219], a[156:159], v[60:63]\n"
"v_add_f32 v236, 1.0, v236\n"
"v_mul_f32 v192, v176, v192\n"
"s_waitcnt lgkmcnt(2)\n"
"v_mfma_f32_16x16x32_bf16 v[64:67], v[220:223], a[144:147], v[64:67]\n"
"ds_read_b64_tr_b16 v[212:213], v225 offset:17408\n"
"v_mul_f32 v237, s61, v177\n"
"v_mfma_f32_16x16x32_bf16 v[68:71], v[220:223], a[148:151], v[68:71]\n"
"ds_read_b64_tr_b16 v[214:215], v225 offset:17920\n"
"v_mul_f32 v193, v177, v193\n"
"v_mfma_f32_16x16x32_bf16 v[72:75], v[220:223], a[152:155], v[72:75]\n"
"v_rcp_f32 v236, v236\n"
"v_mfma_f32_16x16x32_bf16 v[76:79], v[220:223], a[156:159], v[76:79]\n"
"v_exp_f32 v237, v237\n"
"s_waitcnt lgkmcnt(2)\n"
"v_mfma_f32_16x16x32_bf16 v[48:51], v[208:211], a[160:163], v[48:51]\n"
"ds_read_b64_tr_b16 v[216:217], v225 offset:24576\n"
"v_mul_f32 v192, v192, v236\n"
"v_mfma_f32_16x16x32_bf16 v[52:55], v[208:211], a[164:167], v[52:55]\n"
"ds_read_b64_tr_b16 v[218:219], v225 offset:25088\n"
"v_add_f32 v237, 1.0, v237\n"
"v_mfma_f32_16x16x32_bf16 v[56:59], v[208:211], a[168:171], v[56:59]\n"
"v_rcp_f32 v237, v237\n"
"v_mfma_f32_16x16x32_bf16 v[60:63], v[208:211], a[172:175], v[60:63]\n"
"v_mul_f32 v193, v193, v237\n"
"v_mul_f32 v238, s61, v178\n"
"s_waitcnt lgkmcnt(2)\n"
"v_mfma_f32_16x16x32_bf16 v[64:67], v[212:215], a[160:163], v[64:67]\n"
"ds_read_b64_tr_b16 v[220:221], v225 offset:25600\n"
"v_mul_f32 v194, v178, v194\n"
"v_mfma_f32_16x16x32_bf16 v[68:71], v[212:215], a[164:167], v[68:71]\n"
"ds_read_b64_tr_b16 v[222:223], v225 offset:26112\n"
"v_mul_f32 v239, s61, v179\n"
"v_mfma_f32_16x16x32_bf16 v[72:75], v[212:215], a[168:171], v[72:75]\n"
"v_exp_f32 v238, v238\n"
"v_mfma_f32_16x16x32_bf16 v[76:79], v[212:215], a[172:175], v[76:79]\n"
"v_exp_f32 v239, v239\n"
"s_waitcnt lgkmcnt(2)\n"
"v_mfma_f32_16x16x32_bf16 v[48:51], v[216:219], a[176:179], v[48:51]\n"
"ds_read_b64_tr_b16 v[208:209], v225 offset:32768\n"
"v_add_f32 v238, 1.0, v238\n"
"v_mfma_f32_16x16x32_bf16 v[52:55], v[216:219], a[180:183], v[52:55]\n"
"ds_read_b64_tr_b16 v[210:211], v225 offset:33280\n"
"v_add_f32 v239, 1.0, v239\n"
"v_mfma_f32_16x16x32_bf16 v[56:59], v[216:219], a[184:187], v[56:59]\n"
"v_rcp_f32 v238, v238\n"
"v_mfma_f32_16x16x32_bf16 v[60:63], v[216:219], a[188:191], v[60:63]\n"
"v_rcp_f32 v239, v239\n"
"s_waitcnt lgkmcnt(2)\n"
"v_mfma_f32_16x16x32_bf16 v[64:67], v[220:223], a[176:179], v[64:67]\n"
"ds_read_b64_tr_b16 v[212:213], v225 offset:33792\n"
"v_mul_f32 v194, v194, v238\n"
"v_mfma_f32_16x16x32_bf16 v[68:71], v[220:223], a[180:183], v[68:71]\n"
"ds_read_b64_tr_b16 v[214:215], v225 offset:34304\n"
"v_mul_f32 v195, v179, v195\n"
"v_mfma_f32_16x16x32_bf16 v[72:75], v[220:223], a[184:187], v[72:75]\n"
"v_mul_f32 v195, v195, v239\n"
"v_cvt_pk_bf16_f32 v228, v192, v193\n"
"v_mfma_f32_16x16x32_bf16 v[76:79], v[220:223], a[188:191], v[76:79]\n"
"v_cvt_pk_bf16_f32 v229, v194, v195\n"
"v_mul_f32 v240, s61, v180\n"
"s_waitcnt lgkmcnt(2)\n"
"v_mfma_f32_16x16x32_bf16 v[48:51], v[208:211], a[192:195], v[48:51]\n"
"ds_read_b64_tr_b16 v[216:217], v225 offset:40960\n"
"v_mul_f32 v196, v180, v196\n"
"v_mfma_f32_16x16x32_bf16 v[52:55], v[208:211], a[196:199], v[52:55]\n"
"ds_read_b64_tr_b16 v[218:219], v225 offset:41472\n"
"v_mul_f32 v241, s61, v181\n"
"v_mfma_f32_16x16x32_bf16 v[56:59], v[208:211], a[200:203], v[56:59]\n"
"v_exp_f32 v240, v240\n"
"v_mfma_f32_16x16x32_bf16 v[60:63], v[208:211], a[204:207], v[60:63]\n"
"v_exp_f32 v241, v241\n"
"s_waitcnt lgkmcnt(2)\n"
"v_mfma_f32_16x16x32_bf16 v[64:67], v[212:215], a[192:195], v[64:67]\n"
"ds_read_b64_tr_b16 v[220:221], v225 offset:41984\n"
"v_add_f32 v240, 1.0, v240\n"
"v_mfma_f32_16x16x32_bf16 v[68:71], v[212:215], a[196:199], v[68:71]\n"
"ds_read_b64_tr_b16 v[222:223], v225 offset:42496\n"
"v_add_f32 v241, 1.0, v241\n"
"v_mfma_f32_16x16x32_bf16 v[72:75], v[212:215], a[200:203], v[72:75]\n"
"v_rcp_f32 v240, v240\n"
"v_mfma_f32_16x16x32_bf16 v[76:79], v[212:215], a[204:207], v[76:79]\n"
"v_rcp_f32 v241, v241\n"
"s_waitcnt lgkmcnt(2)\n"
"v_mfma_f32_16x16x32_bf16 v[48:51], v[216:219], a[208:211], v[48:51]\n"
"ds_read_b64_tr_b16 v[208:209], v225 offset:49152\n"
"v_mul_f32 v196, v196, v240\n"
"v_mfma_f32_16x16x32_bf16 v[52:55], v[216:219], a[212:215], v[52:55]\n"
"ds_read_b64_tr_b16 v[210:211], v225 offset:49664\n"
"v_mul_f32 v197, v181, v197\n"
"v_mfma_f32_16x16x32_bf16 v[56:59], v[216:219], a[216:219], v[56:59]\n"
"v_mul_f32 v197, v197, v241\n"
"v_mul_f32 v242, s61, v182\n"
"v_mfma_f32_16x16x32_bf16 v[60:63], v[216:219], a[220:223], v[60:63]\n"
"v_exp_f32 v242, v242\n"
"s_waitcnt lgkmcnt(2)\n"
"v_mfma_f32_16x16x32_bf16 v[64:67], v[220:223], a[208:211], v[64:67]\n"
"ds_read_b64_tr_b16 v[212:213], v225 offset:50176\n"
"v_add_f32 v242, 1.0, v242\n"
"v_mfma_f32_16x16x32_bf16 v[68:71], v[220:223], a[212:215], v[68:71]\n"
"ds_read_b64_tr_b16 v[214:215], v225 offset:50688\n"
"v_mul_f32 v198, v182, v198\n"
"v_mfma_f32_16x16x32_bf16 v[72:75], v[220:223], a[216:219], v[72:75]\n"
"v_rcp_f32 v242, v242\n"
"v_mfma_f32_16x16x32_bf16 v[76:79], v[220:223], a[220:223], v[76:79]\n"
"v_mul_f32 v198, v198, v242\n"
"v_mul_f32 v243, s61, v183\n"
"s_waitcnt lgkmcnt(2)\n"
"v_mfma_f32_16x16x32_bf16 v[48:51], v[208:211], a[224:227], v[48:51]\n"
"ds_read_b64_tr_b16 v[216:217], v225 offset:57344\n"
"v_mul_f32 v199, v183, v199\n"
"v_mfma_f32_16x16x32_bf16 v[52:55], v[208:211], a[228:231], v[52:55]\n"
"ds_read_b64_tr_b16 v[218:219], v225 offset:57856\n"
"v_cvt_pk_bf16_f32 v230, v196, v197\n"
"v_mfma_f32_16x16x32_bf16 v[56:59], v[208:211], a[232:235], v[56:59]\n"
"v_exp_f32 v243, v243\n"
"v_mfma_f32_16x16x32_bf16 v[60:63], v[208:211], a[236:239], v[60:63]\n"
"v_add_f32 v243, 1.0, v243\n"
"v_mul_f32 v236, s61, v184\n"
"s_waitcnt lgkmcnt(2)\n"
"v_mfma_f32_16x16x32_bf16 v[64:67], v[212:215], a[224:227], v[64:67]\n"
"ds_read_b64_tr_b16 v[220:221], v225 offset:58368\n"
"v_mul_f32 v200, v184, v200\n"
"v_mfma_f32_16x16x32_bf16 v[68:71], v[212:215], a[228:231], v[68:71]\n"
"ds_read_b64_tr_b16 v[222:223], v225 offset:58880\n"
"v_mul_f32 v237, s61, v185\n"
"v_mfma_f32_16x16x32_bf16 v[72:75], v[212:215], a[232:235], v[72:75]\n"
"v_rcp_f32 v243, v243\n"
"v_mfma_f32_16x16x32_bf16 v[76:79], v[212:215], a[236:239], v[76:79]\n"
"v_exp_f32 v236, v236\n"
"s_waitcnt lgkmcnt(2)\n"
"v_mfma_f32_16x16x32_bf16 v[48:51], v[216:219], a[240:243], v[48:51]\n"
"v_add_u32 v225, 65536, v225\n"
"ds_read_b64_tr_b16 v[208:209], v225 offset:0\n"
"v_mfma_f32_16x16x32_bf16 v[52:55], v[216:219], a[244:247], v[52:55]\n"
"ds_read_b64_tr_b16 v[210:211], v225 offset:512\n"
"v_mul_f32 v199, v199, v243\n"
"v_mfma_f32_16x16x32_bf16 v[56:59], v[216:219], a[248:251], v[56:59]\n"
"v_exp_f32 v237, v237\n"
"v_mfma_f32_16x16x32_bf16 v[60:63], v[216:219], a[252:255], v[60:63]\n"
"v_cvt_pk_bf16_f32 v231, v198, v199\n"
"v_add_f32 v236, 1.0, v236\n"
"s_waitcnt lgkmcnt(2)\n"
"v_mfma_f32_16x16x32_bf16 v[64:67], v[220:223], a[240:243], v[64:67]\n"
"ds_read_b64_tr_b16 v[212:213], v225 offset:1024\n"
"v_permlane16_swap_b32 v228, v230\n"
"v_mfma_f32_16x16x32_bf16 v[68:71], v[220:223], a[244:247], v[68:71]\n"
"ds_read_b64_tr_b16 v[214:215], v225 offset:1536\n"
"v_permlane16_swap_b32 v229, v231\n"
"v_mfma_f32_16x16x32_bf16 v[72:75], v[220:223], a[248:251], v[72:75]\n"
"s_add_u32 s62, %[soffA], 2048\n"
"buffer_store_dwordx4 v[228:231], %[vS], %[srdA], s62 offen sc1\n"
"v_mfma_f32_16x16x32_bf16 v[76:79], v[220:223], a[252:255], v[76:79]\n"
"v_rcp_f32 v236, v236\n"
"s_waitcnt lgkmcnt(2)\n"
"v_mfma_f32_16x16x32_bf16 v[48:51], v[208:211], v[16:19], v[48:51]\n"
"ds_read_b64_tr_b16 v[216:217], v225 offset:8192\n"
"v_mul_f32 v200, v200, v236\n"
"v_mfma_f32_16x16x32_bf16 v[52:55], v[208:211], v[20:23], v[52:55]\n"
"ds_read_b64_tr_b16 v[218:219], v225 offset:8704\n"
"v_add_f32 v237, 1.0, v237\n"
"v_mfma_f32_16x16x32_bf16 v[56:59], v[208:211], v[24:27], v[56:59]\n"
"v_rcp_f32 v237, v237\n"
"v_mfma_f32_16x16x32_bf16 v[60:63], v[208:211], v[28:31], v[60:63]\n"
"v_mul_f32 v201, v185, v201\n"
"v_mul_f32 v238, s61, v186\n"
"s_waitcnt lgkmcnt(2)\n"
"v_mfma_f32_16x16x32_bf16 v[64:67], v[212:215], v[16:19], v[64:67]\n"
"ds_read_b64_tr_b16 v[220:221], v225 offset:9216\n"
"v_mul_f32 v201, v201, v237\n"
"v_mfma_f32_16x16x32_bf16 v[68:71], v[212:215], v[20:23], v[68:71]\n"
"ds_read_b64_tr_b16 v[222:223], v225 offset:9728\n"
"v_mul_f32 v202, v186, v202\n"
"v_mfma_f32_16x16x32_bf16 v[72:75], v[212:215], v[24:27], v[72:75]\n"
"v_exp_f32 v238, v238\n"
"v_mfma_f32_16x16x32_bf16 v[76:79], v[212:215], v[28:31], v[76:79]\n"
"v_add_f32 v238, 1.0, v238\n"
"v_mul_f32 v239, s61, v187\n"
"s_waitcnt lgkmcnt(2)\n"
"v_mfma_f32_16x16x32_bf16 v[48:51], v[216:219], v[32:35], v[48:51]\n"
"ds_read_b64_tr_b16 v[208:209], v226 offset:8192\n"
"v_mul_f32 v203, v187, v203\n"
"v_mfma_f32_16x16x32_bf16 v[52:55], v[216:219], v[36:39], v[52:55]\n"
"ds_read_b64_tr_b16 v[210:211], v226 offset:8704\n"
"v_cvt_pk_bf16_f32 v232, v200, v201\n"
"v_mfma_f32_16x16x32_bf16 v[56:59], v[216:219], v[40:43], v[56:59]\n"
"v_rcp_f32 v238, v238\n"
"v_mfma_f32_16x16x32_bf16 v[60:63], v[216:219], v[44:47], v[60:63]\n"
"v_exp_f32 v239, v239\n"
"s_waitcnt lgkmcnt(2)\n"
"v_mfma_f32_16x16x32_bf16 v[64:67], v[220:223], v[32:35], v[64:67]\n"
"ds_read_b64_tr_b16 v[212:213], v226 offset:9216\n"
"v_mul_f32 v202, v202, v238\n"
"v_mfma_f32_16x16x32_bf16 v[68:71], v[220:223], v[36:39], v[68:71]\n"
"ds_read_b64_tr_b16 v[214:215], v226 offset:9728\n"
"v_add_f32 v239, 1.0, v239\n"
"v_mfma_f32_16x16x32_bf16 v[72:75], v[220:223], v[40:43], v[72:75]\n"
"v_rcp_f32 v239, v239\n"
"v_mfma_f32_16x16x32_bf16 v[76:79], v[220:223], v[44:47], v[76:79]\n"
"v_mul_f32 v203, v203, v239\n"
"v_mul_f32 v240, s61, v188\n"
"s_waitcnt lgkmcnt(2)\n"
"v_mfma_f32_16x16x32_bf16 v[80:83], v[208:211], a[144:147], v[80:83]\n"
"ds_read_b64_tr_b16 v[216:217], v226 offset:16384\n"
"v_cvt_pk_bf16_f32 v233, v202, v203\n"
"v_mfma_f32_16x16x32_bf16 v[84:87], v[208:211], a[148:151], v[84:87]\n"
"ds_read_b64_tr_b16 v[218:219], v226 offset:16896\n"
"v_mul_f32 v204, v188, v204\n"
"v_mfma_f32_16x16x32_bf16 v[88:91], v[208:211], a[152:155], v[88:91]\n"
"v_exp_f32 v240, v240\n"
"v_mfma_f32_16x16x32_bf16 v[92:95], v[208:211], a[156:159], v[92:95]\n"
"v_add_f32 v240, 1.0, v240\n"
"v_mul_f32 v241, s61, v189\n"
"s_waitcnt lgkmcnt(2)\n"
"v_mfma_f32_16x16x32_bf16 v[96:99], v[212:215], a[144:147], v[96:99]\n"
"ds_read_b64_tr_b16 v[220:221], v226 offset:17408\n"
"v_mul_f32 v205, v189, v205\n"
"v_mfma_f32_16x16x32_bf16 v[100:103], v[212:215], a[148:151], v[100:103]\n"
"ds_read_b64_tr_b16 v[222:223], v226 offset:17920\n"
"v_mul_f32 v242, s61, v190\n"
"v_mfma_f32_16x16x32_bf16 v[104:107], v[212:215], a[152:155], v[104:107]\n"
"v_rcp_f32 v240, v240\n"
"v_mfma_f32_16x16x32_bf16 v[108:111], v[212:215], a[156:159], v[108:111]\n"
"v_exp_f32 v241, v241\n"
"s_waitcnt lgkmcnt(2)\n"
"v_mfma_f32_16x16x32_bf16 v[80:83], v[216:219], a[160:163], v[80:83]\n"
"ds_read_b64_tr_b16 v[208:209], v226 offset:24576\n"
"v_mul_f32 v204, v204, v240\n"
"v_mfma_f32_16x16x32_bf16 v[84:87], v[216:219], a[164:167], v[84:87]\n"
"ds_read_b64_tr_b16 v[210:211], v226 offset:25088\n"
"v_add_f32 v241, 1.0, v241\n"
"v_mfma_f32_16x16x32_bf16 v[88:91], v[216:219], a[168:171], v[88:91]\n"
"v_rcp_f32 v241, v241\n"
"v_mfma_f32_16x16x32_bf16 v[92:95], v[216:219], a[172:175], v[92:95]\n"
"v_exp_f32 v242, v242\n"
"s_waitcnt lgkmcnt(2)\n"
"v_mfma_f32_16x16x32_bf16 v[96:99], v[220:223], a[160:163], v[96:99]\n"
"ds_read_b64_tr_b16 v[212:213], v226 offset:25600\n"
"v_mul_f32 v205, v205, v241\n"
"v_mfma_f32_16x16x32_bf16 v[100:103], v[220:223], a[164:167], v[100:103]\n"
"ds_read_b64_tr_b16 v[214:215], v226 offset:26112\n"
"v_add_f32 v242, 1.0, v242\n"
"v_mfma_f32_16x16x32_bf16 v[104:107], v[220:223], a[168:171], v[104:107]\n"
"v_rcp_f32 v242, v242\n"
"v_mfma_f32_16x16x32_bf16 v[108:111], v[220:223], a[172:175], v[108:111]\n"
"v_mul_f32 v206, v190, v206\n"
"v_mul_f32 v243, s61, v191\n"
"s_waitcnt lgkmcnt(2)\n"
"v_mfma_f32_16x16x32_bf16 v[80:83], v[208:211], a[176:179], v[80:83]\n"
"ds_read_b64_tr_b16 v[216:217], v226 offset:32768\n"
"v_mul_f32 v206, v206, v242\n"
"v_mfma_f32_16x16x32_bf16 v[84:87], v[208:211], a[180:183], v[84:87]\n"
"ds_read_b64_tr_b16 v[218:219], v226 offset:33280\n"
"v_mul_f32 v207, v191, v207\n"
"v_mfma_f32_16x16x32_bf16 v[88:91], v[208:211], a[184:187], v[88:91]\n"
"v_exp_f32 v243, v243\n"
"v_mfma_f32_16x16x32_bf16 v[92:95], v[208:211], a[188:191], v[92:95]\n"
"v_add_f32 v243, 1.0, v243\n"
"v_cvt_pk_bf16_f32 v234, v204, v205\n"
"s_waitcnt lgkmcnt(2)\n"
"v_mfma_f32_16x16x32_bf16 v[96:99], v[212:215], a[176:179], v[96:99]\n"
"ds_read_b64_tr_b16 v[220:221], v226 offset:33792\n"
"v_mul_f32 v236, s61, v48\n"
"v_mfma_f32_16x16x32_bf16 v[100:103], v[212:215], a[180:183], v[100:103]\n"
"ds_read_b64_tr_b16 v[222:223], v226 offset:34304\n"
"v_mul_f32 v64, v48, v64\n"
"v_mfma_f32_16x16x32_bf16 v[104:107], v[212:215], a[184:187], v[104:107]\n"
"v_rcp_f32 v243, v243\n"
"v_mfma_f32_16x16x32_bf16 v[108:111], v[212:215], a[188:191], v[108:111]\n"
"v_exp_f32 v236, v236\n"
"s_waitcnt lgkmcnt(2)\n"
"v_mfma_f32_16x16x32_bf16 v[80:83], v[216:219], a[192:195], v[80:83]\n"
"ds_read_b64_tr_b16 v[208:209], v226 offset:40960\n"
"v_mul_f32 v207, v207, v243\n"
"v_mfma_f32_16x16x32_bf16 v[84:87], v[216:219], a[196:199], v[84:87]\n"
"ds_read_b64_tr_b16 v[210:211], v226 offset:41472\n"
"v_cvt_pk_bf16_f32 v235, v206, v207\n"
"v_mfma_f32_16x16x32_bf16 v[88:91], v[216:219], a[200:203], v[88:91]\n"
"v_add_f32 v236, 1.0, v236\n"
"v_permlane16_swap_b32 v232, v234\n"
"v_mfma_f32_16x16x32_bf16 v[92:95], v[216:219], a[204:207], v[92:95]\n"
"v_rcp_f32 v236, v236\n"
"s_waitcnt lgkmcnt(2)\n"
"v_mfma_f32_16x16x32_bf16 v[96:99], v[220:223], a[192:195], v[96:99]\n"
"ds_read_b64_tr_b16 v[212:213], v226 offset:41984\n"
"v_permlane16_swap_b32 v233, v235\n"
"v_mfma_f32_16x16x32_bf16 v[100:103], v[220:223], a[196:199], v[100:103]\n"
"ds_read_b64_tr_b16 v[214:215], v226 offset:42496\n"
"v_mul_f32 v64, v64, v236\n"
"v_mfma_f32_16x16x32_bf16 v[104:107], v[220:223], a[200:203], v[104:107]\n"
"s_add_u32 s62, %[soffA], 296960\n"
"buffer_store_dwordx4 v[232:235], %[vS], %[srdA], s62 offen sc1\n"
"v_mfma_f32_16x16x32_bf16 v[108:111], v[220:223], a[204:207], v[108:111]\n"
"v_mul_f32 v237, s61, v49\n"
"v_mul_f32 v65, v49, v65\n"
"s_waitcnt lgkmcnt(2)\n"
"v_mfma_f32_16x16x32_bf16 v[80:83], v[208:211], a[208:211], v[80:83]\n"
"ds_read_b64_tr_b16 v[216:217], v226 offset:49152\n"
"v_mul_f32 v238, s61, v50\n"
"v_mfma_f32_16x16x32_bf16 v[84:87], v[208:211], a[212:215], v[84:87]\n"
"ds_read_b64_tr_b16 v[218:219], v226 offset:49664\n"
"v_mul_f32 v66, v50, v66\n"
"v_mfma_f32_16x16x32_bf16 v[88:91], v[208:211], a[216:219], v[88:91]\n"
"v_exp_f32 v237, v237\n"
"v_mfma_f32_16x16x32_bf16 v[92:95], v[208:211], a[220:223], v[92:95]\n"
"v_exp_f32 v238, v238\n"
"s_waitcnt lgkmcnt(2)\n"
"v_mfma_f32_16x16x32_bf16 v[96:99], v[212:215], a[208:211], v[96:99]\n"
"ds_read_b64_tr_b16 v[220:221], v226 offset:50176\n"
"v_add_f32 v237, 1.0, v237\n"
"v_mfma_f32_16x16x32_bf16 v[100:103], v[212:215], a[212:215], v[100:103]\n"
"ds_read_b64_tr_b16 v[222:223], v226 offset:50688\n"
"v_add_f32 v238, 1.0, v238\n"
"v_mfma_f32_16x16x32_bf16 v[104:107], v[212:215], a[216:219], v[104:107]\n"
"v_rcp_f32 v237, v237\n"
"v_mfma_f32_16x16x32_bf16 v[108:111], v[212:215], a[220:223], v[108:111]\n"
"v_rcp_f32 v238, v238\n"
"s_waitcnt lgkmcnt(2)\n"
"v_mfma_f32_16x16x32_bf16 v[80:83], v[216:219], a[224:227], v[80:83]\n"
"ds_read_b64_tr_b16 v[208:209], v226 offset:57344\n"
"v_mul_f32 v65, v65, v237\n"
"v_mfma_f32_16x16x32_bf16 v[84:87], v[216:219], a[228:231], v[84:87]\n"
"ds_read_b64_tr_b16 v[210:211], v226 offset:57856\n"
"v_mul_f32 v66, v66, v238\n"
"v_mfma_f32_16x16x32_bf16 v[88:91], v[216:219], a[232:235], v[88:91]\n"
"v_mul_f32 v239, s61, v51\n"
"v_mul_f32 v67, v51, v67\n"
"v_mfma_f32_16x16x32_bf16 v[92:95], v[216:219], a[236:239], v[92:95]\n"
"v_exp_f32 v239, v239\n"
"s_waitcnt lgkmcnt(2)\n"
"v_mfma_f32_16x16x32_bf16 v[96:99], v[220:223], a[224:227], v[96:99]\n"
"ds_read_b64_tr_b16 v[212:213], v226 offset:58368\n"
"v_add_f32 v239, 1.0, v239\n"
"v_mfma_f32_16x16x32_bf16 v[100:103], v[220:223], a[228:231], v[100:103]\n"
"ds_read_b64_tr_b16 v[214:215], v226 offset:58880\n"
"v_cvt_pk_bf16_f32 v228, v64, v65\n"
"v_mfma_f32_16x16x32_bf16 v[104:107], v[220:223], a[232:235], v[104:107]\n"
"v_rcp_f32 v239, v239\n"
"v_mfma_f32_16x16x32_bf16 v[108:111], v[220:223], a[236:239], v[108:111]\n"
"v_mul_f32 v67, v67, v239\n"
"v_mul_f32 v240, s61, v52\n"
"s_waitcnt lgkmcnt(2)\n"
"v_mfma_f32_16x16x32_bf16 v[80:83], v[208:211], a[240:243], v[80:83]\n"
"v_add_u32 v226, 65536, v226\n"
"ds_read_b64_tr_b16 v[216:217], v226 offset:0\n"
"v_mfma_f32_16x16x32_bf16 v[84:87], v[208:211], a[244:247], v[84:87]\n"
"ds_read_b64_tr_b16 v[218:219], v226 offset:512\n"
"v_cvt_pk_bf16_f32 v229, v66, v67\n"
"v_mfma_f32_16x16x32_bf16 v[88:91], v[208:211], a[248:251], v[88:91]\n"
"v_exp_f32 v240, v240\n"
"v_mfma_f32_16x16x32_bf16 v[92:95], v[208:211], a[252:255], v[92:95]\n"
"v_add_f32 v240, 1.0, v240\n"
"v_mul_f32 v68, v52, v68\n"
"s_waitcnt lgkmcnt(2)\n"
"v_mfma_f32_16x16x32_bf16 v[96:99], v[212:215], a[240:243], v[96:99]\n"
"ds_read_b64_tr_b16 v[220:221], v226 offset:1024\n"
"v_mul_f32 v241, s61, v53\n"
"v_mfma_f32_16x16x32_bf16 v[100:103], v[212:215], a[244:247], v[100:103]\n"
"ds_read_b64_tr_b16 v[222:223], v226 offset:1536\n"
"v_mul_f32 v69, v53, v69\n"
"v_mfma_f32_16x16x32_bf16 v[104:107], v[212:215], a[248:251], v[104:107]\n"
"v_rcp_f32 v240, v240\n"
"v_mfma_f32_16x16x32_bf16 v[108:111], v[212:215], a[252:255], v[108:111]\n"
"v_exp_f32 v241, v241\n"
"s_waitcnt lgkmcnt(2)\n"
"v_mfma_f32_16x16x32_bf16 v[80:83], v[216:219], v[16:19], v[80:83]\n"
"ds_read_b64_tr_b16 v[208:209], v226 offset:8192\n"
"v_mul_f32 v68, v68, v240\n"
"v_mfma_f32_16x16x32_bf16 v[84:87], v[216:219], v[20:23], v[84:87]\n"
"ds_read_b64_tr_b16 v[210:211], v226 offset:8704\n"
"v_add_f32 v241, 1.0, v241\n"
"v_mfma_f32_16x16x32_bf16 v[88:91], v[216:219], v[24:27], v[88:91]\n"
"v_rcp_f32 v241, v241\n"
"v_mfma_f32_16x16x32_bf16 v[92:95], v[216:219], v[28:31], v[92:95]\n"
"v_mul_f32 v69, v69, v241\n"
"v_mul_f32 v242, s61, v54\n"
"s_waitcnt lgkmcnt(2)\n"
"v_mfma_f32_16x16x32_bf16 v[96:99], v[220:223], v[16:19], v[96:99]\n"
"ds_read_b64_tr_b16 v[212:213], v226 offset:9216\n"
"v_mul_f32 v70, v54, v70\n"
"v_mfma_f32_16x16x32_bf16 v[100:103], v[220:223], v[20:23], v[100:103]\n"
"ds_read_b64_tr_b16 v[214:215], v226 offset:9728\n"
"v_mul_f32 v243, s61, v55\n"
"v_mfma_f32_16x16x32_bf16 v[104:107], v[220:223], v[24:27], v[104:107]\n"
"v_exp_f32 v242, v242\n"
"v_mfma_f32_16x16x32_bf16 v[108:111], v[220:223], v[28:31], v[108:111]\n"
"v_exp_f32 v243, v243\n"
"s_waitcnt lgkmcnt(2)\n"
"v_mfma_f32_16x16x32_bf16 v[80:83], v[208:211], v[32:35], v[80:83]\n"
"ds_read_b64_tr_b16 v[216:217], v227 offset:8192\n"
"v_add_f32 v242, 1.0, v242\n"
"v_mfma_f32_16x16x32_bf16 v[84:87], v[208:211], v[36:39], v[84:87]\n"
"ds_read_b64_tr_b16 v[218:219], v227 offset:8704\n"
"v_add_f32 v243, 1.0, v243\n"
"v_mfma_f32_16x16x32_bf16 v[88:91], v[208:211], v[40:43], v[88:91]\n"
"v_rcp_f32 v242, v242\n"
"v_mfma_f32_16x16x32_bf16 v[92:95], v[208:211], v[44:47], v[92:95]\n"
"v_rcp_f32 v243, v243\n"
"s_waitcnt lgkmcnt(2)\n"
"v_mfma_f32_16x16x32_bf16 v[96:99], v[212:215], v[32:35], v[96:99]\n"
"ds_read_b64_tr_b16 v[220:221], v227 offset:9216\n"
"v_mul_f32 v70, v70, v242\n"
"v_mfma_f32_16x16x32_bf16 v[100:103], v[212:215], v[36:39], v[100:103]\n"
"ds_read_b64_tr_b16 v[222:223], v227 offset:9728\n"
"v_mul_f32 v71, v55, v71\n"
"v_mfma_f32_16x16x32_bf16 v[104:107], v[212:215], v[40:43], v[104:107]\n"
"v_mul_f32 v71, v71, v243\n"
"v_cvt_pk_bf16_f32 v230, v68, v69\n"
"v_mfma_f32_16x16x32_bf16 v[108:111], v[212:215], v[44:47], v[108:111]\n"
"v_cvt_pk_bf16_f32 v231, v70, v71\n"
"v_mul_f32 v236, s61, v56\n"
"s_waitcnt lgkmcnt(2)\n"
"v_mfma_f32_16x16x32_bf16 v[112:115], v[216:219], a[144:147], v[112:115]\n"
"ds_read_b64_tr_b16 v[208:209], v227 offset:16384\n"
"v_permlane16_swap_b32 v228, v230\n"
"v_mfma_f32_16x16x32_bf16 v[116:119], v[216:219], a[148:151], v[116:119]\n"
"ds_read_b64_tr_b16 v[210:211], v227 offset:16896\n"
"v_permlane16_swap_b32 v229, v231\n"
"v_mfma_f32_16x16x32_bf16 v[120:123], v[216:219], a[152:155], v[120:123]\n"
"s_add_u32 s62, %[soffA], 2560\n"
"buffer_store_dwordx4 v[228:231], %[vS], %[srdA], s62 offen sc1\n"
"v_mfma_f32_16x16x32_bf16 v[124:127], v[216:219], a[156:159], v[124:127]\n"
"v_exp_f32 v236, v236\n"
"s_waitcnt lgkmcnt(2)\n"
"v_mfma_f32_16x16x32_bf16 v[128:131], v[220:223], a[144:147], v[128:131]\n"
"ds_read_b64_tr_b16 v[212:213], v227 offset:17408\n"
"v_add_f32 v236, 1.0, v236\n"
"v_mfma_f32_16x16x32_bf16 v[132:135], v[220:223], a[148:151], v[132:135]\n"
"ds_read_b64_tr_b16 v[214:215], v227 offset:17920\n"
"v_mul_f32 v72, v56, v72\n"
"v_mfma_f32_16x16x32_bf16 v[136:139], v[220:223], a[152:155], v[136:139]\n"
"v_rcp_f32 v236, v236\n"
"v_mfma_f32_16x16x32_bf16 v[140:143], v[220:223], a[156:159], v[140:143]\n"
"v_mul_f32 v72, v72, v236\n"
"v_mul_f32 v237, s61, v57\n"
"s_waitcnt lgkmcnt(2)\n"
"v_mfma_f32_16x16x32_bf16 v[112:115], v[208:211], a[160:163], v[112:115]\n"
"ds_read_b64_tr_b16 v[216:217], v227 offset:24576\n"
"v_mul_f32 v73, v57, v73\n"
"v_mfma_f32_16x16x32_bf16 v[116:119], v[208:211], a[164:167], v[116:119]\n"
"ds_read_b64_tr_b16 v[218:219], v227 offset:25088\n"
"v_mul_f32 v238, s61, v58\n"
"v_mfma_f32_16x16x32_bf16 v[120:123], v[208:211], a[168:171], v[120:123]\n"
"v_exp_f32 v237, v237\n"
"v_mfma_f32_16x16x32_bf16 v[124:127], v[208:211], a[172:175], v[124:127]\n"
"v_exp_f32 v238, v238\n"
"s_waitcnt lgkmcnt(2)\n"
"v_mfma_f32_16x16x32_bf16 v[128:131], v[212:215], a[160:163], v[128:131]\n"
"ds_read_b64_tr_b16 v[220:221], v227 offset:25600\n"
"v_add_f32 v237, 1.0, v237\n"
"v_mfma_f32_16x16x32_bf16 v[132:135], v[212:215], a[164:167], v[132:135]\n"
"ds_read_b64_tr_b16 v[222:223], v227 offset:26112\n"
"v_add_f32 v238, 1.0, v238\n"
"v_mfma_f32_16x16x32_bf16 v[136:139], v[212:215], a[168:171], v[136:139]\n"
"v_rcp_f32 v237, v237\n"
"v_mfma_f32_16x16x32_bf16 v[140:143], v[212:215], a[172:175], v[140:143]\n"
"v_rcp_f32 v238, v238\n"
"s_waitcnt lgkmcnt(2)\n"
"v_mfma_f32_16x16x32_bf16 v[112:115], v[216:219], a[176:179], v[112:115]\n"
"ds_read_b64_tr_b16 v[208:209], v227 offset:32768\n"
"v_mul_f32 v73, v73, v237\n"
"v_mfma_f32_16x16x32_bf16 v[116:119], v[216:219], a[180:183], v[116:119]\n"
"ds_read_b64_tr_b16 v[210:211], v227 offset:33280\n"
"v_mul_f32 v74, v58, v74\n"
"v_mfma_f32_16x16x32_bf16 v[120:123], v[216:219], a[184:187], v[120:123]\n"
"v_mul_f32 v74, v74, v238\n"
"v_mul_f32 v239, s61, v59\n"
"v_mfma_f32_16x16x32_bf16 v[124:127], v[216:219], a[188:191], v[124:127]\n"
"v_exp_f32 v239, v239\n"
"s_waitcnt lgkmcnt(2)\n"
"v_mfma_f32_16x16x32_bf16 v[128:131], v[220:223], a[176:179], v[128:131]\n"
"ds_read_b64_tr_b16 v[212:213], v227 offset:33792\n"
"v_add_f32 v239, 1.0, v239\n"
"v_mfma_f32_16x16x32_bf16 v[132:135], v[220:223], a[180:183], v[132:135]\n"
"ds_read_b64_tr_b16 v[214:215], v227 offset:34304\n"
"v_mul_f32 v75, v59, v75\n"
"v_mfma_f32_16x16x32_bf16 v[136:139], v[220:223], a[184:187], v[136:139]\n"
"v_rcp_f32 v239, v239\n"
"v_mfma_f32_16x16x32_bf16 v[140:143], v[220:223], a[188:191], v[140:143]\n"
"v_mul_f32 v75, v75, v239\n"
"v_cvt_pk_bf16_f32 v232, v72, v73\n"
"s_waitcnt lgkmcnt(2)\n"
"v_mfma_f32_16x16x32_bf16 v[112:115], v[208:211], a[192:195], v[112:115]\n"
"ds_read_b64_tr_b16 v[216:217], v227 offset:40960\n"
"v_cvt_pk_bf16_f32 v233, v74, v75\n"
"v_mfma_f32_16x16x32_bf16 v[116:119], v[208:211], a[196:199], v[116:119]\n"
"ds_read_b64_tr_b16 v[218:219], v227 offset:41472\n"
"v_mul_f32 v240, s61, v60\n"
"v_mfma_f32_16x16x32_bf16 v[120:123], v[208:211], a[200:203], v[120:123]\n"
"v_exp_f32 v240, v240\n"
"v_mfma_f32_16x16x32_bf16 v[124:127], v[208:211], a[204:207], v[124:127]\n"
"v_add_f32 v240, 1.0, v240\n"
"v_mul_f32 v76, v60, v76\n"
"s_waitcnt lgkmcnt(2)\n"
"v_mfma_f32_16x16x32_bf16 v[128:131], v[212:215], a[192:195], v[128:131]\n"
"ds_read_b64_tr_b16 v[220:221], v227 offset:41984\n"
"v_mul_f32 v241, s61, v61\n"
"v_mfma_f32_16x16x32_bf16 v[132:135], v[212:215], a[196:199], v[132:135]\n"
"ds_read_b64_tr_b16 v[222:223], v227 offset:42496\n"
"v_mul_f32 v77, v61, v77\n"
"v_mfma_f32_16x16x32_bf16 v[136:139], v[212:215], a[200:203], v[136:139]\n"
"v_rcp_f32 v240, v240\n"
"v_mfma_f32_16x16x32_bf16 v[140:143], v[212:215], a[204:207], v[140:143]\n"
"v_exp_f32 v241, v241\n"
"s_waitcnt lgkmcnt(2)\n"
"v_mfma_f32_16x16x32_bf16 v[112:115], v[216:219], a[208:211], v[112:115]\n"
"ds_read_b64_tr_b16 v[208:209], v227 offset:49152\n"
"v_mul_f32 v76, v76, v240\n"
"v_mfma_f32_16x16x32_bf16 v[116:119], v[216:219], a[212:215], v[116:119]\n"
"ds_read_b64_tr_b16 v[210:211], v227 offset:49664\n"
"v_add_f32 v241, 1.0, v241\n"
"v_mfma_f32_16x16x32_bf16 v[120:123], v[216:219], a[216:219], v[120:123]\n"
"v_rcp_f32 v241, v241\n"
"v_mfma_f32_16x16x32_bf16 v[124:127], v[216:219], a[220:223], v[124:127]\n"
"v_mul_f32 v77, v77, v241\n"
"v_mul_f32 v242, s61, v62\n"
"s_waitcnt lgkmcnt(2)\n"
"v_mfma_f32_16x16x32_bf16 v[128:131], v[220:223], a[208:211], v[128:131]\n"
"ds_read_b64_tr_b16 v[212:213], v227 offset:50176\n"
"v_mul_f32 v78, v62, v78\n"
"v_mfma_f32_16x16x32_bf16 v[132:135], v[220:223], a[212:215], v[132:135]\n"
"ds_read_b64_tr_b16 v[214:215], v227 offset:50688\n"
"v_mul_f32 v243, s61, v63\n"
"v_mfma_f32_16x16x32_bf16 v[136:139], v[220:223], a[216:219], v[136:139]\n"
"v_exp_f32 v242, v242\n"
"v_mfma_f32_16x16x32_bf16 v[140:143], v[220:223], a[220:223], v[140:143]\n"
"v_exp_f32 v243, v243\n"
"s_waitcnt lgkmcnt(2)\n"
"v_mfma_f32_16x16x32_bf16 v[112:115], v[208:211], a[224:227], v[112:115]\n"
"ds_read_b64_tr_b16 v[216:217], v227 offset:57344\n"
"v_add_f32 v242, 1.0, v242\n"
"v_mfma_f32_16x16x32_bf16 v[116:119], v[208:211], a[228:231], v[116:119]\n"
"ds_read_b64_tr_b16 v[218:219], v227 offset:57856\n"
"v_add_f32 v243, 1.0, v243\n"
"v_mfma_f32_16x16x32_bf16 v[120:123], v[208:211], a[232:235], v[120:123]\n"
"v_rcp_f32 v242, v242\n"
"v_mfma_f32_16x16x32_bf16 v[124:127], v[208:211], a[236:239], v[124:127]\n"
"v_rcp_f32 v243, v243\n"
"s_waitcnt lgkmcnt(2)\n"
"v_mfma_f32_16x16x32_bf16 v[128:131], v[212:215], a[224:227], v[128:131]\n"
"ds_read_b64_tr_b16 v[220:221], v227 offset:58368\n"
"v_mul_f32 v78, v78, v242\n"
"v_mfma_f32_16x16x32_bf16 v[132:135], v[212:215], a[228:231], v[132:135]\n"
"ds_read_b64_tr_b16 v[222:223], v227 offset:58880\n"
"v_mul_f32 v79, v63, v79\n"
"v_mfma_f32_16x16x32_bf16 v[136:139], v[212:215], a[232:235], v[136:139]\n"
"v_mul_f32 v79, v79, v243\n"
"v_cvt_pk_bf16_f32 v234, v76, v77\n"
"v_mfma_f32_16x16x32_bf16 v[140:143], v[212:215], a[236:239], v[140:143]\n"
"v_cvt_pk_bf16_f32 v235, v78, v79\n"
"v_mul_f32 v236, s61, v80\n"
"s_waitcnt lgkmcnt(2)\n"
"v_mfma_f32_16x16x32_bf16 v[112:115], v[216:219], a[240:243], v[112:115]\n"
"v_add_u32 v227, 65536, v227\n"
"ds_read_b64_tr_b16 v[208:209], v227 offset:0\n"
"v_mfma_f32_16x16x32_bf16 v[116:119], v[216:219], a[244:247], v[116:119]\n"
"ds_read_b64_tr_b16 v[210:211], v227 offset:512\n"
"v_permlane16_swap_b32 v232, v234\n"
"v_mfma_f32_16x16x32_bf16 v[120:123], v[216:219], a[248:251], v[120:123]\n"
"v_exp_f32 v236, v236\n"
"v_mfma_f32_16x16x32_bf16 v[124:127], v[216:219], a[252:255], v[124:127]\n"
"v_permlane16_swap_b32 v233, v235\n"
"v_add_f32 v236, 1.0, v236\n"
"s_waitcnt lgkmcnt(2)\n"
"v_mfma_f32_16x16x32_bf16 v[128:131], v[220:223], a[240:243], v[128:131]\n"
"ds_read_b64_tr_b16 v[212:213], v227 offset:1024\n"
"v_mul_f32 v96, v80, v96\n"
"v_mfma_f32_16x16x32_bf16 v[132:135], v[220:223], a[244:247], v[132:135]\n"
"ds_read_b64_tr_b16 v[214:215], v227 offset:1536\n"
"v_mul_f32 v237, s61, v81\n"
"v_mfma_f32_16x16x32_bf16 v[136:139], v[220:223], a[248:251], v[136:139]\n"
"s_add_u32 s62, %[soffA], 297472\n"
"buffer_store_dwordx4 v[232:235], %[vS], %[srdA], s62 offen sc1\n"
"v_mfma_f32_16x16x32_bf16 v[140:143], v[220:223], a[252:255], v[140:143]\n"
"v_rcp_f32 v236, v236\n"
"s_waitcnt lgkmcnt(2)\n"
"v_mfma_f32_16x16x32_bf16 v[112:115], v[208:211], v[16:19], v[112:115]\n"
"ds_read_b64_tr_b16 v[216:217], v227 offset:8192\n"
"v_mul_f32 v96, v96, v236\n"
"v_mfma_f32_16x16x32_bf16 v[116:119], v[208:211], v[20:23], v[116:119]\n"
"ds_read_b64_tr_b16 v[218:219], v227 offset:8704\n"
"v_mul_f32 v97, v81, v97\n"
"v_mfma_f32_16x16x32_bf16 v[120:123], v[208:211], v[24:27], v[120:123]\n"
"v_exp_f32 v237, v237\n"
"v_mfma_f32_16x16x32_bf16 v[124:127], v[208:211], v[28:31], v[124:127]\n"
"v_add_f32 v237, 1.0, v237\n"
"v_mul_f32 v238, s61, v82\n"
"s_waitcnt lgkmcnt(2)\n"
"v_mfma_f32_16x16x32_bf16 v[128:131], v[212:215], v[16:19], v[128:131]\n"
"ds_read_b64_tr_b16 v[220:221], v227 offset:9216\n"
"v_mul_f32 v98, v82, v98\n"
"v_mfma_f32_16x16x32_bf16 v[132:135], v[212:215], v[20:23], v[132:135]\n"
"ds_read_b64_tr_b16 v[222:223], v227 offset:9728\n"
"v_mul_f32 v239, s61, v83\n"
"v_mfma_f32_16x16x32_bf16 v[136:139], v[212:215], v[24:27], v[136:139]\n"
"v_rcp_f32 v237, v237\n"
"v_mfma_f32_16x16x32_bf16 v[140:143], v[212:215], v[28:31], v[140:143]\n"
"v_exp_f32 v238, v238\n"
"s_waitcnt vmcnt(4)\n"
"s_barrier\n"
"buffer_store_dword %[vFA], %[vFA], %[srdF4], 0 offen sc1\n"
"s_waitcnt lgkmcnt(2)\n"
"v_mfma_f32_16x16x32_bf16 v[112:115], v[216:219], v[32:35], v[112:115]\n"
"ds_read_b64_tr_b16 v[208:209], %[vS0] offset:0\n"
"v_mul_f32 v97, v97, v237\n"
"v_mfma_f32_16x16x32_bf16 v[116:119], v[216:219], v[36:39], v[116:119]\n"
"ds_read_b64_tr_b16 v[210:211], %[vS1] offset:0\n"
"v_add_f32 v238, 1.0, v238\n"
"v_mfma_f32_16x16x32_bf16 v[120:123], v[216:219], v[40:43], v[120:123]\n"
"v_rcp_f32 v238, v238\n"
"v_mfma_f32_16x16x32_bf16 v[124:127], v[216:219], v[44:47], v[124:127]\n"
"v_exp_f32 v239, v239\n"
"s_waitcnt lgkmcnt(2)\n"
"v_mfma_f32_16x16x32_bf16 v[128:131], v[220:223], v[32:35], v[128:131]\n"
"ds_read_b64_tr_b16 v[212:213], %[vS0] offset:1024\n"
"v_mul_f32 v98, v98, v238\n"
"v_mfma_f32_16x16x32_bf16 v[132:135], v[220:223], v[36:39], v[132:135]\n"
"ds_read_b64_tr_b16 v[214:215], %[vS1] offset:1024\n"
"v_add_f32 v239, 1.0, v239\n"
"v_mfma_f32_16x16x32_bf16 v[136:139], v[220:223], v[40:43], v[136:139]\n"
"v_rcp_f32 v239, v239\n"
"v_mfma_f32_16x16x32_bf16 v[140:143], v[220:223], v[44:47], v[140:143]\n"
"v_mul_f32 v99, v83, v99\n"
"v_cvt_pk_bf16_f32 v228, v96, v97\n"
"s_waitcnt lgkmcnt(2)\n"
"v_mfma_f32_16x16x32_bf16 v[144:147], v[208:211], a[0:3], 0\n"
"ds_read_b64_tr_b16 v[216:217], %[vS0] offset:2048\n"
"v_mul_f32 v99, v99, v239\n"
"v_mfma_f32_16x16x32_bf16 v[148:151], v[208:211], a[4:7], 0\n"
"ds_read_b64_tr_b16 v[218:219], %[vS1] offset:2048\n"
"v_cvt_pk_bf16_f32 v229, v98, v99\n"
"v_mfma_f32_16x16x32_bf16 v[152:155], v[208:211], a[8:11], 0\n"
"v_mul_f32 v240, s61, v84\n"
"v_mul_f32 v100, v84, v100\n"
"v_mfma_f32_16x16x32_bf16 v[156:159], v[208:211], a[12:15], 0\n"
"v_exp_f32 v240, v240\n"
"s_waitcnt lgkmcnt(2)\n"
"v_mfma_f32_16x16x32_bf16 v[160:163], v[212:215], a[0:3], 0\n"
"ds_read_b64_tr_b16 v[220:221], %[vS0] offset:3072\n"
"v_add_f32 v240, 1.0, v240\n"
"v_mfma_f32_16x16x32_bf16 v[164:167], v[212:215], a[4:7], 0\n"
"ds_read_b64_tr_b16 v[222:223], %[vS1] offset:3072\n"
"v_mul_f32 v241, s61, v85\n"
"v_mfma_f32_16x16x32_bf16 v[168:171], v[212:215], a[8:11], 0\n"
"v_rcp_f32 v240, v240\n"
"v_mfma_f32_16x16x32_bf16 v[172:175], v[212:215], a[12:15], 0\n"
"v_exp_f32 v241, v241\n"
"s_waitcnt lgkmcnt(2)\n"
"v_mfma_f32_16x16x32_bf16 v[144:147], v[216:219], a[16:19], v[144:147]\n"
"ds_read_b64_tr_b16 v[208:209], %[vS0] offset:4096\n"
"v_mul_f32 v100, v100, v240\n"
"v_mfma_f32_16x16x32_bf16 v[148:151], v[216:219], a[20:23], v[148:151]\n"
"ds_read_b64_tr_b16 v[210:211], %[vS1] offset:4096\n"
"v_add_f32 v241, 1.0, v241\n"
"v_mfma_f32_16x16x32_bf16 v[152:155], v[216:219], a[24:27], v[152:155]\n"
"v_rcp_f32 v241, v241\n"
"v_mfma_f32_16x16x32_bf16 v[156:159], v[216:219], a[28:31], v[156:159]\n"
"v_mul_f32 v101, v85, v101\n"
"v_mul_f32 v242, s61, v86\n"
"s_waitcnt lgkmcnt(2)\n"
"v_mfma_f32_16x16x32_bf16 v[160:163], v[220:223], a[16:19], v[160:163]\n"
"ds_read_b64_tr_b16 v[212:213], %[vS0] offset:5120\n"
"v_mul_f32 v101, v101, v241\n"
"v_mfma_f32_16x16x32_bf16 v[164:167], v[220:223], a[20:23], v[164:167]\n"
"ds_read_b64_tr_b16 v[214:215], %[vS1] offset:5120\n"
"v_mul_f32 v102, v86, v102\n"
"v_mfma_f32_16x16x32_bf16 v[168:171], v[220:223], a[24:27], v[168:171]\n"
"v_exp_f32 v242, v242\n"
"v_mfma_f32_16x16x32_bf16 v[172:175], v[220:223], a[28:31], v[172:175]\n"
"v_add_f32 v242, 1.0, v242\n"
"v_mul_f32 v243, s61, v87\n"
"s_waitcnt lgkmcnt(2)\n"
"v_mfma_f32_16x16x32_bf16 v[144:147], v[208:211], a[32:35], v[144:147]\n"
"ds_read_b64_tr_b16 v[216:217], %[vS0] offset:6144\n"
"v_mul_f32 v103, v87, v103\n"
"v_mfma_f32_16x16x32_bf16 v[148:151], v[208:211], a[36:39], v[148:151]\n"
"ds_read_b64_tr_b16 v[218:219], %[vS1] offset:6144\n"
"v_cvt_pk_bf16_f32 v230, v100, v101\n"
"v_mfma_f32_16x16x32_bf16 v[152:155], v[208:211], a[40:43], v[152:155]\n"
"v_rcp_f32 v242, v242\n"
"v_mfma_f32_16x16x32_bf16 v[156:159], v[208:211], a[44:47], v[156:159]\n"
"v_exp_f32 v243, v243\n"
"s_waitcnt lgkmcnt(2)\n"
"v_mfma_f32_16x16x32_bf16 v[160:163], v[212:215], a[32:35], v[160:163]\n"
"ds_read_b64_tr_b16 v[220:221], %[vS0] offset:7168\n"
"v_mul_f32 v102, v102, v242\n"
"v_mfma_f32_16x16x32_bf16 v[164:167], v[212:215], a[36:39], v[164:167]\n"
"ds_read_b64_tr_b16 v[222:223], %[vS1] offset:7168\n"
"v_add_f32 v243, 1.0, v243\n"
"v_mfma_f32_16x16x32_bf16 v[168:171], v[212:215], a[40:43], v[168:171]\n"
"v_rcp_f32 v243, v243\n"
"v_mfma_f32_16x16x32_bf16 v[172:175], v[212:215], a[44:47], v[172:175]\n"
"v_mul_f32 v103, v103, v243\n"
"v_mul_f32 v236, s61, v88\n"
"s_waitcnt lgkmcnt(2)\n"
"v_mfma_f32_16x16x32_bf16 v[144:147], v[216:219], a[48:51], v[144:147]\n"
"ds_read_b64_tr_b16 v[208:209], %[vS0] offset:8192\n"
"v_cvt_pk_bf16_f32 v231, v102, v103\n"
"v_mfma_f32_16x16x32_bf16 v[148:151], v[216:219], a[52:55], v[148:151]\n"
"ds_read_b64_tr_b16 v[210:211], %[vS1] offset:8192\n"
"v_permlane16_swap_b32 v228, v230\n"
"v_mfma_f32_16x16x32_bf16 v[152:155], v[216:219], a[56:59], v[152:155]\n"
"v_exp_f32 v236, v236\n"
"v_mfma_f32_16x16x32_bf16 v[156:159], v[216:219], a[60:63], v[156:159]\n"
"v_permlane16_swap_b32 v229, v231\n"
"v_add_f32 v236, 1.0, v236\n"
"s_waitcnt lgkmcnt(2)\n"
"v_mfma_f32_16x16x32_bf16 v[160:163], v[220:223], a[48:51], v[160:163]\n"
"ds_read_b64_tr_b16 v[212:213], %[vS0] offset:9216\n"
"v_mul_f32 v104, v88, v104\n"
"v_mfma_f32_16x16x32_bf16 v[164:167], v[220:223], a[52:55], v[164:167]\n"
"ds_read_b64_tr_b16 v[214:215], %[vS1] offset:9216\n"
"v_mul_f32 v237, s61, v89\n"
"v_mfma_f32_16x16x32_bf16 v[168:171], v[220:223], a[56:59], v[168:171]\n"
"s_add_u32 s62, %[soffA], 3072\n"
"buffer_store_dwordx4 v[228:231], %[vS], %[srdA], s62 offen sc1\n"
"v_mfma_f32_16x16x32_bf16 v[172:175], v[220:223], a[60:63], v[172:175]\n"
"v_rcp_f32 v236, v236\n"
"s_waitcnt lgkmcnt(2)\n"
"v_mfma_f32_16x16x32_bf16 v[144:147], v[208:211], a[64:67], v[144:147]\n"
"ds_read_b64_tr_b16 v[216:217], %[vS0] offset:10240\n"
"v_mul_f32 v104, v104, v236\n"
"v_mfma_f32_16x16x32_bf16 v[148:151], v[208:211], a[68:71], v[148:151]\n"
"ds_read_b64_tr_b16 v[218:219], %[vS1] offset:10240\n"
"v_mul_f32 v105, v89, v105\n"
"v_mfma_f32_16x16x32_bf16 v[152:155], v[208:211], a[72:75], v[152:155]\n"
"v_exp_f32 v237, v237\n"
"v_mfma_f32_16x16x32_bf16 v[156:159], v[208:211], a[76:79], v[156:159]\n"
"v_add_f32 v237, 1.0, v237\n"
"v_mul_f32 v238, s61, v90\n"
"s_waitcnt lgkmcnt(2)\n"
"v_mfma_f32_16x16x32_bf16 v[160:163], v[212:215], a[64:67], v[160:163]\n"
"ds_read_b64_tr_b16 v[220:221], %[vS0] offset:11264\n"
"v_mul_f32 v106, v90, v106\n"
"v_mfma_f32_16x16x32_bf16 v[164:167], v[212:215], a[68:71], v[164:167]\n"
"ds_read_b64_tr_b16 v[222:223], %[vS1] offset:11264\n"
"v_mul_f32 v239, s61, v91\n"
"v_mfma_f32_16x16x32_bf16 v[168:171], v[212:215], a[72:75], v[168:171]\n"
"v_rcp_f32 v237, v237\n"
"v_mfma_f32_16x16x32_bf16 v[172:175], v[212:215], a[76:79], v[172:175]\n"
"v_exp_f32 v238, v238\n"
"s_waitcnt lgkmcnt(2)\n"
"v_mfma_f32_16x16x32_bf16 v[144:147], v[216:219], a[80:83], v[144:147]\n"
"ds_read_b64_tr_b16 v[208:209], %[vS0] offset:12288\n"
"v_mul_f32 v105, v105, v237\n"
"v_mfma_f32_16x16x32_bf16 v[148:151], v[216:219], a[84:87], v[148:151]\n"
"ds_read_b64_tr_b16 v[210:211], %[vS1] offset:12288\n"
"v_add_f32 v238, 1.0, v238\n"
"v_mfma_f32_16x16x32_bf16 v[152:155], v[216:219], a[88:91], v[152:155]\n"
"v_rcp_f32 v238, v238\n"
"v_mfma_f32_16x16x32_bf16 v[156:159], v[216:219], a[92:95], v[156:159]\n"
"v_exp_f32 v239, v239\n"
"s_waitcnt lgkmcnt(2)\n"
"v_mfma_f32_16x16x32_bf16 v[160:163], v[220:223], a[80:83], v[160:163]\n"
"ds_read_b64_tr_b16 v[212:213], %[vS0] offset:13312\n"
"v_mul_f32 v106, v106, v238\n"
"v_mfma_f32_16x16x32_bf16 v[164:167], v[220:223], a[84:87], v[164:167]\n"
"ds_read_b64_tr_b16 v[214:215], %[vS1] offset:13312\n"
"v_add_f32 v239, 1.0, v239\n"
"v_mfma_f32_16x16x32_bf16 v[168:171], v[220:223], a[88:91], v[168:171]\n"
"v_rcp_f32 v239, v239\n"
"v_mfma_f32_16x16x32_bf16 v[172:175], v[220:223], a[92:95], v[172:175]\n"
"v_mul_f32 v107, v91, v107\n"
"v_cvt_pk_bf16_f32 v232, v104, v105\n"
"s_waitcnt lgkmcnt(2)\n"
"v_mfma_f32_16x16x32_bf16 v[144:147], v[208:211], a[96:99], v[144:147]\n"
"ds_read_b64_tr_b16 v[216:217], %[vS0] offset:14336\n"
"v_mul_f32 v107, v107, v239\n"
"v_mfma_f32_16x16x32_bf16 v[148:151], v[208:211], a[100:103], v[148:151]\n"
"ds_read_b64_tr_b16 v[218:219], %[vS1] offset:14336\n"
"v_cvt_pk_bf16_f32 v233, v106, v107\n"
"v_mfma_f32_16x16x32_bf16 v[152:155], v[208:211], a[104:107], v[152:155]\n"
"v_mul_f32 v240, s61, v92\n"
"v_mul_f32 v108, v92, v108\n"
"v_mfma_f32_16x16x32_bf16 v[156:159], v[208:211], a[108:111], v[156:159]\n"
"v_exp_f32 v240, v240\n"
"s_waitcnt lgkmcnt(2)\n"
"v_mfma_f32_16x16x32_bf16 v[160:163], v[212:215], a[96:99], v[160:163]\n"
"ds_read_b64_tr_b16 v[220:221], %[vS0] offset:15360\n"
"v_add_f32 v240, 1.0, v240\n"
"v_mfma_f32_16x16x32_bf16 v[164:167], v[212:215], a[100:103], v[164:167]\n"
"ds_read_b64_tr_b16 v[222:223], %[vS1] offset:15360\n"
"v_mul_f32 v241, s61, v93\n"
"v_mfma_f32_16x16x32_bf16 v[168:171], v[212:215], a[104:107], v[168:171]\n"
"v_rcp_f32 v240, v240\n"
"v_mfma_f32_16x16x32_bf16 v[172:175], v[212:215], a[108:111], v[172:175]\n"
"v_exp_f32 v241, v241\n"
"s_waitcnt lgkmcnt(2)\n"
"v_mfma_f32_16x16x32_bf16 v[144:147], v[216:219], a[112:115], v[144:147]\n"
"ds_read_b64_tr_b16 v[208:209], %[vS0] offset:16384\n"
"v_mul_f32 v108, v108, v240\n"
"v_mfma_f32_16x16x32_bf16 v[148:151], v[216:219], a[116:119], v[148:151]\n"
"ds_read_b64_tr_b16 v[210:211], %[vS1] offset:16384\n"
"v_add_f32 v241, 1.0, v241\n"
"v_mfma_f32_16x16x32_bf16 v[152:155], v[216:219], a[120:123], v[152:155]\n"
"v_rcp_f32 v241, v241\n"
"v_mfma_f32_16x16x32_bf16 v[156:159], v[216:219], a[124:127], v[156:159]\n"
"v_mul_f32 v109, v93, v109\n"
"v_mul_f32 v242, s61, v94\n"
"s_waitcnt lgkmcnt(2)\n"
"v_mfma_f32_16x16x32_bf16 v[160:163], v[220:223], a[112:115], v[160:163]\n"
"ds_read_b64_tr_b16 v[212:213], %[vS0] offset:17408\n"
"v_mul_f32 v109, v109, v241\n"
"v_mfma_f32_16x16x32_bf16 v[164:167], v[220:223], a[116:119], v[164:167]\n"
"ds_read_b64_tr_b16 v[214:215], %[vS1] offset:17408\n"
"v_mul_f32 v110, v94, v110\n"
"v_mfma_f32_16x16x32_bf16 v[168:171], v[220:223], a[120:123], v[168:171]\n"
"v_exp_f32 v242, v242\n"
"v_mfma_f32_16x16x32_bf16 v[172:175], v[220:223], a[124:127], v[172:175]\n"
"v_add_f32 v242, 1.0, v242\n"
"v_mul_f32 v243, s61, v95\n"
"s_waitcnt lgkmcnt(2)\n"
"v_mfma_f32_16x16x32_bf16 v[144:147], v[208:211], a[128:131], v[144:147]\n"
"ds_read_b64_tr_b16 v[216:217], %[vS0] offset:18432\n"
"v_mul_f32 v111, v95, v111\n"
"v_mfma_f32_16x16x32_bf16 v[148:151], v[208:211], a[132:135], v[148:151]\n"
"ds_read_b64_tr_b16 v[218:219], %[vS1] offset:18432\n"
"v_cvt_pk_bf16_f32 v234, v108, v109\n"
"v_mfma_f32_16x16x32_bf16 v[152:155], v[208:211], a[136:139], v[152:155]\n"
"v_rcp_f32 v242, v242\n"
"v_mfma_f32_16x16x32_bf16 v[156:159], v[208:211], a[140:143], v[156:159]\n"
"v_exp_f32 v243, v243\n"
"s_waitcnt lgkmcnt(2)\n"
"v_mfma_f32_16x16x32_bf16 v[160:163], v[212:215], a[128:131], v[160:163]\n"
"ds_read_b64_tr_b16 v[220:221], %[vS0] offset:19456\n"
"v_mul_f32 v110, v110, v242\n"
"v_mfma_f32_16x16x32_bf16 v[164:167], v[212:215], a[132:135], v[164:167]\n"
"ds_read_b64_tr_b16 v[222:223], %[vS1] offset:19456\n"
"v_add_f32 v243, 1.0, v243\n"
"v_mfma_f32_16x16x32_bf16 v[168:171], v[212:215], a[136:139], v[168:171]\n"
"v_rcp_f32 v243, v243\n"
"v_mfma_f32_16x16x32_bf16 v[172:175], v[212:215], a[140:143], v[172:175]\n"
"v_mul_f32 v111, v111, v243\n"
"v_mul_f32 v236, s61, v112\n"
"s_waitcnt lgkmcnt(2)\n"
"v_mfma_f32_16x16x32_bf16 v[144:147], v[216:219], a[144:147], v[144:147]\n"
"ds_read_b64_tr_b16 v[208:209], %[vS0] offset:20480\n"
"v_cvt_pk_bf16_f32 v235, v110, v111\n"
"v_mfma_f32_16x16x32_bf16 v[148:151], v[216:219], a[148:151], v[148:151]\n"
"ds_read_b64_tr_b16 v[210:211], %[vS1] offset:20480\n"
"v_permlane16_swap_b32 v232, v234\n"
"v_mfma_f32_16x16x32_bf16 v[152:155], v[216:219], a[152:155], v[152:155]\n"
"v_exp_f32 v236, v236\n"
"v_mfma_f32_16x16x32_bf16 v[156:159], v[216:219], a[156:159], v[156:159]\n"
"v_permlane16_swap_b32 v233, v235\n"
"v_add_f32 v236, 1.0, v236\n"
"s_waitcnt lgkmcnt(2)\n"
"v_mfma_f32_16x16x32_bf16 v[160:163], v[220:223], a[144:147], v[160:163]\n"
"ds_read_b64_tr_b16 v[212:213], %[vS0] offset:21504\n"
"v_mul_f32 v128, v112, v128\n"
"v_mfma_f32_16x16x32_bf16 v[164:167], v[220:223], a[148:151], v[164:167]\n"
"ds_read_b64_tr_b16 v[214:215], %[vS1] offset:21504\n"
"v_mul_f32 v237, s61, v113\n"
"v_mfma_f32_16x16x32_bf16 v[168:171], v[220:223], a[152:155], v[168:171]\n"
"s_add_u32 s62, %[soffA], 297984\n"
"buffer_store_dwordx4 v[232:235], %[vS], %[srdA], s62 offen sc1\n"
"v_mfma_f32_16x16x32_bf16 v[172:175], v[220:223], a[156:159], v[172:175]\n"
"v_rcp_f32 v236, v236\n"
"s_waitcnt lgkmcnt(2)\n"
"v_mfma_f32_16x16x32_bf16 v[144:147], v[208:211], a[160:163], v[144:147]\n"
"ds_read_b64_tr_b16 v[216:217], %[vS0] offset:22528\n"
"v_mul_f32 v128, v128, v236\n"
"v_mfma_f32_16x16x32_bf16 v[148:151], v[208:211], a[164:167], v[148:151]\n"
"ds_read_b64_tr_b16 v[218:219], %[vS1] offset:22528\n"
"v_mul_f32 v129, v113, v129\n"
"v_mfma_f32_16x16x32_bf16 v[152:155], v[208:211], a[168:171], v[152:155]\n"
"v_exp_f32 v237, v237\n"
"v_mfma_f32_16x16x32_bf16 v[156:159], v[208:211], a[172:175], v[156:159]\n"
"v_add_f32 v237, 1.0, v237\n"
"v_mul_f32 v238, s61, v114\n"
"s_waitcnt lgkmcnt(2)\n"
"v_mfma_f32_16x16x32_bf16 v[160:163], v[212:215], a[160:163], v[160:163]\n"
"ds_read_b64_tr_b16 v[220:221], %[vS0] offset:23552\n"
"v_mul_f32 v130, v114, v130\n"
"v_mfma_f32_16x16x32_bf16 v[164:167], v[212:215], a[164:167], v[164:167]\n"
"ds_read_b64_tr_b16 v[222:223], %[vS1] offset:23552\n"
"v_mul_f32 v239, s61, v115\n"
"v_mfma_f32_16x16x32_bf16 v[168:171], v[212:215], a[168:171], v[168:171]\n"
"v_rcp_f32 v237, v237\n"
"v_mfma_f32_16x16x32_bf16 v[172:175], v[212:215], a[172:175], v[172:175]\n"
"v_exp_f32 v238, v238\n"
"s_waitcnt lgkmcnt(2)\n"
"v_mfma_f32_16x16x32_bf16 v[144:147], v[216:219], a[176:179], v[144:147]\n"
"ds_read_b64_tr_b16 v[208:209], %[vS0] offset:24576\n"
"v_mul_f32 v129, v129, v237\n"
"v_mfma_f32_16x16x32_bf16 v[148:151], v[216:219], a[180:183], v[148:151]\n"
"ds_read_b64_tr_b16 v[210:211], %[vS1] offset:24576\n"
"v_add_f32 v238, 1.0, v238\n"
"v_mfma_f32_16x16x32_bf16 v[152:155], v[216:219], a[184:187], v[152:155]\n"
"v_rcp_f32 v238, v238\n"
"v_mfma_f32_16x16x32_bf16 v[156:159], v[216:219], a[188:191], v[156:159]\n"
"v_exp_f32 v239, v239\n"
"s_waitcnt lgkmcnt(2)\n"
"v_mfma_f32_16x16x32_bf16 v[160:163], v[220:223], a[176:179], v[160:163]\n"
"ds_read_b64_tr_b16 v[212:213], %[vS0] offset:25600\n"
"v_mul_f32 v130, v130, v238\n"
"v_mfma_f32_16x16x32_bf16 v[164:167], v[220:223], a[180:183], v[164:167]\n"
"ds_read_b64_tr_b16 v[214:215], %[vS1] offset:25600\n"
"v_add_f32 v239, 1.0, v239\n"
"v_mfma_f32_16x16x32_bf16 v[168:171], v[220:223], a[184:187], v[168:171]\n"
"v_rcp_f32 v239, v239\n"
"v_mfma_f32_16x16x32_bf16 v[172:175], v[220:223], a[188:191], v[172:175]\n"
"v_mul_f32 v131, v115, v131\n"
"v_cvt_pk_bf16_f32 v228, v128, v129\n"
"s_waitcnt lgkmcnt(2)\n"
"v_mfma_f32_16x16x32_bf16 v[144:147], v[208:211], a[192:195], v[144:147]\n"
"ds_read_b64_tr_b16 v[216:217], %[vS0] offset:26624\n"
"v_mul_f32 v131, v131, v239\n"
"v_mfma_f32_16x16x32_bf16 v[148:151], v[208:211], a[196:199], v[148:151]\n"
"ds_read_b64_tr_b16 v[218:219], %[vS1] offset:26624\n"
"v_cvt_pk_bf16_f32 v229, v130, v131\n"
"v_mfma_f32_16x16x32_bf16 v[152:155], v[208:211], a[200:203], v[152:155]\n"
"v_mul_f32 v240, s61, v116\n"
"v_mul_f32 v132, v116, v132\n"
"v_mfma_f32_16x16x32_bf16 v[156:159], v[208:211], a[204:207], v[156:159]\n"
"v_exp_f32 v240, v240\n"
"s_waitcnt lgkmcnt(2)\n"
"v_mfma_f32_16x16x32_bf16 v[160:163], v[212:215], a[192:195], v[160:163]\n"
"ds_read_b64_tr_b16 v[220:221], %[vS0] offset:27648\n"
"v_add_f32 v240, 1.0, v240\n"
"v_mfma_f32_16x16x32_bf16 v[164:167], v[212:215], a[196:199], v[164:167]\n"
"ds_read_b64_tr_b16 v[222:223], %[vS1] offset:27648\n"
"v_mul_f32 v241, s61, v117\n"
"v_mfma_f32_16x16x32_bf16 v[168:171], v[212:215], a[200:203], v[168:171]\n"
"v_rcp_f32 v240, v240\n"
"v_mfma_f32_16x16x32_bf16 v[172:175], v[212:215], a[204:207], v[172:175]\n"
"v_exp_f32 v241, v241\n"
"s_waitcnt lgkmcnt(2)\n"
"v_mfma_f32_16x16x32_bf16 v[144:147], v[216:219], a[208:211], v[144:147]\n"
"ds_read_b64_tr_b16 v[208:209], %[vS0] offset:28672\n"
"v_mul_f32 v132, v132, v240\n"
"v_mfma_f32_16x16x32_bf16 v[148:151], v[216:219], a[212:215], v[148:151]\n"
"ds_read_b64_tr_b16 v[210:211], %[vS1] offset:28672\n"
"v_add_f32 v241, 1.0, v241\n"
"v_mfma_f32_16x16x32_bf16 v[152:155], v[216:219], a[216:219], v[152:155]\n"
"v_rcp_f32 v241, v241\n"
"v_mfma_f32_16x16x32_bf16 v[156:159], v[216:219], a[220:223], v[156:159]\n"
"v_mul_f32 v133, v117, v133\n"
"v_mul_f32 v242, s61, v118\n"
"s_waitcnt lgkmcnt(2)\n"
"v_mfma_f32_16x16x32_bf16 v[160:163], v[220:223], a[208:211], v[160:163]\n"
"ds_read_b64_tr_b16 v[212:213], %[vS0] offset:29696\n"
"v_mul_f32 v133, v133, v241\n"
"v_mfma_f32_16x16x32_bf16 v[164:167], v[220:223], a[212:215], v[164:167]\n"
"ds_read_b64_tr_b16 v[214:215], %[vS1] offset:29696\n"
"v_mul_f32 v134, v118, v134\n"
"v_mfma_f32_16x16x32_bf16 v[168:171], v[220:223], a[216:219], v[168:171]\n"
"v_exp_f32 v242, v242\n"
"v_mfma_f32_16x16x32_bf16 v[172:175], v[220:223], a[220:223], v[172:175]\n"
"v_add_f32 v242, 1.0, v242\n"
"v_mul_f32 v243, s61, v119\n"
"buffer_load_dword v244, %[vFAp], %[srdF4], 0 offen sc1\n"
"s_waitcnt lgkmcnt(2)\n"
"v_mfma_f32_16x16x32_bf16 v[144:147], v[208:211], a[224:227], v[144:147]\n"
"ds_read_b64_tr_b16 v[216:217], %[vS0] offset:30720\n"
"v_mul_f32 v135, v119, v135\n"
"v_mfma_f32_16x16x32_bf16 v[148:151], v[208:211], a[228:231], v[148:151]\n"
"ds_read_b64_tr_b16 v[218:219], %[vS1] offset:30720\n"
"v_cvt_pk_bf16_f32 v230, v132, v133\n"
"v_mfma_f32_16x16x32_bf16 v[152:155], v[208:211], a[232:235], v[152:155]\n"
"v_rcp_f32 v242, v242\n"
"v_mfma_f32_16x16x32_bf16 v[156:159], v[208:211], a[236:239], v[156:159]\n"
"v_exp_f32 v243, v243\n"
"s_waitcnt lgkmcnt(2)\n"
"v_mfma_f32_16x16x32_bf16 v[160:163], v[212:215], a[224:227], v[160:163]\n"
"ds_read_b64_tr_b16 v[220:221], %[vS0] offset:31744\n"
"v_mul_f32 v134, v134, v242\n"
"v_mfma_f32_16x16x32_bf16 v[164:167], v[212:215], a[228:231], v[164:167]\n"
"ds_read_b64_tr_b16 v[222:223], %[vS1] offset:31744\n"
"v_add_f32 v243, 1.0, v243\n"
"v_mfma_f32_16x16x32_bf16 v[168:171], v[212:215], a[232:235], v[168:171]\n"
"v_rcp_f32 v243, v243\n"
"v_mfma_f32_16x16x32_bf16 v[172:175], v[212:215], a[236:239], v[172:175]\n"
"v_mul_f32 v135, v135, v243\n"
"v_mul_f32 v236, s61, v120\n"
"s_waitcnt lgkmcnt(2)\n"
"v_mfma_f32_16x16x32_bf16 v[144:147], v[216:219], a[240:243], v[144:147]\n"
"ds_read_b64_tr_b16 v[208:209], %[vS0] offset:32768\n"
"v_cvt_pk_bf16_f32 v231, v134, v135\n"
"v_mfma_f32_16x16x32_bf16 v[148:151], v[216:219], a[244:247], v[148:151]\n"
"ds_read_b64_tr_b16 v[210:211], %[vS1] offset:32768\n"
"v_permlane16_swap_b32 v228, v230\n"
"v_mfma_f32_16x16x32_bf16 v[152:155], v[216:219], a[248:251], v[152:155]\n"
"v_exp_f32 v236, v236\n"
"v_mfma_f32_16x16x32_bf16 v[156:159], v[216:219], a[252:255], v[156:159]\n"
"v_permlane16_swap_b32 v229, v231\n"
"v_add_f32 v236, 1.0, v236\n"
"s_waitcnt lgkmcnt(2)\n"
"v_mfma_f32_16x16x32_bf16 v[160:163], v[220:223], a[240:243], v[160:163]\n"
"ds_read_b64_tr_b16 v[212:213], %[vS0] offset:33792\n"
"v_mul_f32 v136, v120, v136\n"
"v_mfma_f32_16x16x32_bf16 v[164:167], v[220:223], a[244:247], v[164:167]\n"
"ds_read_b64_tr_b16 v[214:215], %[vS1] offset:33792\n"
"v_mul_f32 v237, s61, v121\n"
"v_mfma_f32_16x16x32_bf16 v[168:171], v[220:223], a[248:251], v[168:171]\n"
"s_add_u32 s62, %[soffA], 3584\n"
"buffer_store_dwordx4 v[228:231], %[vS], %[srdA], s62 offen sc1\n"
"v_mfma_f32_16x16x32_bf16 v[172:175], v[220:223], a[252:255], v[172:175]\n"
"v_rcp_f32 v236, v236\n"
"s_waitcnt lgkmcnt(2)\n"
"v_mfma_f32_16x16x32_bf16 v[144:147], v[208:211], v[16:19], v[144:147]\n"
"ds_read_b64_tr_b16 v[216:217], %[vS0] offset:34816\n"
"v_mul_f32 v136, v136, v236\n"
"v_mfma_f32_16x16x32_bf16 v[148:151], v[208:211], v[20:23], v[148:151]\n"
"ds_read_b64_tr_b16 v[218:219], %[vS1] offset:34816\n"
"v_mul_f32 v137, v121, v137\n"
"v_mfma_f32_16x16x32_bf16 v[152:155], v[208:211], v[24:27], v[152:155]\n"
"v_exp_f32 v237, v237\n"
"v_mfma_f32_16x16x32_bf16 v[156:159], v[208:211], v[28:31], v[156:159]\n"
"v_add_f32 v237, 1.0, v237\n"
"v_mul_f32 v238, s61, v122\n"
"s_waitcnt lgkmcnt(2)\n"
"v_mfma_f32_16x16x32_bf16 v[160:163], v[212:215], v[16:19], v[160:163]\n"
"ds_read_b64_tr_b16 v[220:221], %[vS0] offset:35840\n"
"v_mul_f32 v138, v122, v138\n"
"v_mfma_f32_16x16x32_bf16 v[164:167], v[212:215], v[20:23], v[164:167]\n"
"ds_read_b64_tr_b16 v[222:223], %[vS1] offset:35840\n"
"v_mul_f32 v239, s61, v123\n"
"v_mfma_f32_16x16x32_bf16 v[168:171], v[212:215], v[24:27], v[168:171]\n"
"v_rcp_f32 v237, v237\n"
"v_mfma_f32_16x16x32_bf16 v[172:175], v[212:215], v[28:31], v[172:175]\n"
"v_exp_f32 v238, v238\n"
"s_waitcnt lgkmcnt(2)\n"
"v_mfma_f32_16x16x32_bf16 v[144:147], v[216:219], v[32:35], v[144:147]\n"
"v_exp_f32 v239, v239\n"
"v_mfma_f32_16x16x32_bf16 v[148:151], v[216:219], v[36:39], v[148:151]\n"
"v_mul_f32 v137, v137, v237\n"
"v_add_f32 v238, 1.0, v238\n"
"v_mfma_f32_16x16x32_bf16 v[152:155], v[216:219], v[40:43], v[152:155]\n"
"v_rcp_f32 v238, v238\n"
"v_mfma_f32_16x16x32_bf16 v[156:159], v[216:219], v[44:47], v[156:159]\n"
"v_mul_f32 v138, v138, v238\n"
"v_add_f32 v239, 1.0, v239\n"
"s_waitcnt lgkmcnt(0)\n"
"v_mfma_f32_16x16x32_bf16 v[160:163], v[220:223], v[32:35], v[160:163]\n"
"v_rcp_f32 v239, v239\n"
"v_mfma_f32_16x16x32_bf16 v[164:167], v[220:223], v[36:39], v[164:167]\n"
"v_mul_f32 v139, v123, v139\n"
"v_cvt_pk_bf16_f32 v232, v136, v137\n"
"v_mfma_f32_16x16x32_bf16 v[168:171], v[220:223], v[40:43], v[168:171]\n"
"v_mul_f32 v139, v139, v239\n"
"v_mul_f32 v240, s61, v124\n"
"v_mfma_f32_16x16x32_bf16 v[172:175], v[220:223], v[44:47], v[172:175]\n"
"v_exp_f32 v240, v240\n"
"s_barrier\n"
"s_waitcnt vmcnt(1)\n"
"v_cmp_ne_u32 vcc, %[vFAp], v244\n"
"s_cbranch_vccz .Lfa_ok_%=\n"
"s_movk_i32 s63, 0x2000\n"
".Lfa_poll_%=:\n"
"buffer_load_dword v244, %[vFAp], %[srdF4], 0 offen sc1\n"
"s_waitcnt vmcnt(0)\n"
"v_cmp_ne_u32 vcc, %[vFAp], v244\n"
"s_cbranch_vccz .Lfa_ok_%=\n"
"s_sub_u32 s63, s63, 1\n"
"s_cbranch_scc0 .Lfa_poll_%=\n"
".Lfa_ok_%=:\n"
"v_mbcnt_lo_u32_b32 v23, -1, 0\n"
"v_mbcnt_hi_u32_b32 v23, -1, v23\n"
"v_lshrrev_b32 v24, 4, v23\n"
"v_and_b32 v25, 15, v23\n"
"v_lshlrev_b32 v26, 1, v24\n"
"v_xor_b32 v25, v25, v26\n"
"v_lshlrev_b32 v25, 4, v25\n"
"v_mul_u32_u24 v24, 0x480, v24\n"
"v_add_u32 v24, %[sC5], v24\n"
"v_xor_b32 v26, 0x80, v25\n"
"v_add_u32 v16, v24, v25\n"
"v_add_u32 v26, v24, v26\n"
"v_add_u32 v17, 3584, v16\n"
"v_add_u32 v18, 7168, v26\n"
"v_add_u32 v19, 10752, v26\n"
"v_lshlrev_b32 v20, 4, v23\n"
"v_add_u32 v21, %[sB05], v20\n"
"v_add_u32 v22, 145408, v21\n"
"s_add_u32 s63, %[soffA5], 0\n"
"s_add_u32 m0, %[m0A5], 0\n"
"s_nop 0\n"
"buffer_load_dwordx4 v16, %[srdA5], s63 offen offset:0 lds\n"
"buffer_load_dwordx4 v17, %[srdA5], s63 offen offset:1024 lds\n"
"v_cvt_pk_bf16_f32 v233, v138, v139\n"
"v_add_f32 v240, 1.0, v240\n"
"v_rcp_f32 v240, v240\n"
"v_mul_f32 v140, v124, v140\n"
"v_mul_f32 v140, v140, v240\n"
"v_mul_f32 v241, s61, v125\n"
"v_exp_f32 v241, v241\n"
"v_mul_f32 v141, v125, v141\n"
"v_add_f32 v241, 1.0, v241\n"
"v_rcp_f32 v241, v241\n"
"buffer_load_dwordx4 v18, %[srdA5], s63 offen offset:2048 lds\n"
"v_mul_f32 v141, v141, v241\n"
"v_mul_f32 v242, s61, v126\n"
"v_exp_f32 v242, v242\n"
"v_mul_f32 v142, v126, v142\n"
"v_add_f32 v242, 1.0, v242\n"
"v_rcp_f32 v242, v242\n"
"v_mul_f32 v243, s61, v127\n"
"v_mul_f32 v142, v142, v242\n"
"v_exp_f32 v243, v243\n"
"buffer_load_dwordx4 v19, %[srdA5], s63 offen offset:3072 lds\n"
"v_add_f32 v243, 1.0, v243\n"
"v_rcp_f32 v243, v243\n"
"v_mul_f32 v143, v127, v143\n"
"v_mul_f32 v143, v143, v243\n"
"v_cvt_pk_bf16_f32 v234, v140, v141\n"
"v_cvt_pk_bf16_f32 v235, v142, v143\n"
"v_mul_f32 v236, s61, v144\n"
"v_exp_f32 v236, v236\n"
"v_permlane16_swap_b32 v232, v234\n"
"v_permlane16_swap_b32 v233, v235\n"
"s_add_u32 s62, %[soffA], 298496\n"
"buffer_store_dwordx4 v[232:235], %[vS], %[srdA], s62 offen sc1\n"
"s_add_u32 m0, %[m0A25], 0\n"
"s_add_u32 s62, %[soffRm5], 0\n"
"s_mov_b32 exec_hi, 0\n"
"buffer_load_dwordx4 v20, %[srdRm5], s62 offen lds\n"
"s_mov_b32 exec_hi, -1\n"
"v_add_f32 v236, 1.0, v236\n"
"v_rcp_f32 v236, v236\n"
"v_mul_f32 v160, v144, v160\n"
"v_mul_f32 v160, v160, v236\n"
"v_mul_f32 v237, s61, v145\n"
"v_exp_f32 v237, v237\n"
"v_mul_f32 v161, v145, v161\n"
"v_add_f32 v237, 1.0, v237\n"
"v_rcp_f32 v237, v237\n"
"s_add_u32 s60, %[soffB5], 0\n"
"s_add_u32 m0, %[m0B5], 0\n"
"s_nop 0\n"
"buffer_load_dwordx4 v21, %[srdB5], s60 offen offset:0 sc1 lds\n"
"v_mul_f32 v161, v161, v237\n"
"v_mul_f32 v238, s61, v146\n"
"v_exp_f32 v238, v238\n"
"v_mul_f32 v162, v146, v162\n"
"v_add_f32 v238, 1.0, v238\n"
"v_rcp_f32 v238, v238\n"
"v_mul_f32 v239, s61, v147\n"
"v_mul_f32 v162, v162, v238\n"
"v_exp_f32 v239, v239\n"
"buffer_load_dwordx4 v21, %[srdB5], s60 offen offset:1024 sc1 lds\n"
"v_add_f32 v239, 1.0, v239\n"
"v_rcp_f32 v239, v239\n"
"v_mul_f32 v163, v147, v163\n"
"v_mul_f32 v163, v163, v239\n"
"v_cvt_pk_bf16_f32 v228, v160, v161\n"
"v_cvt_pk_bf16_f32 v229, v162, v163\n"
"v_mul_f32 v240, s61, v148\n"
"v_exp_f32 v240, v240\n"
"v_mul_f32 v164, v148, v164\n"
"v_add_f32 v240, 1.0, v240\n"
"buffer_load_dwordx4 v22, %[srdB5], s60 offen offset:2048 sc1 lds\n"
"v_rcp_f32 v240, v240\n"
"v_mul_f32 v241, s61, v149\n"
"v_mul_f32 v164, v164, v240\n"
"v_exp_f32 v241, v241\n"
"v_mul_f32 v165, v149, v165\n"
"v_add_f32 v241, 1.0, v241\n"
"v_rcp_f32 v241, v241\n"
"buffer_load_dwordx4 v22, %[srdB5], s60 offen offset:3072 sc1 lds\n"
"v_mul_f32 v165, v165, v241\n"
"v_mul_f32 v242, s61, v150\n"
"v_exp_f32 v242, v242\n"
"v_mul_f32 v166, v150, v166\n"
"v_add_f32 v242, 1.0, v242\n"
"v_rcp_f32 v242, v242\n"
"v_mul_f32 v243, s61, v151\n"
"v_mul_f32 v166, v166, v242\n"
"v_exp_f32 v243, v243\n"
"s_add_u32 s63, %[soffA5], 147456\n"
"s_add_u32 m0, %[m0A5], 34816\n"
"s_nop 0\n"
"buffer_load_dwordx4 v16, %[srdA5], s63 offen offset:0 lds\n"
"v_add_f32 v243, 1.0, v243\n"
"v_rcp_f32 v243, v243\n"
"v_mul_f32 v167, v151, v167\n"
"v_mul_f32 v167, v167, v243\n"
"v_cvt_pk_bf16_f32 v230, v164, v165\n"
"v_cvt_pk_bf16_f32 v231, v166, v167\n"
"v_mul_f32 v236, s61, v152\n"
"v_exp_f32 v236, v236\n"
"v_permlane16_swap_b32 v228, v230\n"
"v_permlane16_swap_b32 v229, v231\n"
"s_add_u32 s62, %[soffAR], 0\n"
"buffer_store_dwordx4 v[228:231], %[vS], %[srdA], s62 offen sc1\n"
"buffer_load_dwordx4 v17, %[srdA5], s63 offen offset:1024 lds\n"
"v_add_f32 v236, 1.0, v236\n"
"v_rcp_f32 v236, v236\n"
"v_mul_f32 v168, v152, v168\n"
"v_mul_f32 v168, v168, v236\n"
"v_mul_f32 v237, s61, v153\n"
"v_exp_f32 v237, v237\n"
"v_mul_f32 v169, v153, v169\n"
"v_add_f32 v237, 1.0, v237\n"
"v_rcp_f32 v237, v237\n"
"buffer_load_dwordx4 v18, %[srdA5], s63 offen offset:2048 lds\n"
"v_mul_f32 v169, v169, v237\n"
"v_mul_f32 v238, s61, v154\n"
"v_exp_f32 v238, v238\n"
"v_mul_f32 v170, v154, v170\n"
"v_add_f32 v238, 1.0, v238\n"
"v_rcp_f32 v238, v238\n"
"v_mul_f32 v239, s61, v155\n"
"v_mul_f32 v170, v170, v238\n"
"v_exp_f32 v239, v239\n"
"buffer_load_dwordx4 v19, %[srdA5], s63 offen offset:3072 lds\n"
"v_add_f32 v239, 1.0, v239\n"
"v_rcp_f32 v239, v239\n"
"v_mul_f32 v171, v155, v171\n"
"v_mul_f32 v171, v171, v239\n"
"v_cvt_pk_bf16_f32 v232, v168, v169\n"
"v_cvt_pk_bf16_f32 v233, v170, v171\n"
"v_mul_f32 v240, s61, v156\n"
"v_exp_f32 v240, v240\n"
"v_mul_f32 v172, v156, v172\n"
"v_add_f32 v240, 1.0, v240\n"
"s_add_u32 m0, %[m0A25], 34816\n"
"s_add_u32 s62, %[soffRm5], 4096\n"
"s_mov_b32 exec_hi, 0\n"
"buffer_load_dwordx4 v20, %[srdRm5], s62 offen lds\n"
"s_mov_b32 exec_hi, -1\n"
"v_rcp_f32 v240, v240\n"
"v_mul_f32 v241, s61, v157\n"
"v_mul_f32 v172, v172, v240\n"
"v_exp_f32 v241, v241\n"
"v_mul_f32 v173, v157, v173\n"
"v_add_f32 v241, 1.0, v241\n"
"v_rcp_f32 v241, v241\n"
"s_add_u32 s60, %[soffB5], 4096\n"
"s_add_u32 m0, %[m0B5], 34816\n"
"s_nop 0\n"
"buffer_load_dwordx4 v21, %[srdB5], s60 offen offset:0 sc1 lds\n"
"v_mul_f32 v173, v173, v241\n"
"v_mul_f32 v242, s61, v158\n"
"v_exp_f32 v242, v242\n"
"v_mul_f32 v174, v158, v174\n"
"v_add_f32 v242, 1.0, v242\n"
"v_rcp_f32 v242, v242\n"
"v_mul_f32 v243, s61, v159\n"
"v_mul_f32 v174, v174, v242\n"
"v_exp_f32 v243, v243\n"
"buffer_load_dwordx4 v21, %[srdB5], s60 offen offset:1024 sc1 lds\n"
"v_add_f32 v243, 1.0, v243\n"
"v_rcp_f32 v243, v243\n"
"v_mul_f32 v175, v159, v175\n"
"v_mul_f32 v175, v175, v243\n"
"v_cvt_pk_bf16_f32 v234, v172, v173\n"
"v_cvt_pk_bf16_f32 v235, v174, v175\n"
"s_nop 0\n"
"s_nop 0\n"
"v_permlane16_swap_b32 v232, v234\n"
"v_permlane16_swap_b32 v233, v235\n"
"s_add_u32 s62, %[soffAR], 294912\n"
"buffer_store_dwordx4 v[232:235], %[vS], %[srdA], s62 offen sc1\n"
"buffer_load_dwordx4 v22, %[srdB5], s60 offen offset:2048 sc1 lds\n"
"buffer_load_dwordx4 v22, %[srdB5], s60 offen offset:3072 sc1 lds\n"
    :: [srdG]"s"(srdG), [srdU]"s"(srdU), [srdR]"s"(srdR), [srdH]"s"(srdH), [srdA]"s"(srdA), [soffW]"s"(soffW), [m0W]"s"(m0W), [soffR]"s"(soffR), [m0R]"s"(m0R),
       [soffH]"s"(soffH), [soffA]"s"(soffA), [soffAR]"s"(soffAR),
       [vW]"v"(vW), [vR]"v"(vR), [vT0]"v"(vT0), [vT1]"v"(vT1), [vT2]"v"(vT2), [vT3]"v"(vT3), [vS0]"v"(vS0), [vS1]"v"(vS1), [vH]"v"(vH), [vS]"v"(vS), [srdA5]"s"(srdA5), [srdB5]"s"(srdB5), [srdRm5]"s"(srdRm5), [soffA5]"s"(soffA5), [soffB5]"s"(soffB5), [soffRm5]"s"(soffRm5), [m0A5]"s"(m0A5), [m0A25]"s"(m0A25), [m0B5]"s"(m0B5), [sB05]"s"(sB05), [sC5]"s"(sC5), [srdF4]"s"(srdF4), [vFA]"v"(vFA), [vFAp]"v"(vFAp) , [srdPF]"s"(srdPF), [vPF]"v"(vPF), [srdC]"s"(srdC), [vC]"v"(vC)
    : "memory", "s60", "s61", "s62", "s63", "vcc",  "scc", "v16","v17","v18","v19","v20","v21","v22","v23","v24","v25","v26","v27","v28","v29","v30","v31","v32","v33","v34","v35","v36","v37","v38","v39","v40","v41","v42","v43","v44","v45","v46","v47","v48","v49","v50","v51","v52","v53","v54","v55","v56","v57","v58","v59","v60","v61","v62","v63","v64","v65","v66","v67","v68","v69","v70","v71","v72","v73","v74","v75","v76","v77","v78","v79","v80","v81","v82","v83","v84","v85","v86","v87","v88","v89","v90","v91","v92","v93","v94","v95","v96","v97","v98","v99","v100","v101","v102","v103","v104","v105","v106","v107","v108","v109","v110","v111","v112","v113","v114","v115","v116","v117","v118","v119","v120","v121","v122","v123","v124","v125","v126","v127","v128","v129","v130","v131","v132","v133","v134","v135","v136","v137","v138","v139","v140","v141","v142","v143","v144","v145","v146","v147","v148","v149","v150","v151","v152","v153","v154","v155","v156","v157","v158","v159","v160","v161","v162","v163","v164","v165","v166","v167","v168","v169","v170","v171","v172","v173","v174","v175","v176","v177","v178","v179","v180","v181","v182","v183","v184","v185","v186","v187","v188","v189","v190","v191","v192","v193","v194","v195","v196","v197","v198","v199","v200","v201","v202","v203","v204","v205","v206","v207","v208","v209","v210","v211","v212","v213","v214","v215","v216","v217","v218","v219","v220","v221","v222","v223","v224","v225","v226","v227","v228","v229","v230","v231","v232","v233","v234","v235","v236","v237","v238","v239","v240","v241","v242","v243","v244","v245","v246","v247","v248","v249","v250","v251","v252","v253","v254","v255", "a0","a1","a2","a3","a4","a5","a6","a7","a8","a9","a10","a11","a12","a13","a14","a15","a16","a17","a18","a19","a20","a21","a22","a23","a24","a25","a26","a27","a28","a29","a30","a31","a32","a33","a34","a35","a36","a37","a38","a39","a40","a41","a42","a43","a44","a45","a46","a47","a48","a49","a50","a51","a52","a53","a54","a55","a56","a57","a58","a59","a60","a61","a62","a63","a64","a65","a66","a67","a68","a69","a70","a71","a72","a73","a74","a75","a76","a77","a78","a79","a80","a81","a82","a83","a84","a85","a86","a87","a88","a89","a90","a91","a92","a93","a94","a95","a96","a97","a98","a99","a100","a101","a102","a103","a104","a105","a106","a107","a108","a109","a110","a111","a112","a113","a114","a115","a116","a117","a118","a119","a120","a121","a122","a123","a124","a125","a126","a127","a128","a129","a130","a131","a132","a133","a134","a135","a136","a137","a138","a139","a140","a141","a142","a143","a144","a145","a146","a147","a148","a149","a150","a151","a152","a153","a154","a155","a156","a157","a158","a159","a160","a161","a162","a163","a164","a165","a166","a167","a168","a169","a170","a171","a172","a173","a174","a175","a176","a177","a178","a179","a180","a181","a182","a183","a184","a185","a186","a187","a188","a189","a190","a191","a192","a193","a194","a195","a196","a197","a198","a199","a200","a201","a202","a203","a204","a205","a206","a207","a208","a209","a210","a211","a212","a213","a214","a215","a216","a217","a218","a219","a220","a221","a222","a223","a224","a225","a226","a227","a228","a229","a230","a231","a232","a233","a234","a235","a236","a237","a238","a239","a240","a241","a242","a243","a244","a245","a246","a247","a248","a249","a250","a251","a252","a253","a254","a255");
  asm volatile("kc4e:");
  unsigned wl = w, bl = b;
  {

  CODE_LABEL("kc5b");
  KARG_LOAD4(ka, 0x18); KARG_LOAD4(kb, 0x28);
  __builtin_amdgcn_sched_barrier(0);
  unsigned l = lane_id();
  const unsigned w = wl;
  const unsigned b = bl, xcd = b & 7, bi = b >> 3;
  const unsigned rt = 8 * (xcd & 1) + (bi & 7), kq = xcd >> 1, ct = bi >> 3;
  const unsigned rg = w & 1, ch = w >> 1;
  const unsigned lds = (unsigned)(size_t)smem;

  const unsigned soffA = (1024u * kq + 16u * w) * 1152u, soffA2 = (4096u + 128u * kq + 16u * w) * 1152u;
  const unsigned m0A = lds + (w >> 1) * 9216u + (w & 1) * 4096u, m0A2 = lds + (w >> 1) * 9216u + 8192u + (w & 1) * 512u, m0B = lds + 18432u + w * 4096u;
  const unsigned r4 = l >> 4;
  const unsigned vm0 = r4 * 1152u + 256u * ct + 16u * ((l & 15) ^ (r4 << 1)), vm1 = r4 * 1152u + 256u * ct + 16u * ((l & 15) ^ (r4 << 1) ^ 8);
  const unsigned vA0 = vm0, vA1 = vm0 + 4u * 1152u - 1024u, vA2 = vm1 + 8u * 1152u - 2048u, vA3 = vm1 + 12u * 1152u - 3072u;
  const unsigned vAh = l * 16u, soffRm = (kq * 4u + ct) * 36864u + w * 512u;
  const unsigned vB0 = (8u * rt + 2u * w) * 147456u + l * 16u, vB1 = vB0 + 147456u - 2048u;
  const unsigned soffB = 36u * kq * 1024u;
  asm volatile("s_waitcnt lgkmcnt(0)" : "+s"(ka), "+s"(kb) : "v"(vA0), "v"(vA1), "v"(vA2), "v"(vA3), "v"(vAh), "v"(vB0), "v"(vB1), "s"(soffA), "s"(m0A), "s"(m0A2), "s"(m0B), "s"(soffB), "s"(soffRm));
  u32x4 srdA = srd_of(ka[2], ka[3], 4608u * 576u * 2u);
  u32x4 srdB = srd_of(ka[0], ka[1], 2048u * 4608u * 2u);
  const unsigned long long rm = (((unsigned long long)ka[1] << 32) | ka[0]) + (18u << 20) + 1152u * 1024u;
  u32x4 srdRm = srd_of((unsigned)rm, (unsigned)(rm >> 32), 576u * 1024u);
  const unsigned g = l >> 4;
  u32x4 srdP = srd_of(kb[0], kb[1], 4u * 2048u * 576u * 2u);
  const unsigned vP = l * 16u, soffP = (((kq * 16 + rt) * 4 + ct) * 4 + w) * 9216u;
  const unsigned q = (l & 15) >> 2, p = l & 3, sw = q | ((g & 1) << 2);
  const unsigned tA = lds + (8 * g + q) * 256u + 8u * p;
  const unsigned vT00 = tA + (((4 * ch + 0) ^ sw) << 5), vT10 = tA + (((4 * ch + 1) ^ sw) << 5), vT20 = tA + (((4 * ch + 2) ^ sw) << 5), vT30 = tA + (((4 * ch + 3) ^ sw) << 5);
  const unsigned vT01 = vT00 + 2u * 34816u, vT11 = vT10 + 2u * 34816u, vT21 = vT20 + 2u * 34816u, vT31 = vT30 + 2u * 34816u;
  const unsigned vS00 = lds + 8192u + (8 * g + (4 * (g & 1)) + q) * 32u + 8u * p, vS10 = lds + 8192u + (8 * g + (4 * ((g & 1) ^ 1)) + q) * 32u + 8u * p;
  const unsigned vS01 = vS00 + 2u * 34816u, vS11 = vS10 + 2u * 34816u;
  const unsigned tb = lds + 18432u + 4u * rg * 2048u + l * 16u;
  const unsigned vTBa0 = tb + ch * 4096u, vTBb0 = tb + (ch ? 0u - 4096u : 0u), vTBa1 = vTBa0 + 2u * 34816u, vTBb1 = vTBb0 + 2u * 34816u;
  const unsigned js = (kq + 2u * ch) & 3u, vP8 = (ch == (kq >> 1) && (g & 1u) == (kq & 1u)) ? 0x7fffffffu : vP;
  u32x4 xo0, xo1, xo2;
  CODE_SRD(srdC, "kc6b", "kc6e");
  const unsigned vC = (l * 32u + bi) * 128u;

  const unsigned long long fb4 = (((unsigned long long)kb[1] << 32) | kb[0]) + 0xB84000u;
  const u32x4 srdF4 = srd_of((unsigned)fb4, (unsigned)(fb4 >> 32), 3u * 16384u);
  const unsigned vFB = (w == 3u && l == 0u) ? 0x4000u + b * 64u : 0x7fffffffu, vFBp = 0x4000u + (xcd + 8u * (((bi & 7u) >> 1) + 4u * (l & 7u))) * 64u;
  asm volatile(
"s_waitcnt vmcnt(11)\n"
"s_barrier\n"
"ds_read_b128 a[0:3], %[vTBa0] offset:0\n"
"ds_read_b64_tr_b16 v[112:113], %[vT00] offset:0\n"
"ds_read_b64_tr_b16 v[114:115], %[vT00] offset:1024\n"
"ds_read_b128 a[4:7], %[vTBa0] offset:2048\n"
"ds_read_b128 a[8:11], %[vTBb0] offset:4096\n"
"ds_read_b128 a[12:15], %[vTBb0] offset:6144\n"
"ds_read_b64_tr_b16 v[116:117], %[vT10] offset:0\n"
"ds_read_b64_tr_b16 v[118:119], %[vT10] offset:1024\n"
"s_waitcnt lgkmcnt(2)\n"
"v_mfma_f32_16x16x32_bf16 v[32:35], v[112:115], a[0:3], 0\n"
"ds_read_b64_tr_b16 v[120:121], %[vT20] offset:0\n"
"v_mfma_f32_16x16x32_bf16 v[36:39], v[112:115], a[4:7], 0\n"
"ds_read_b64_tr_b16 v[122:123], %[vT20] offset:1024\n"
"v_mfma_f32_16x16x32_bf16 v[40:43], v[112:115], a[8:11], 0\n"
"ds_read_b128 a[16:19], %[vTBa0] offset:1024\n"
"v_mfma_f32_16x16x32_bf16 v[44:47], v[112:115], a[12:15], 0\n"
"ds_read_b128 a[20:23], %[vTBa0] offset:3072\n"
"s_waitcnt lgkmcnt(4)\n"
"v_mfma_f32_16x16x32_bf16 v[48:51], v[116:119], a[0:3], 0\n"
"ds_read_b128 a[24:27], %[vTBb0] offset:5120\n"
"v_mfma_f32_16x16x32_bf16 v[52:55], v[116:119], a[4:7], 0\n"
"ds_read_b128 a[28:31], %[vTBb0] offset:7168\n"
"v_mfma_f32_16x16x32_bf16 v[56:59], v[116:119], a[8:11], 0\n"
"ds_read_b64_tr_b16 v[124:125], %[vT30] offset:0\n"
"v_mfma_f32_16x16x32_bf16 v[60:63], v[116:119], a[12:15], 0\n"
"ds_read_b64_tr_b16 v[126:127], %[vT30] offset:1024\n"
"s_waitcnt lgkmcnt(6)\n"
"v_mfma_f32_16x16x32_bf16 v[64:67], v[120:123], a[0:3], 0\n"
"ds_read_b64_tr_b16 v[128:129], %[vS00] offset:0\n"
"v_mfma_f32_16x16x32_bf16 v[68:71], v[120:123], a[4:7], 0\n"
"ds_read_b64_tr_b16 v[130:131], %[vS10] offset:0\n"
"v_mfma_f32_16x16x32_bf16 v[72:75], v[120:123], a[8:11], 0\n"
"s_add_u32 s61, %[soffA], 294912\n"
"s_add_u32 m0, %[m0A], 69632\n"
"s_nop 0\n"
"buffer_load_dwordx4 %[vA0], %[srdA], s61 offen offset:0 lds\n"
"v_mfma_f32_16x16x32_bf16 v[76:79], v[120:123], a[12:15], 0\n"
"buffer_load_dwordx4 %[vA1], %[srdA], s61 offen offset:1024 lds\n"
"s_waitcnt lgkmcnt(2)\n"
"v_mfma_f32_16x16x32_bf16 v[80:83], v[124:127], a[0:3], 0\n"
"ds_read_b64_tr_b16 v[112:113], %[vT00] offset:9216\n"
"v_mfma_f32_16x16x32_bf16 v[84:87], v[124:127], a[4:7], 0\n"
"ds_read_b64_tr_b16 v[114:115], %[vT00] offset:10240\n"
"v_mfma_f32_16x16x32_bf16 v[88:91], v[124:127], a[8:11], 0\n"
"buffer_load_dwordx4 %[vA2], %[srdA], s61 offen offset:2048 lds\n"
"v_mfma_f32_16x16x32_bf16 v[92:95], v[124:127], a[12:15], 0\n"
"buffer_load_dwordx4 %[vA3], %[srdA], s61 offen offset:3072 lds\n"
"s_waitcnt lgkmcnt(2)\n"
"v_mfma_f32_16x16x32_bf16 v[96:99], v[128:131], a[0:3], 0\n"
"ds_read_b64_tr_b16 v[116:117], %[vT10] offset:9216\n"
"v_mfma_f32_16x16x32_bf16 v[100:103], v[128:131], a[4:7], 0\n"
"ds_read_b64_tr_b16 v[118:119], %[vT10] offset:10240\n"
"s_waitcnt lgkmcnt(2)\n"
"v_mfma_f32_16x16x32_bf16 v[32:35], v[112:115], a[16:19], v[32:35]\n"
"ds_read_b64_tr_b16 v[120:121], %[vT20] offset:9216\n"
"v_mfma_f32_16x16x32_bf16 v[36:39], v[112:115], a[20:23], v[36:39]\n"
"ds_read_b64_tr_b16 v[122:123], %[vT20] offset:10240\n"
"v_mfma_f32_16x16x32_bf16 v[40:43], v[112:115], a[24:27], v[40:43]\n"
"s_add_u32 m0, %[m0A2], 69632\n"
"s_add_u32 s62, %[soffRm], 8192\n"
"s_mov_b32 exec_hi, 0\n"
"buffer_load_dwordx4 %[vAh], %[srdRm], s62 offen lds\n"
"s_mov_b32 exec_hi, -1\n"
"v_mfma_f32_16x16x32_bf16 v[44:47], v[112:115], a[28:31], v[44:47]\n"
"s_add_u32 s60, %[soffB], 8192\n"
"s_add_u32 m0, %[m0B], 69632\n"
"s_nop 0\n"
"buffer_load_dwordx4 %[vB0], %[srdB], s60 offen offset:0 sc1 lds\n"
"s_waitcnt lgkmcnt(2)\n"
"v_mfma_f32_16x16x32_bf16 v[48:51], v[116:119], a[16:19], v[48:51]\n"
"ds_read_b64_tr_b16 v[124:125], %[vT30] offset:9216\n"
"v_mfma_f32_16x16x32_bf16 v[52:55], v[116:119], a[20:23], v[52:55]\n"
"ds_read_b64_tr_b16 v[126:127], %[vT30] offset:10240\n"
"v_mfma_f32_16x16x32_bf16 v[56:59], v[116:119], a[24:27], v[56:59]\n"
"buffer_load_dwordx4 %[vB0], %[srdB], s60 offen offset:1024 sc1 lds\n"
"v_mfma_f32_16x16x32_bf16 v[60:63], v[116:119], a[28:31], v[60:63]\n"
"buffer_load_dwordx4 %[vB1], %[srdB], s60 offen offset:2048 sc1 lds\n"
"s_waitcnt lgkmcnt(2)\n"
"v_mfma_f32_16x16x32_bf16 v[64:67], v[120:123], a[16:19], v[64:67]\n"
"ds_read_b64_tr_b16 v[128:129], %[vS00] offset:9216\n"
"v_mfma_f32_16x16x32_bf16 v[68:71], v[120:123], a[20:23], v[68:71]\n"
"buffer_load_dwordx4 %[vB1], %[srdB], s60 offen offset:3072 sc1 lds\n"
"s_waitcnt vmcnt(9)\n"
"s_barrier\n"
"buffer_store_dword %[vFB], %[vFB], %[srdF4], 0 offen sc1\n"
"ds_read_b64_tr_b16 v[130:131], %[vS10] offset:9216\n"
"v_mfma_f32_16x16x32_bf16 v[72:75], v[120:123], a[24:27], v[72:75]\n"
"ds_read_b128 a[0:3], %[vTBa0] offset:34816\n"
"v_mfma_f32_16x16x32_bf16 v[76:79], v[120:123], a[28:31], v[76:79]\n"
"ds_read_b128 a[4:7], %[vTBa0] offset:36864\n"
"s_waitcnt lgkmcnt(4)\n"
"v_mfma_f32_16x16x32_bf16 v[80:83], v[124:127], a[16:19], v[80:83]\n"
"ds_read_b128 a[8:11], %[vTBb0] offset:38912\n"
"v_mfma_f32_16x16x32_bf16 v[84:87], v[124:127], a[20:23], v[84:87]\n"
"ds_read_b128 a[12:15], %[vTBb0] offset:40960\n"
"v_mfma_f32_16x16x32_bf16 v[88:91], v[124:127], a[24:27], v[88:91]\n"
"ds_read_b64_tr_b16 v[112:113], %[vT00] offset:34816\n"
"v_mfma_f32_16x16x32_bf16 v[92:95], v[124:127], a[28:31], v[92:95]\n"
"ds_read_b64_tr_b16 v[114:115], %[vT00] offset:35840\n"
"s_waitcnt lgkmcnt(6)\n"
"v_mfma_f32_16x16x32_bf16 v[96:99], v[128:131], a[16:19], v[96:99]\n"
"ds_read_b64_tr_b16 v[116:117], %[vT10] offset:34816\n"
"v_mfma_f32_16x16x32_bf16 v[100:103], v[128:131], a[20:23], v[100:103]\n"
"ds_read_b64_tr_b16 v[118:119], %[vT10] offset:35840\n"
"s_waitcnt lgkmcnt(2)\n"
"v_mfma_f32_16x16x32_bf16 v[32:35], v[112:115], a[0:3], v[32:35]\n"
"ds_read_b64_tr_b16 v[120:121], %[vT20] offset:34816\n"
"v_mfma_f32_16x16x32_bf16 v[36:39], v[112:115], a[4:7], v[36:39]\n"
"ds_read_b64_tr_b16 v[122:123], %[vT20] offset:35840\n"
"v_mfma_f32_16x16x32_bf16 v[40:43], v[112:115], a[8:11], v[40:43]\n"
"ds_read_b128 a[16:19], %[vTBa0] offset:35840\n"
"v_mfma_f32_16x16x32_bf16 v[44:47], v[112:115], a[12:15], v[44:47]\n"
"ds_read_b128 a[20:23], %[vTBa0] offset:37888\n"
"s_waitcnt lgkmcnt(4)\n"
"v_mfma_f32_16x16x32_bf16 v[48:51], v[116:119], a[0:3], v[48:51]\n"
"ds_read_b128 a[24:27], %[vTBb0] offset:39936\n"
"v_mfma_f32_16x16x32_bf16 v[52:55], v[116:119], a[4:7], v[52:55]\n"
"ds_read_b128 a[28:31], %[vTBb0] offset:41984\n"
"v_mfma_f32_16x16x32_bf16 v[56:59], v[116:119], a[8:11], v[56:59]\n"
"ds_read_b64_tr_b16 v[124:125], %[vT30] offset:34816\n"
"v_mfma_f32_16x16x32_bf16 v[60:63], v[116:119], a[12:15], v[60:63]\n"
"ds_read_b64_tr_b16 v[126:127], %[vT30] offset:35840\n"
"s_waitcnt lgkmcnt(6)\n"
"v_mfma_f32_16x16x32_bf16 v[64:67], v[120:123], a[0:3], v[64:67]\n"
"ds_read_b64_tr_b16 v[128:129], %[vS00] offset:34816\n"
"v_mfma_f32_16x16x32_bf16 v[68:71], v[120:123], a[4:7], v[68:71]\n"
"ds_read_b64_tr_b16 v[130:131], %[vS10] offset:34816\n"
"v_mfma_f32_16x16x32_bf16 v[72:75], v[120:123], a[8:11], v[72:75]\n"
"s_add_u32 s61, %[soffA], 442368\n"
"s_add_u32 m0, %[m0A], 104448\n"
"s_nop 0\n"
"buffer_load_dwordx4 %[vA0], %[srdA], s61 offen offset:0 lds\n"
"v_mfma_f32_16x16x32_bf16 v[76:79], v[120:123], a[12:15], v[76:79]\n"
"buffer_load_dwordx4 %[vA1], %[srdA], s61 offen offset:1024 lds\n"
"s_waitcnt lgkmcnt(2)\n"
"v_mfma_f32_16x16x32_bf16 v[80:83], v[124:127], a[0:3], v[80:83]\n"
"ds_read_b64_tr_b16 v[112:113], %[vT00] offset:44032\n"
"v_mfma_f32_16x16x32_bf16 v[84:87], v[124:127], a[4:7], v[84:87]\n"
"ds_read_b64_tr_b16 v[114:115], %[vT00] offset:45056\n"
"v_mfma_f32_16x16x32_bf16 v[88:91], v[124:127], a[8:11], v[88:91]\n"
"buffer_load_dwordx4 %[vA2], %[srdA], s61 offen offset:2048 lds\n"
"v_mfma_f32_16x16x32_bf16 v[92:95], v[124:127], a[12:15], v[92:95]\n"
"buffer_load_dwordx4 %[vA3], %[srdA], s61 offen offset:3072 lds\n"
"s_waitcnt lgkmcnt(2)\n"
"v_mfma_f32_16x16x32_bf16 v[96:99], v[128:131], a[0:3], v[96:99]\n"
"ds_read_b64_tr_b16 v[116:117], %[vT10] offset:44032\n"
"v_mfma_f32_16x16x32_bf16 v[100:103], v[128:131], a[4:7], v[100:103]\n"
"ds_read_b64_tr_b16 v[118:119], %[vT10] offset:45056\n"
"s_waitcnt lgkmcnt(2)\n"
"v_mfma_f32_16x16x32_bf16 v[32:35], v[112:115], a[16:19], v[32:35]\n"
"ds_read_b64_tr_b16 v[120:121], %[vT20] offset:44032\n"
"v_mfma_f32_16x16x32_bf16 v[36:39], v[112:115], a[20:23], v[36:39]\n"
"ds_read_b64_tr_b16 v[122:123], %[vT20] offset:45056\n"
"v_mfma_f32_16x16x32_bf16 v[40:43], v[112:115], a[24:27], v[40:43]\n"
"s_add_u32 m0, %[m0A2], 104448\n"
"s_add_u32 s62, %[soffRm], 12288\n"
"s_mov_b32 exec_hi, 0\n"
"buffer_load_dwordx4 %[vAh], %[srdRm], s62 offen lds\n"
"s_mov_b32 exec_hi, -1\n"
"v_mfma_f32_16x16x32_bf16 v[44:47], v[112:115], a[28:31], v[44:47]\n"
"s_add_u32 s60, %[soffB], 12288\n"
"s_add_u32 m0, %[m0B], 104448\n"
"s_nop 0\n"
"buffer_load_dwordx4 %[vB0], %[srdB], s60 offen offset:0 sc1 lds\n"
"s_waitcnt lgkmcnt(2)\n"
"v_mfma_f32_16x16x32_bf16 v[48:51], v[116:119], a[16:19], v[48:51]\n"
"ds_read_b64_tr_b16 v[124:125], %[vT30] offset:44032\n"
"v_mfma_f32_16x16x32_bf16 v[52:55], v[116:119], a[20:23], v[52:55]\n"
"ds_read_b64_tr_b16 v[126:127], %[vT30] offset:45056\n"
"v_mfma_f32_16x16x32_bf16 v[56:59], v[116:119], a[24:27], v[56:59]\n"
"buffer_load_dwordx4 %[vB0], %[srdB], s60 offen offset:1024 sc1 lds\n"
"v_mfma_f32_16x16x32_bf16 v[60:63], v[116:119], a[28:31], v[60:63]\n"
"buffer_load_dwordx4 %[vB1], %[srdB], s60 offen offset:2048 sc1 lds\n"
"s_waitcnt lgkmcnt(2)\n"
"v_mfma_f32_16x16x32_bf16 v[64:67], v[120:123], a[16:19], v[64:67]\n"
"ds_read_b64_tr_b16 v[128:129], %[vS00] offset:44032\n"
"v_mfma_f32_16x16x32_bf16 v[68:71], v[120:123], a[20:23], v[68:71]\n"
"buffer_load_dwordx4 %[vB1], %[srdB], s60 offen offset:3072 sc1 lds\n"
"s_waitcnt vmcnt(10)\n"
"s_barrier\n"
"ds_read_b64_tr_b16 v[130:131], %[vS10] offset:44032\n"
"v_mfma_f32_16x16x32_bf16 v[72:75], v[120:123], a[24:27], v[72:75]\n"
"ds_read_b128 a[0:3], %[vTBa1] offset:0\n"
"v_mfma_f32_16x16x32_bf16 v[76:79], v[120:123], a[28:31], v[76:79]\n"
"ds_read_b128 a[4:7], %[vTBa1] offset:2048\n"
"s_waitcnt lgkmcnt(4)\n"
"v_mfma_f32_16x16x32_bf16 v[80:83], v[124:127], a[16:19], v[80:83]\n"
"ds_read_b128 a[8:11], %[vTBb1] offset:4096\n"
"v_mfma_f32_16x16x32_bf16 v[84:87], v[124:127], a[20:23], v[84:87]\n"
"ds_read_b128 a[12:15], %[vTBb1] offset:6144\n"
"v_mfma_f32_16x16x32_bf16 v[88:91], v[124:127], a[24:27], v[88:91]\n"
"ds_read_b64_tr_b16 v[112:113], %[vT01] offset:0\n"
"v_mfma_f32_16x16x32_bf16 v[92:95], v[124:127], a[28:31], v[92:95]\n"
"ds_read_b64_tr_b16 v[114:115], %[vT01] offset:1024\n"
"s_waitcnt lgkmcnt(6)\n"
"v_mfma_f32_16x16x32_bf16 v[96:99], v[128:131], a[16:19], v[96:99]\n"
"ds_read_b64_tr_b16 v[116:117], %[vT11] offset:0\n"
"v_mfma_f32_16x16x32_bf16 v[100:103], v[128:131], a[20:23], v[100:103]\n"
"ds_read_b64_tr_b16 v[118:119], %[vT11] offset:1024\n"
"s_waitcnt lgkmcnt(2)\n"
"v_mfma_f32_16x16x32_bf16 v[32:35], v[112:115], a[0:3], v[32:35]\n"
"ds_read_b64_tr_b16 v[120:121], %[vT21] offset:0\n"
"v_mfma_f32_16x16x32_bf16 v[36:39], v[112:115], a[4:7], v[36:39]\n"
"ds_read_b64_tr_b16 v[122:123], %[vT21] offset:1024\n"
"v_mfma_f32_16x16x32_bf16 v[40:43], v[112:115], a[8:11], v[40:43]\n"
"ds_read_b128 a[16:19], %[vTBa1] offset:1024\n"
"v_mfma_f32_16x16x32_bf16 v[44:47], v[112:115], a[12:15], v[44:47]\n"
"ds_read_b128 a[20:23], %[vTBa1] offset:3072\n"
"s_waitcnt lgkmcnt(4)\n"
"v_mfma_f32_16x16x32_bf16 v[48:51], v[116:119], a[0:3], v[48:51]\n"
"ds_read_b128 a[24:27], %[vTBb1] offset:5120\n"
"v_mfma_f32_16x16x32_bf16 v[52:55], v[116:119], a[4:7], v[52:55]\n"
"ds_read_b128 a[28:31], %[vTBb1] offset:7168\n"
"v_mfma_f32_16x16x32_bf16 v[56:59], v[116:119], a[8:11], v[56:59]\n"
"ds_read_b64_tr_b16 v[124:125], %[vT31] offset:0\n"
"v_mfma_f32_16x16x32_bf16 v[60:63], v[116:119], a[12:15], v[60:63]\n"
"ds_read_b64_tr_b16 v[126:127], %[vT31] offset:1024\n"
"s_waitcnt lgkmcnt(6)\n"
"v_mfma_f32_16x16x32_bf16 v[64:67], v[120:123], a[0:3], v[64:67]\n"
"ds_read_b64_tr_b16 v[128:129], %[vS01] offset:0\n"
"v_mfma_f32_16x16x32_bf16 v[68:71], v[120:123], a[4:7], v[68:71]\n"
"ds_read_b64_tr_b16 v[130:131], %[vS11] offset:0\n"
"v_mfma_f32_16x16x32_bf16 v[72:75], v[120:123], a[8:11], v[72:75]\n"
"s_add_u32 s61, %[soffA], 589824\n"
"s_add_u32 m0, %[m0A], 0\n"
"s_nop 0\n"
"buffer_load_dwordx4 %[vA0], %[srdA], s61 offen offset:0 lds\n"
"v_mfma_f32_16x16x32_bf16 v[76:79], v[120:123], a[12:15], v[76:79]\n"
"buffer_load_dwordx4 %[vA1], %[srdA], s61 offen offset:1024 lds\n"
"s_waitcnt lgkmcnt(2)\n"
"v_mfma_f32_16x16x32_bf16 v[80:83], v[124:127], a[0:3], v[80:83]\n"
"ds_read_b64_tr_b16 v[112:113], %[vT01] offset:9216\n"
"v_mfma_f32_16x16x32_bf16 v[84:87], v[124:127], a[4:7], v[84:87]\n"
"ds_read_b64_tr_b16 v[114:115], %[vT01] offset:10240\n"
"v_mfma_f32_16x16x32_bf16 v[88:91], v[124:127], a[8:11], v[88:91]\n"
"buffer_load_dwordx4 %[vA2], %[srdA], s61 offen offset:2048 lds\n"
"v_mfma_f32_16x16x32_bf16 v[92:95], v[124:127], a[12:15], v[92:95]\n"
"buffer_load_dwordx4 %[vA3], %[srdA], s61 offen offset:3072 lds\n"
"s_waitcnt lgkmcnt(2)\n"
"v_mfma_f32_16x16x32_bf16 v[96:99], v[128:131], a[0:3], v[96:99]\n"
"ds_read_b64_tr_b16 v[116:117], %[vT11] offset:9216\n"
"v_mfma_f32_16x16x32_bf16 v[100:103], v[128:131], a[4:7], v[100:103]\n"
"ds_read_b64_tr_b16 v[118:119], %[vT11] offset:10240\n"
"s_waitcnt lgkmcnt(2)\n"
"v_mfma_f32_16x16x32_bf16 v[32:35], v[112:115], a[16:19], v[32:35]\n"
"ds_read_b64_tr_b16 v[120:121], %[vT21] offset:9216\n"
"v_mfma_f32_16x16x32_bf16 v[36:39], v[112:115], a[20:23], v[36:39]\n"
"ds_read_b64_tr_b16 v[122:123], %[vT21] offset:10240\n"
"v_mfma_f32_16x16x32_bf16 v[40:43], v[112:115], a[24:27], v[40:43]\n"
"s_add_u32 m0, %[m0A2], 0\n"
"s_add_u32 s62, %[soffRm], 16384\n"
"s_mov_b32 exec_hi, 0\n"
"buffer_load_dwordx4 %[vAh], %[srdRm], s62 offen lds\n"
"s_mov_b32 exec_hi, -1\n"
"v_mfma_f32_16x16x32_bf16 v[44:47], v[112:115], a[28:31], v[44:47]\n"
"s_add_u32 s60, %[soffB], 16384\n"
"s_add_u32 m0, %[m0B], 0\n"
"s_nop 0\n"
"buffer_load_dwordx4 %[vB0], %[srdB], s60 offen offset:0 sc1 lds\n"
"s_waitcnt lgkmcnt(2)\n"
"v_mfma_f32_16x16x32_bf16 v[48:51], v[116:119], a[16:19], v[48:51]\n"
"ds_read_b64_tr_b16 v[124:125], %[vT31] offset:9216\n"
"v_mfma_f32_16x16x32_bf16 v[52:55], v[116:119], a[20:23], v[52:55]\n"
"ds_read_b64_tr_b16 v[126:127], %[vT31] offset:10240\n"
"v_mfma_f32_16x16x32_bf16 v[56:59], v[116:119], a[24:27], v[56:59]\n"
"buffer_load_dwordx4 %[vB0], %[srdB], s60 offen offset:1024 sc1 lds\n"
"v_mfma_f32_16x16x32_bf16 v[60:63], v[116:119], a[28:31], v[60:63]\n"
"buffer_load_dwordx4 %[vB1], %[srdB], s60 offen offset:2048 sc1 lds\n"
"s_waitcnt lgkmcnt(2)\n"
"v_mfma_f32_16x16x32_bf16 v[64:67], v[120:123], a[16:19], v[64:67]\n"
"ds_read_b64_tr_b16 v[128:129], %[vS01] offset:9216\n"
"v_mfma_f32_16x16x32_bf16 v[68:71], v[120:123], a[20:23], v[68:71]\n"
"buffer_load_dwordx4 %[vB1], %[srdB], s60 offen offset:3072 sc1 lds\n"
"s_waitcnt vmcnt(9)\n"
"s_barrier\n"
"ds_read_b64_tr_b16 v[130:131], %[vS11] offset:9216\n"
"v_mfma_f32_16x16x32_bf16 v[72:75], v[120:123], a[24:27], v[72:75]\n"
"ds_read_b128 a[0:3], %[vTBa1] offset:34816\n"
"v_mfma_f32_16x16x32_bf16 v[76:79], v[120:123], a[28:31], v[76:79]\n"
"ds_read_b128 a[4:7], %[vTBa1] offset:36864\n"
"s_waitcnt lgkmcnt(4)\n"
"v_mfma_f32_16x16x32_bf16 v[80:83], v[124:127], a[16:19], v[80:83]\n"
"ds_read_b128 a[8:11], %[vTBb1] offset:38912\n"
"v_mfma_f32_16x16x32_bf16 v[84:87], v[124:127], a[20:23], v[84:87]\n"
"ds_read_b128 a[12:15], %[vTBb1] offset:40960\n"
"v_mfma_f32_16x16x32_bf16 v[88:91], v[124:127], a[24:27], v[88:91]\n"
"ds_read_b64_tr_b16 v[112:113], %[vT01] offset:34816\n"
"v_mfma_f32_16x16x32_bf16 v[92:95], v[124:127], a[28:31], v[92:95]\n"
"ds_read_b64_tr_b16 v[114:115], %[vT01] offset:35840\n"
"s_waitcnt lgkmcnt(6)\n"
"v_mfma_f32_16x16x32_bf16 v[96:99], v[128:131], a[16:19], v[96:99]\n"
"ds_read_b64_tr_b16 v[116:117], %[vT11] offset:34816\n"
"v_mfma_f32_16x16x32_bf16 v[100:103], v[128:131], a[20:23], v[100:103]\n"
"ds_read_b64_tr_b16 v[118:119], %[vT11] offset:35840\n"
"s_waitcnt lgkmcnt(2)\n"
"v_mfma_f32_16x16x32_bf16 v[32:35], v[112:115], a[0:3], v[32:35]\n"
"ds_read_b64_tr_b16 v[120:121], %[vT21] offset:34816\n"
"v_mfma_f32_16x16x32_bf16 v[36:39], v[112:115], a[4:7], v[36:39]\n"
"ds_read_b64_tr_b16 v[122:123], %[vT21] offset:35840\n"
"v_mfma_f32_16x16x32_bf16 v[40:43], v[112:115], a[8:11], v[40:43]\n"
"ds_read_b128 a[16:19], %[vTBa1] offset:35840\n"
"v_mfma_f32_16x16x32_bf16 v[44:47], v[112:115], a[12:15], v[44:47]\n"
"ds_read_b128 a[20:23], %[vTBa1] offset:37888\n"
"s_waitcnt lgkmcnt(4)\n"
"v_mfma_f32_16x16x32_bf16 v[48:51], v[116:119], a[0:3], v[48:51]\n"
"ds_read_b128 a[24:27], %[vTBb1] offset:39936\n"
"v_mfma_f32_16x16x32_bf16 v[52:55], v[116:119], a[4:7], v[52:55]\n"
"ds_read_b128 a[28:31], %[vTBb1] offset:41984\n"
"v_mfma_f32_16x16x32_bf16 v[56:59], v[116:119], a[8:11], v[56:59]\n"
"ds_read_b64_tr_b16 v[124:125], %[vT31] offset:34816\n"
"v_mfma_f32_16x16x32_bf16 v[60:63], v[116:119], a[12:15], v[60:63]\n"
"ds_read_b64_tr_b16 v[126:127], %[vT31] offset:35840\n"
"s_waitcnt lgkmcnt(6)\n"
"v_mfma_f32_16x16x32_bf16 v[64:67], v[120:123], a[0:3], v[64:67]\n"
"ds_read_b64_tr_b16 v[128:129], %[vS01] offset:34816\n"
"v_mfma_f32_16x16x32_bf16 v[68:71], v[120:123], a[4:7], v[68:71]\n"
"ds_read_b64_tr_b16 v[130:131], %[vS11] offset:34816\n"
"v_mfma_f32_16x16x32_bf16 v[72:75], v[120:123], a[8:11], v[72:75]\n"
"s_add_u32 s61, %[soffA], 737280\n"
"s_add_u32 m0, %[m0A], 34816\n"
"s_nop 0\n"
"buffer_load_dwordx4 %[vA0], %[srdA], s61 offen offset:0 lds\n"
"v_mfma_f32_16x16x32_bf16 v[76:79], v[120:123], a[12:15], v[76:79]\n"
"buffer_load_dwordx4 %[vA1], %[srdA], s61 offen offset:1024 lds\n"
"s_waitcnt lgkmcnt(2)\n"
"v_mfma_f32_16x16x32_bf16 v[80:83], v[124:127], a[0:3], v[80:83]\n"
"ds_read_b64_tr_b16 v[112:113], %[vT01] offset:44032\n"
"v_mfma_f32_16x16x32_bf16 v[84:87], v[124:127], a[4:7], v[84:87]\n"
"ds_read_b64_tr_b16 v[114:115], %[vT01] offset:45056\n"
"v_mfma_f32_16x16x32_bf16 v[88:91], v[124:127], a[8:11], v[88:91]\n"
"buffer_load_dwordx4 %[vA2], %[srdA], s61 offen offset:2048 lds\n"
"v_mfma_f32_16x16x32_bf16 v[92:95], v[124:127], a[12:15], v[92:95]\n"
"buffer_load_dwordx4 %[vA3], %[srdA], s61 offen offset:3072 lds\n"
"s_waitcnt lgkmcnt(2)\n"
"v_mfma_f32_16x16x32_bf16 v[96:99], v[128:131], a[0:3], v[96:99]\n"
"ds_read_b64_tr_b16 v[116:117], %[vT11] offset:44032\n"
"v_mfma_f32_16x16x32_bf16 v[100:103], v[128:131], a[4:7], v[100:103]\n"
"ds_read_b64_tr_b16 v[118:119], %[vT11] offset:45056\n"
"s_waitcnt lgkmcnt(2)\n"
"v_mfma_f32_16x16x32_bf16 v[32:35], v[112:115], a[16:19], v[32:35]\n"
"ds_read_b64_tr_b16 v[120:121], %[vT21] offset:44032\n"
"v_mfma_f32_16x16x32_bf16 v[36:39], v[112:115], a[20:23], v[36:39]\n"
"ds_read_b64_tr_b16 v[122:123], %[vT21] offset:45056\n"
"v_mfma_f32_16x16x32_bf16 v[40:43], v[112:115], a[24:27], v[40:43]\n"
"s_add_u32 m0, %[m0A2], 34816\n"
"s_add_u32 s62, %[soffRm], 20480\n"
"s_mov_b32 exec_hi, 0\n"
"buffer_load_dwordx4 %[vAh], %[srdRm], s62 offen lds\n"
"s_mov_b32 exec_hi, -1\n"
"v_mfma_f32_16x16x32_bf16 v[44:47], v[112:115], a[28:31], v[44:47]\n"
"s_add_u32 s60, %[soffB], 20480\n"
"s_add_u32 m0, %[m0B], 34816\n"
"s_nop 0\n"
"buffer_load_dwordx4 %[vB0], %[srdB], s60 offen offset:0 sc1 lds\n"
"s_waitcnt lgkmcnt(2)\n"
"v_mfma_f32_16x16x32_bf16 v[48:51], v[116:119], a[16:19], v[48:51]\n"
"ds_read_b64_tr_b16 v[124:125], %[vT31] offset:44032\n"
"v_mfma_f32_16x16x32_bf16 v[52:55], v[116:119], a[20:23], v[52:55]\n"
"ds_read_b64_tr_b16 v[126:127], %[vT31] offset:45056\n"
"v_mfma_f32_16x16x32_bf16 v[56:59], v[116:119], a[24:27], v[56:59]\n"
"buffer_load_dwordx4 %[vB0], %[srdB], s60 offen offset:1024 sc1 lds\n"
"v_mfma_f32_16x16x32_bf16 v[60:63], v[116:119], a[28:31], v[60:63]\n"
"buffer_load_dwordx4 %[vB1], %[srdB], s60 offen offset:2048 sc1 lds\n"
"s_waitcnt lgkmcnt(2)\n"
"v_mfma_f32_16x16x32_bf16 v[64:67], v[120:123], a[16:19], v[64:67]\n"
"ds_read_b64_tr_b16 v[128:129], %[vS01] offset:44032\n"
"v_mfma_f32_16x16x32_bf16 v[68:71], v[120:123], a[20:23], v[68:71]\n"
"buffer_load_dwordx4 %[vB1], %[srdB], s60 offen offset:3072 sc1 lds\n"
"s_waitcnt vmcnt(9)\n"
"s_barrier\n"
"ds_read_b64_tr_b16 v[130:131], %[vS11] offset:44032\n"
"v_mfma_f32_16x16x32_bf16 v[72:75], v[120:123], a[24:27], v[72:75]\n"
"ds_read_b128 a[0:3], %[vTBa0] offset:0\n"
"v_mfma_f32_16x16x32_bf16 v[76:79], v[120:123], a[28:31], v[76:79]\n"
"ds_read_b128 a[4:7], %[vTBa0] offset:2048\n"
"s_waitcnt lgkmcnt(4)\n"
"v_mfma_f32_16x16x32_bf16 v[80:83], v[124:127], a[16:19], v[80:83]\n"
"ds_read_b128 a[8:11], %[vTBb0] offset:4096\n"
"v_mfma_f32_16x16x32_bf16 v[84:87], v[124:127], a[20:23], v[84:87]\n"
"ds_read_b128 a[12:15], %[vTBb0] offset:6144\n"
"v_mfma_f32_16x16x32_bf16 v[88:91], v[124:127], a[24:27], v[88:91]\n"
"ds_read_b64_tr_b16 v[112:113], %[vT00] offset:0\n"
"v_mfma_f32_16x16x32_bf16 v[92:95], v[124:127], a[28:31], v[92:95]\n"
"ds_read_b64_tr_b16 v[114:115], %[vT00] offset:1024\n"
"s_waitcnt lgkmcnt(6)\n"
"v_mfma_f32_16x16x32_bf16 v[96:99], v[128:131], a[16:19], v[96:99]\n"
"ds_read_b64_tr_b16 v[116:117], %[vT10] offset:0\n"
"v_mfma_f32_16x16x32_bf16 v[100:103], v[128:131], a[20:23], v[100:103]\n"
"ds_read_b64_tr_b16 v[118:119], %[vT10] offset:1024\n"
"s_waitcnt lgkmcnt(2)\n"
"v_mfma_f32_16x16x32_bf16 v[32:35], v[112:115], a[0:3], v[32:35]\n"
"ds_read_b64_tr_b16 v[120:121], %[vT20] offset:0\n"
"v_mfma_f32_16x16x32_bf16 v[36:39], v[112:115], a[4:7], v[36:39]\n"
"ds_read_b64_tr_b16 v[122:123], %[vT20] offset:1024\n"
"v_mfma_f32_16x16x32_bf16 v[40:43], v[112:115], a[8:11], v[40:43]\n"
"ds_read_b128 a[16:19], %[vTBa0] offset:1024\n"
"v_mfma_f32_16x16x32_bf16 v[44:47], v[112:115], a[12:15], v[44:47]\n"
"ds_read_b128 a[20:23], %[vTBa0] offset:3072\n"
"s_waitcnt lgkmcnt(4)\n"
"v_mfma_f32_16x16x32_bf16 v[48:51], v[116:119], a[0:3], v[48:51]\n"
"ds_read_b128 a[24:27], %[vTBb0] offset:5120\n"
"v_mfma_f32_16x16x32_bf16 v[52:55], v[116:119], a[4:7], v[52:55]\n"
"ds_read_b128 a[28:31], %[vTBb0] offset:7168\n"
"v_mfma_f32_16x16x32_bf16 v[56:59], v[116:119], a[8:11], v[56:59]\n"
"ds_read_b64_tr_b16 v[124:125], %[vT30] offset:0\n"
"v_mfma_f32_16x16x32_bf16 v[60:63], v[116:119], a[12:15], v[60:63]\n"
"ds_read_b64_tr_b16 v[126:127], %[vT30] offset:1024\n"
"s_waitcnt lgkmcnt(6)\n"
"v_mfma_f32_16x16x32_bf16 v[64:67], v[120:123], a[0:3], v[64:67]\n"
"ds_read_b64_tr_b16 v[128:129], %[vS00] offset:0\n"
"v_mfma_f32_16x16x32_bf16 v[68:71], v[120:123], a[4:7], v[68:71]\n"
"ds_read_b64_tr_b16 v[130:131], %[vS10] offset:0\n"
"v_mfma_f32_16x16x32_bf16 v[72:75], v[120:123], a[8:11], v[72:75]\n"
"s_add_u32 s61, %[soffA], 884736\n"
"s_add_u32 m0, %[m0A], 69632\n"
"s_nop 0\n"
"buffer_load_dwordx4 %[vA0], %[srdA], s61 offen offset:0 lds\n"
"v_mfma_f32_16x16x32_bf16 v[76:79], v[120:123], a[12:15], v[76:79]\n"
"buffer_load_dwordx4 %[vA1], %[srdA], s61 offen offset:1024 lds\n"
"s_waitcnt lgkmcnt(2)\n"
"v_mfma_f32_16x16x32_bf16 v[80:83], v[124:127], a[0:3], v[80:83]\n"
"ds_read_b64_tr_b16 v[112:113], %[vT00] offset:9216\n"
"v_mfma_f32_16x16x32_bf16 v[84:87], v[124:127], a[4:7], v[84:87]\n"
"ds_read_b64_tr_b16 v[114:115], %[vT00] offset:10240\n"
"v_mfma_f32_16x16x32_bf16 v[88:91], v[124:127], a[8:11], v[88:91]\n"
"buffer_load_dwordx4 %[vA2], %[srdA], s61 offen offset:2048 lds\n"
"v_mfma_f32_16x16x32_bf16 v[92:95], v[124:127], a[12:15], v[92:95]\n"
"buffer_load_dwordx4 %[vA3], %[srdA], s61 offen offset:3072 lds\n"
"s_waitcnt lgkmcnt(2)\n"
"v_mfma_f32_16x16x32_bf16 v[96:99], v[128:131], a[0:3], v[96:99]\n"
"ds_read_b64_tr_b16 v[116:117], %[vT10] offset:9216\n"
"v_mfma_f32_16x16x32_bf16 v[100:103], v[128:131], a[4:7], v[100:103]\n"
"ds_read_b64_tr_b16 v[118:119], %[vT10] offset:10240\n"
"s_waitcnt lgkmcnt(2)\n"
"v_mfma_f32_16x16x32_bf16 v[32:35], v[112:115], a[16:19], v[32:35]\n"
"ds_read_b64_tr_b16 v[120:121], %[vT20] offset:9216\n"
"v_mfma_f32_16x16x32_bf16 v[36:39], v[112:115], a[20:23], v[36:39]\n"
"ds_read_b64_tr_b16 v[122:123], %[vT20] offset:10240\n"
"v_mfma_f32_16x16x32_bf16 v[40:43], v[112:115], a[24:27], v[40:43]\n"
"s_add_u32 m0, %[m0A2], 69632\n"
"s_add_u32 s62, %[soffRm], 24576\n"
"s_mov_b32 exec_hi, 0\n"
"buffer_load_dwordx4 %[vAh], %[srdRm], s62 offen lds\n"
"s_mov_b32 exec_hi, -1\n"
"v_mfma_f32_16x16x32_bf16 v[44:47], v[112:115], a[28:31], v[44:47]\n"
"s_add_u32 s60, %[soffB], 24576\n"
"s_add_u32 m0, %[m0B], 69632\n"
"s_nop 0\n"
"buffer_load_dwordx4 %[vB0], %[srdB], s60 offen offset:0 sc1 lds\n"
"s_waitcnt lgkmcnt(2)\n"
"v_mfma_f32_16x16x32_bf16 v[48:51], v[116:119], a[16:19], v[48:51]\n"
"ds_read_b64_tr_b16 v[124:125], %[vT30] offset:9216\n"
"v_mfma_f32_16x16x32_bf16 v[52:55], v[116:119], a[20:23], v[52:55]\n"
"ds_read_b64_tr_b16 v[126:127], %[vT30] offset:10240\n"
"v_mfma_f32_16x16x32_bf16 v[56:59], v[116:119], a[24:27], v[56:59]\n"
"buffer_load_dwordx4 %[vB0], %[srdB], s60 offen offset:1024 sc1 lds\n"
"v_mfma_f32_16x16x32_bf16 v[60:63], v[116:119], a[28:31], v[60:63]\n"
"buffer_load_dwordx4 %[vB1], %[srdB], s60 offen offset:2048 sc1 lds\n"
"s_waitcnt lgkmcnt(2)\n"
"v_mfma_f32_16x16x32_bf16 v[64:67], v[120:123], a[16:19], v[64:67]\n"
"ds_read_b64_tr_b16 v[128:129], %[vS00] offset:9216\n"
"v_mfma_f32_16x16x32_bf16 v[68:71], v[120:123], a[20:23], v[68:71]\n"
"buffer_load_dwordx4 %[vB1], %[srdB], s60 offen offset:3072 sc1 lds\n"
"s_waitcnt vmcnt(9)\n"
"s_barrier\n"
"ds_read_b64_tr_b16 v[130:131], %[vS10] offset:9216\n"
"v_mfma_f32_16x16x32_bf16 v[72:75], v[120:123], a[24:27], v[72:75]\n"
"ds_read_b128 a[0:3], %[vTBa0] offset:34816\n"
"v_mfma_f32_16x16x32_bf16 v[76:79], v[120:123], a[28:31], v[76:79]\n"
"ds_read_b128 a[4:7], %[vTBa0] offset:36864\n"
"s_waitcnt lgkmcnt(4)\n"
"v_mfma_f32_16x16x32_bf16 v[80:83], v[124:127], a[16:19], v[80:83]\n"
"ds_read_b128 a[8:11], %[vTBb0] offset:38912\n"
"v_mfma_f32_16x16x32_bf16 v[84:87], v[124:127], a[20:23], v[84:87]\n"
"ds_read_b128 a[12:15], %[vTBb0] offset:40960\n"
"v_mfma_f32_16x16x32_bf16 v[88:91], v[124:127], a[24:27], v[88:91]\n"
"ds_read_b64_tr_b16 v[112:113], %[vT00] offset:34816\n"
"v_mfma_f32_16x16x32_bf16 v[92:95], v[124:127], a[28:31], v[92:95]\n"
"ds_read_b64_tr_b16 v[114:115], %[vT00] offset:35840\n"
"s_waitcnt lgkmcnt(6)\n"
"v_mfma_f32_16x16x32_bf16 v[96:99], v[128:131], a[16:19], v[96:99]\n"
"ds_read_b64_tr_b16 v[116:117], %[vT10] offset:34816\n"
"v_mfma_f32_16x16x32_bf16 v[100:103], v[128:131], a[20:23], v[100:103]\n"
"ds_read_b64_tr_b16 v[118:119], %[vT10] offset:35840\n"
"s_waitcnt lgkmcnt(2)\n"
"v_mfma_f32_16x16x32_bf16 v[32:35], v[112:115], a[0:3], v[32:35]\n"
"ds_read_b64_tr_b16 v[120:121], %[vT20] offset:34816\n"
"v_mfma_f32_16x16x32_bf16 v[36:39], v[112:115], a[4:7], v[36:39]\n"
"ds_read_b64_tr_b16 v[122:123], %[vT20] offset:35840\n"
"v_mfma_f32_16x16x32_bf16 v[40:43], v[112:115], a[8:11], v[40:43]\n"
"ds_read_b128 a[16:19], %[vTBa0] offset:35840\n"
"v_mfma_f32_16x16x32_bf16 v[44:47], v[112:115], a[12:15], v[44:47]\n"
"ds_read_b128 a[20:23], %[vTBa0] offset:37888\n"
"s_waitcnt lgkmcnt(4)\n"
"v_mfma_f32_16x16x32_bf16 v[48:51], v[116:119], a[0:3], v[48:51]\n"
"ds_read_b128 a[24:27], %[vTBb0] offset:39936\n"
"v_mfma_f32_16x16x32_bf16 v[52:55], v[116:119], a[4:7], v[52:55]\n"
"ds_read_b128 a[28:31], %[vTBb0] offset:41984\n"
"v_mfma_f32_16x16x32_bf16 v[56:59], v[116:119], a[8:11], v[56:59]\n"
"ds_read_b64_tr_b16 v[124:125], %[vT30] offset:34816\n"
"v_mfma_f32_16x16x32_bf16 v[60:63], v[116:119], a[12:15], v[60:63]\n"
"ds_read_b64_tr_b16 v[126:127], %[vT30] offset:35840\n"
"s_waitcnt lgkmcnt(6)\n"
"v_mfma_f32_16x16x32_bf16 v[64:67], v[120:123], a[0:3], v[64:67]\n"
"ds_read_b64_tr_b16 v[128:129], %[vS00] offset:34816\n"
"v_mfma_f32_16x16x32_bf16 v[68:71], v[120:123], a[4:7], v[68:71]\n"
"ds_read_b64_tr_b16 v[130:131], %[vS10] offset:34816\n"
"v_mfma_f32_16x16x32_bf16 v[72:75], v[120:123], a[8:11], v[72:75]\n"
"buffer_load_dword v141, %[vFBp], %[srdF4], 0 offen sc1\n"
"v_mfma_f32_16x16x32_bf16 v[76:79], v[120:123], a[12:15], v[76:79]\n"
"s_add_u32 s61, %[soffA], 1032192\n"
"s_add_u32 m0, %[m0A], 104448\n"
"s_nop 0\n"
"buffer_load_dwordx4 %[vA0], %[srdA], s61 offen offset:0 lds\n"
"s_waitcnt lgkmcnt(2)\n"
"v_mfma_f32_16x16x32_bf16 v[80:83], v[124:127], a[0:3], v[80:83]\n"
"ds_read_b64_tr_b16 v[112:113], %[vT00] offset:44032\n"
"v_mfma_f32_16x16x32_bf16 v[84:87], v[124:127], a[4:7], v[84:87]\n"
"ds_read_b64_tr_b16 v[114:115], %[vT00] offset:45056\n"
"v_mfma_f32_16x16x32_bf16 v[88:91], v[124:127], a[8:11], v[88:91]\n"
"buffer_load_dwordx4 %[vA1], %[srdA], s61 offen offset:1024 lds\n"
"v_mfma_f32_16x16x32_bf16 v[92:95], v[124:127], a[12:15], v[92:95]\n"
"buffer_load_dwordx4 %[vA2], %[srdA], s61 offen offset:2048 lds\n"
"s_waitcnt lgkmcnt(2)\n"
"v_mfma_f32_16x16x32_bf16 v[96:99], v[128:131], a[0:3], v[96:99]\n"
"ds_read_b64_tr_b16 v[116:117], %[vT10] offset:44032\n"
"v_mfma_f32_16x16x32_bf16 v[100:103], v[128:131], a[4:7], v[100:103]\n"
"ds_read_b64_tr_b16 v[118:119], %[vT10] offset:45056\n"
"s_waitcnt lgkmcnt(2)\n"
"v_mfma_f32_16x16x32_bf16 v[32:35], v[112:115], a[16:19], v[32:35]\n"
"ds_read_b64_tr_b16 v[120:121], %[vT20] offset:44032\n"
"v_mfma_f32_16x16x32_bf16 v[36:39], v[112:115], a[20:23], v[36:39]\n"
"ds_read_b64_tr_b16 v[122:123], %[vT20] offset:45056\n"
"v_mfma_f32_16x16x32_bf16 v[40:43], v[112:115], a[24:27], v[40:43]\n"
"buffer_load_dwordx4 %[vA3], %[srdA], s61 offen offset:3072 lds\n"
"v_mfma_f32_16x16x32_bf16 v[44:47], v[112:115], a[28:31], v[44:47]\n"
"s_add_u32 m0, %[m0A2], 104448\n"
"s_add_u32 s62, %[soffRm], 28672\n"
"s_mov_b32 exec_hi, 0\n"
"buffer_load_dwordx4 %[vAh], %[srdRm], s62 offen lds\n"
"s_mov_b32 exec_hi, -1\n"
"s_waitcnt lgkmcnt(2)\n"
"v_mfma_f32_16x16x32_bf16 v[48:51], v[116:119], a[16:19], v[48:51]\n"
"ds_read_b64_tr_b16 v[124:125], %[vT30] offset:44032\n"
"v_mfma_f32_16x16x32_bf16 v[52:55], v[116:119], a[20:23], v[52:55]\n"
"ds_read_b64_tr_b16 v[126:127], %[vT30] offset:45056\n"
"v_mfma_f32_16x16x32_bf16 v[56:59], v[116:119], a[24:27], v[56:59]\n"
"s_add_u32 s60, %[soffB], 28672\n"
"s_add_u32 m0, %[m0B], 104448\n"
"s_nop 0\n"
"buffer_load_dwordx4 %[vB0], %[srdB], s60 offen offset:0 sc1 lds\n"
"v_mfma_f32_16x16x32_bf16 v[60:63], v[116:119], a[28:31], v[60:63]\n"
"buffer_load_dwordx4 %[vB0], %[srdB], s60 offen offset:1024 sc1 lds\n"
"s_waitcnt lgkmcnt(2)\n"
"v_mfma_f32_16x16x32_bf16 v[64:67], v[120:123], a[16:19], v[64:67]\n"
"ds_read_b64_tr_b16 v[128:129], %[vS00] offset:44032\n"
"v_mfma_f32_16x16x32_bf16 v[68:71], v[120:123], a[20:23], v[68:71]\n"
"buffer_load_dwordx4 %[vB1], %[srdB], s60 offen offset:2048 sc1 lds\n"
"buffer_load_dwordx4 %[vB1], %[srdB], s60 offen offset:3072 sc1 lds\n"
"s_waitcnt vmcnt(9)\n"
"v_cmp_ne_u32 vcc, %[vFBp], v141\n"
"s_cbranch_vccz .Lfb_ok_%=\n"
"s_movk_i32 s63, 0x2000\n"
".Lfb_poll_%=:\n"
"buffer_load_dword v141, %[vFBp], %[srdF4], 0 offen sc1\n"
"s_waitcnt vmcnt(0)\n"
"v_cmp_ne_u32 vcc, %[vFBp], v141\n"
"s_cbranch_vccz .Lfb_ok_%=\n"
"s_sub_u32 s63, s63, 1\n"
"s_cbranch_scc0 .Lfb_poll_%=\n"
".Lfb_ok_%=:\n"
"s_barrier\n"
"ds_read_b64_tr_b16 v[130:131], %[vS10] offset:44032\n"
"v_mfma_f32_16x16x32_bf16 v[72:75], v[120:123], a[24:27], v[72:75]\n"
"ds_read_b128 a[0:3], %[vTBa1] offset:0\n"
"v_mfma_f32_16x16x32_bf16 v[76:79], v[120:123], a[28:31], v[76:79]\n"
"ds_read_b128 a[4:7], %[vTBa1] offset:2048\n"
"s_waitcnt lgkmcnt(4)\n"
"v_mfma_f32_16x16x32_bf16 v[80:83], v[124:127], a[16:19], v[80:83]\n"
"ds_read_b128 a[8:11], %[vTBb1] offset:4096\n"
"v_mfma_f32_16x16x32_bf16 v[84:87], v[124:127], a[20:23], v[84:87]\n"
"ds_read_b128 a[12:15], %[vTBb1] offset:6144\n"
"v_mfma_f32_16x16x32_bf16 v[88:91], v[124:127], a[24:27], v[88:91]\n"
"ds_read_b64_tr_b16 v[112:113], %[vT01] offset:0\n"
"v_mfma_f32_16x16x32_bf16 v[92:95], v[124:127], a[28:31], v[92:95]\n"
"ds_read_b64_tr_b16 v[114:115], %[vT01] offset:1024\n"
"s_waitcnt lgkmcnt(6)\n"
"v_mfma_f32_16x16x32_bf16 v[96:99], v[128:131], a[16:19], v[96:99]\n"
"ds_read_b64_tr_b16 v[116:117], %[vT11] offset:0\n"
"v_mfma_f32_16x16x32_bf16 v[100:103], v[128:131], a[20:23], v[100:103]\n"
"ds_read_b64_tr_b16 v[118:119], %[vT11] offset:1024\n"
"s_waitcnt lgkmcnt(2)\n"
"v_mfma_f32_16x16x32_bf16 v[32:35], v[112:115], a[0:3], v[32:35]\n"
"ds_read_b64_tr_b16 v[120:121], %[vT21] offset:0\n"
"v_mfma_f32_16x16x32_bf16 v[36:39], v[112:115], a[4:7], v[36:39]\n"
"ds_read_b64_tr_b16 v[122:123], %[vT21] offset:1024\n"
"v_mfma_f32_16x16x32_bf16 v[40:43], v[112:115], a[8:11], v[40:43]\n"
"ds_read_b128 a[16:19], %[vTBa1] offset:1024\n"
"v_mfma_f32_16x16x32_bf16 v[44:47], v[112:115], a[12:15], v[44:47]\n"
"ds_read_b128 a[20:23], %[vTBa1] offset:3072\n"
"s_waitcnt lgkmcnt(4)\n"
"v_mfma_f32_16x16x32_bf16 v[48:51], v[116:119], a[0:3], v[48:51]\n"
"ds_read_b128 a[24:27], %[vTBb1] offset:5120\n"
"v_mfma_f32_16x16x32_bf16 v[52:55], v[116:119], a[4:7], v[52:55]\n"
"ds_read_b128 a[28:31], %[vTBb1] offset:7168\n"
"v_mfma_f32_16x16x32_bf16 v[56:59], v[116:119], a[8:11], v[56:59]\n"
"ds_read_b64_tr_b16 v[124:125], %[vT31] offset:0\n"
"v_mfma_f32_16x16x32_bf16 v[60:63], v[116:119], a[12:15], v[60:63]\n"
"ds_read_b64_tr_b16 v[126:127], %[vT31] offset:1024\n"
"s_waitcnt lgkmcnt(6)\n"
"v_mfma_f32_16x16x32_bf16 v[64:67], v[120:123], a[0:3], v[64:67]\n"
"ds_read_b64_tr_b16 v[128:129], %[vS01] offset:0\n"
"v_mfma_f32_16x16x32_bf16 v[68:71], v[120:123], a[4:7], v[68:71]\n"
"ds_read_b64_tr_b16 v[130:131], %[vS11] offset:0\n"
"v_mfma_f32_16x16x32_bf16 v[72:75], v[120:123], a[8:11], v[72:75]\n"
"s_add_u32 s61, %[soffA], 73728\n"
"s_add_u32 m0, %[m0A], 0\n"
"s_nop 0\n"
"buffer_load_dwordx4 %[vA0], %[srdA], s61 offen offset:0 lds\n"
"v_mfma_f32_16x16x32_bf16 v[76:79], v[120:123], a[12:15], v[76:79]\n"
"buffer_load_dwordx4 %[vA1], %[srdA], s61 offen offset:1024 lds\n"
"s_waitcnt lgkmcnt(2)\n"
"v_mfma_f32_16x16x32_bf16 v[80:83], v[124:127], a[0:3], v[80:83]\n"
"ds_read_b64_tr_b16 v[112:113], %[vT01] offset:9216\n"
"v_mfma_f32_16x16x32_bf16 v[84:87], v[124:127], a[4:7], v[84:87]\n"
"ds_read_b64_tr_b16 v[114:115], %[vT01] offset:10240\n"
"v_mfma_f32_16x16x32_bf16 v[88:91], v[124:127], a[8:11], v[88:91]\n"
"buffer_load_dwordx4 %[vA2], %[srdA], s61 offen offset:2048 lds\n"
"v_mfma_f32_16x16x32_bf16 v[92:95], v[124:127], a[12:15], v[92:95]\n"
"buffer_load_dwordx4 %[vA3], %[srdA], s61 offen offset:3072 lds\n"
"s_waitcnt lgkmcnt(2)\n"
"v_mfma_f32_16x16x32_bf16 v[96:99], v[128:131], a[0:3], v[96:99]\n"
"ds_read_b64_tr_b16 v[116:117], %[vT11] offset:9216\n"
"v_mfma_f32_16x16x32_bf16 v[100:103], v[128:131], a[4:7], v[100:103]\n"
"ds_read_b64_tr_b16 v[118:119], %[vT11] offset:10240\n"
"s_waitcnt lgkmcnt(2)\n"
"v_mfma_f32_16x16x32_bf16 v[32:35], v[112:115], a[16:19], v[32:35]\n"
"ds_read_b64_tr_b16 v[120:121], %[vT21] offset:9216\n"
"v_mfma_f32_16x16x32_bf16 v[36:39], v[112:115], a[20:23], v[36:39]\n"
"ds_read_b64_tr_b16 v[122:123], %[vT21] offset:10240\n"
"v_mfma_f32_16x16x32_bf16 v[40:43], v[112:115], a[24:27], v[40:43]\n"
"s_add_u32 m0, %[m0A2], 0\n"
"s_add_u32 s62, %[soffRm], 2048\n"
"s_mov_b32 exec_hi, 0\n"
"buffer_load_dwordx4 %[vAh], %[srdRm], s62 offen lds\n"
"s_mov_b32 exec_hi, -1\n"
"v_mfma_f32_16x16x32_bf16 v[44:47], v[112:115], a[28:31], v[44:47]\n"
"s_add_u32 s60, %[soffB], 2048\n"
"s_add_u32 m0, %[m0B], 0\n"
"s_nop 0\n"
"buffer_load_dwordx4 %[vB0], %[srdB], s60 offen offset:0 sc1 lds\n"
"s_waitcnt lgkmcnt(2)\n"
"v_mfma_f32_16x16x32_bf16 v[48:51], v[116:119], a[16:19], v[48:51]\n"
"ds_read_b64_tr_b16 v[124:125], %[vT31] offset:9216\n"
"v_mfma_f32_16x16x32_bf16 v[52:55], v[116:119], a[20:23], v[52:55]\n"
"ds_read_b64_tr_b16 v[126:127], %[vT31] offset:10240\n"
"v_mfma_f32_16x16x32_bf16 v[56:59], v[116:119], a[24:27], v[56:59]\n"
"buffer_load_dwordx4 %[vB0], %[srdB], s60 offen offset:1024 sc1 lds\n"
"v_mfma_f32_16x16x32_bf16 v[60:63], v[116:119], a[28:31], v[60:63]\n"
"buffer_load_dwordx4 %[vB1], %[srdB], s60 offen offset:2048 sc1 lds\n"
"s_waitcnt lgkmcnt(2)\n"
"v_mfma_f32_16x16x32_bf16 v[64:67], v[120:123], a[16:19], v[64:67]\n"
"ds_read_b64_tr_b16 v[128:129], %[vS01] offset:9216\n"
"v_mfma_f32_16x16x32_bf16 v[68:71], v[120:123], a[20:23], v[68:71]\n"
"buffer_load_dwordx4 %[vB1], %[srdB], s60 offen offset:3072 sc1 lds\n"
"s_waitcnt vmcnt(9)\n"
"s_barrier\n"
"ds_read_b64_tr_b16 v[130:131], %[vS11] offset:9216\n"
"v_mfma_f32_16x16x32_bf16 v[72:75], v[120:123], a[24:27], v[72:75]\n"
"ds_read_b128 a[0:3], %[vTBa1] offset:34816\n"
"v_mfma_f32_16x16x32_bf16 v[76:79], v[120:123], a[28:31], v[76:79]\n"
"ds_read_b128 a[4:7], %[vTBa1] offset:36864\n"
"s_waitcnt lgkmcnt(4)\n"
"v_mfma_f32_16x16x32_bf16 v[80:83], v[124:127], a[16:19], v[80:83]\n"
"ds_read_b128 a[8:11], %[vTBb1] offset:38912\n"
"v_mfma_f32_16x16x32_bf16 v[84:87], v[124:127], a[20:23], v[84:87]\n"
"ds_read_b128 a[12:15], %[vTBb1] offset:40960\n"
"v_mfma_f32_16x16x32_bf16 v[88:91], v[124:127], a[24:27], v[88:91]\n"
"ds_read_b64_tr_b16 v[112:113], %[vT01] offset:34816\n"
"v_mfma_f32_16x16x32_bf16 v[92:95], v[124:127], a[28:31], v[92:95]\n"
"ds_read_b64_tr_b16 v[114:115], %[vT01] offset:35840\n"
"s_waitcnt lgkmcnt(6)\n"
"v_mfma_f32_16x16x32_bf16 v[96:99], v[128:131], a[16:19], v[96:99]\n"
"ds_read_b64_tr_b16 v[116:117], %[vT11] offset:34816\n"
"v_mfma_f32_16x16x32_bf16 v[100:103], v[128:131], a[20:23], v[100:103]\n"
"ds_read_b64_tr_b16 v[118:119], %[vT11] offset:35840\n"
"s_waitcnt lgkmcnt(2)\n"
"v_mfma_f32_16x16x32_bf16 v[32:35], v[112:115], a[0:3], v[32:35]\n"
"ds_read_b64_tr_b16 v[120:121], %[vT21] offset:34816\n"
"v_mfma_f32_16x16x32_bf16 v[36:39], v[112:115], a[4:7], v[36:39]\n"
"ds_read_b64_tr_b16 v[122:123], %[vT21] offset:35840\n"
"v_mfma_f32_16x16x32_bf16 v[40:43], v[112:115], a[8:11], v[40:43]\n"
"ds_read_b128 a[16:19], %[vTBa1] offset:35840\n"
"v_mfma_f32_16x16x32_bf16 v[44:47], v[112:115], a[12:15], v[44:47]\n"
"ds_read_b128 a[20:23], %[vTBa1] offset:37888\n"
"s_waitcnt lgkmcnt(4)\n"
"v_mfma_f32_16x16x32_bf16 v[48:51], v[116:119], a[0:3], v[48:51]\n"
"ds_read_b128 a[24:27], %[vTBb1] offset:39936\n"
"v_mfma_f32_16x16x32_bf16 v[52:55], v[116:119], a[4:7], v[52:55]\n"
"ds_read_b128 a[28:31], %[vTBb1] offset:41984\n"
"v_mfma_f32_16x16x32_bf16 v[56:59], v[116:119], a[8:11], v[56:59]\n"
"ds_read_b64_tr_b16 v[124:125], %[vT31] offset:34816\n"
"v_mfma_f32_16x16x32_bf16 v[60:63], v[116:119], a[12:15], v[60:63]\n"
"ds_read_b64_tr_b16 v[126:127], %[vT31] offset:35840\n"
"s_waitcnt lgkmcnt(6)\n"
"v_mfma_f32_16x16x32_bf16 v[64:67], v[120:123], a[0:3], v[64:67]\n"
"ds_read_b64_tr_b16 v[128:129], %[vS01] offset:34816\n"
"v_mfma_f32_16x16x32_bf16 v[68:71], v[120:123], a[4:7], v[68:71]\n"
"ds_read_b64_tr_b16 v[130:131], %[vS11] offset:34816\n"
"v_mfma_f32_16x16x32_bf16 v[72:75], v[120:123], a[8:11], v[72:75]\n"
"s_add_u32 s61, %[soffA], 221184\n"
"s_add_u32 m0, %[m0A], 34816\n"
"s_nop 0\n"
"buffer_load_dwordx4 %[vA0], %[srdA], s61 offen offset:0 lds\n"
"v_mfma_f32_16x16x32_bf16 v[76:79], v[120:123], a[12:15], v[76:79]\n"
"buffer_load_dwordx4 %[vA1], %[srdA], s61 offen offset:1024 lds\n"
"s_waitcnt lgkmcnt(2)\n"
"v_mfma_f32_16x16x32_bf16 v[80:83], v[124:127], a[0:3], v[80:83]\n"
"ds_read_b64_tr_b16 v[112:113], %[vT01] offset:44032\n"
"v_mfma_f32_16x16x32_bf16 v[84:87], v[124:127], a[4:7], v[84:87]\n"
"ds_read_b64_tr_b16 v[114:115], %[vT01] offset:45056\n"
"v_mfma_f32_16x16x32_bf16 v[88:91], v[124:127], a[8:11], v[88:91]\n"
"buffer_load_dwordx4 %[vA2], %[srdA], s61 offen offset:2048 lds\n"
"v_mfma_f32_16x16x32_bf16 v[92:95], v[124:127], a[12:15], v[92:95]\n"
"buffer_load_dwordx4 %[vA3], %[srdA], s61 offen offset:3072 lds\n"
"s_waitcnt lgkmcnt(2)\n"
"v_mfma_f32_16x16x32_bf16 v[96:99], v[128:131], a[0:3], v[96:99]\n"
"ds_read_b64_tr_b16 v[116:117], %[vT11] offset:44032\n"
"v_mfma_f32_16x16x32_bf16 v[100:103], v[128:131], a[4:7], v[100:103]\n"
"ds_read_b64_tr_b16 v[118:119], %[vT11] offset:45056\n"
"s_waitcnt lgkmcnt(2)\n"
"v_mfma_f32_16x16x32_bf16 v[32:35], v[112:115], a[16:19], v[32:35]\n"
"ds_read_b64_tr_b16 v[120:121], %[vT21] offset:44032\n"
"v_mfma_f32_16x16x32_bf16 v[36:39], v[112:115], a[20:23], v[36:39]\n"
"ds_read_b64_tr_b16 v[122:123], %[vT21] offset:45056\n"
"v_mfma_f32_16x16x32_bf16 v[40:43], v[112:115], a[24:27], v[40:43]\n"
"s_add_u32 m0, %[m0A2], 34816\n"
"s_add_u32 s62, %[soffRm], 6144\n"
"s_mov_b32 exec_hi, 0\n"
"buffer_load_dwordx4 %[vAh], %[srdRm], s62 offen lds\n"
"s_mov_b32 exec_hi, -1\n"
"v_mfma_f32_16x16x32_bf16 v[44:47], v[112:115], a[28:31], v[44:47]\n"
"s_add_u32 s60, %[soffB], 6144\n"
"s_add_u32 m0, %[m0B], 34816\n"
"s_nop 0\n"
"buffer_load_dwordx4 %[vB0], %[srdB], s60 offen offset:0 sc1 lds\n"
"s_waitcnt lgkmcnt(2)\n"
"v_mfma_f32_16x16x32_bf16 v[48:51], v[116:119], a[16:19], v[48:51]\n"
"ds_read_b64_tr_b16 v[124:125], %[vT31] offset:44032\n"
"v_mfma_f32_16x16x32_bf16 v[52:55], v[116:119], a[20:23], v[52:55]\n"
"ds_read_b64_tr_b16 v[126:127], %[vT31] offset:45056\n"
"v_mfma_f32_16x16x32_bf16 v[56:59], v[116:119], a[24:27], v[56:59]\n"
"buffer_load_dwordx4 %[vB0], %[srdB], s60 offen offset:1024 sc1 lds\n"
"v_mfma_f32_16x16x32_bf16 v[60:63], v[116:119], a[28:31], v[60:63]\n"
"buffer_load_dwordx4 %[vB1], %[srdB], s60 offen offset:2048 sc1 lds\n"
"s_waitcnt lgkmcnt(2)\n"
"v_mfma_f32_16x16x32_bf16 v[64:67], v[120:123], a[16:19], v[64:67]\n"
"ds_read_b64_tr_b16 v[128:129], %[vS01] offset:44032\n"
"v_mfma_f32_16x16x32_bf16 v[68:71], v[120:123], a[20:23], v[68:71]\n"
"buffer_load_dwordx4 %[vB1], %[srdB], s60 offen offset:3072 sc1 lds\n"
"s_waitcnt vmcnt(9)\n"
"s_barrier\n"
"ds_read_b64_tr_b16 v[130:131], %[vS11] offset:44032\n"
"v_mfma_f32_16x16x32_bf16 v[72:75], v[120:123], a[24:27], v[72:75]\n"
"ds_read_b128 a[0:3], %[vTBa0] offset:0\n"
"v_mfma_f32_16x16x32_bf16 v[76:79], v[120:123], a[28:31], v[76:79]\n"
"ds_read_b128 a[4:7], %[vTBa0] offset:2048\n"
"s_waitcnt lgkmcnt(4)\n"
"v_mfma_f32_16x16x32_bf16 v[80:83], v[124:127], a[16:19], v[80:83]\n"
"ds_read_b128 a[8:11], %[vTBb0] offset:4096\n"
"v_mfma_f32_16x16x32_bf16 v[84:87], v[124:127], a[20:23], v[84:87]\n"
"ds_read_b128 a[12:15], %[vTBb0] offset:6144\n"
"v_mfma_f32_16x16x32_bf16 v[88:91], v[124:127], a[24:27], v[88:91]\n"
"ds_read_b64_tr_b16 v[112:113], %[vT00] offset:0\n"
"v_mfma_f32_16x16x32_bf16 v[92:95], v[124:127], a[28:31], v[92:95]\n"
"ds_read_b64_tr_b16 v[114:115], %[vT00] offset:1024\n"
"s_waitcnt lgkmcnt(6)\n"
"v_mfma_f32_16x16x32_bf16 v[96:99], v[128:131], a[16:19], v[96:99]\n"
"ds_read_b64_tr_b16 v[116:117], %[vT10] offset:0\n"
"v_mfma_f32_16x16x32_bf16 v[100:103], v[128:131], a[20:23], v[100:103]\n"
"ds_read_b64_tr_b16 v[118:119], %[vT10] offset:1024\n"
"s_waitcnt lgkmcnt(2)\n"
"v_mfma_f32_16x16x32_bf16 v[32:35], v[112:115], a[0:3], v[32:35]\n"
"ds_read_b64_tr_b16 v[120:121], %[vT20] offset:0\n"
"v_mfma_f32_16x16x32_bf16 v[36:39], v[112:115], a[4:7], v[36:39]\n"
"ds_read_b64_tr_b16 v[122:123], %[vT20] offset:1024\n"
"v_mfma_f32_16x16x32_bf16 v[40:43], v[112:115], a[8:11], v[40:43]\n"
"ds_read_b128 a[16:19], %[vTBa0] offset:1024\n"
"v_mfma_f32_16x16x32_bf16 v[44:47], v[112:115], a[12:15], v[44:47]\n"
"ds_read_b128 a[20:23], %[vTBa0] offset:3072\n"
"s_waitcnt lgkmcnt(4)\n"
"v_mfma_f32_16x16x32_bf16 v[48:51], v[116:119], a[0:3], v[48:51]\n"
"ds_read_b128 a[24:27], %[vTBb0] offset:5120\n"
"v_mfma_f32_16x16x32_bf16 v[52:55], v[116:119], a[4:7], v[52:55]\n"
"ds_read_b128 a[28:31], %[vTBb0] offset:7168\n"
"v_mfma_f32_16x16x32_bf16 v[56:59], v[116:119], a[8:11], v[56:59]\n"
"ds_read_b64_tr_b16 v[124:125], %[vT30] offset:0\n"
"v_mfma_f32_16x16x32_bf16 v[60:63], v[116:119], a[12:15], v[60:63]\n"
"ds_read_b64_tr_b16 v[126:127], %[vT30] offset:1024\n"
"s_waitcnt lgkmcnt(6)\n"
"v_mfma_f32_16x16x32_bf16 v[64:67], v[120:123], a[0:3], v[64:67]\n"
"ds_read_b64_tr_b16 v[128:129], %[vS00] offset:0\n"
"v_mfma_f32_16x16x32_bf16 v[68:71], v[120:123], a[4:7], v[68:71]\n"
"ds_read_b64_tr_b16 v[130:131], %[vS10] offset:0\n"
"v_mfma_f32_16x16x32_bf16 v[72:75], v[120:123], a[8:11], v[72:75]\n"
"s_add_u32 s61, %[soffA], 368640\n"
"s_add_u32 m0, %[m0A], 69632\n"
"s_nop 0\n"
"buffer_load_dwordx4 %[vA0], %[srdA], s61 offen offset:0 lds\n"
"v_mfma_f32_16x16x32_bf16 v[76:79], v[120:123], a[12:15], v[76:79]\n"
"buffer_load_dwordx4 %[vA1], %[srdA], s61 offen offset:1024 lds\n"
"s_waitcnt lgkmcnt(2)\n"
"v_mfma_f32_16x16x32_bf16 v[80:83], v[124:127], a[0:3], v[80:83]\n"
"ds_read_b64_tr_b16 v[112:113], %[vT00] offset:9216\n"
"v_mfma_f32_16x16x32_bf16 v[84:87], v[124:127], a[4:7], v[84:87]\n"
"ds_read_b64_tr_b16 v[114:115], %[vT00] offset:10240\n"
"v_mfma_f32_16x16x32_bf16 v[88:91], v[124:127], a[8:11], v[88:91]\n"
"buffer_load_dwordx4 %[vA2], %[srdA], s61 offen offset:2048 lds\n"
"v_mfma_f32_16x16x32_bf16 v[92:95], v[124:127], a[12:15], v[92:95]\n"
"buffer_load_dwordx4 %[vA3], %[srdA], s61 offen offset:3072 lds\n"
"s_waitcnt lgkmcnt(2)\n"
"v_mfma_f32_16x16x32_bf16 v[96:99], v[128:131], a[0:3], v[96:99]\n"
"ds_read_b64_tr_b16 v[116:117], %[vT10] offset:9216\n"
"v_mfma_f32_16x16x32_bf16 v[100:103], v[128:131], a[4:7], v[100:103]\n"
"ds_read_b64_tr_b16 v[118:119], %[vT10] offset:10240\n"
"s_waitcnt lgkmcnt(2)\n"
"v_mfma_f32_16x16x32_bf16 v[32:35], v[112:115], a[16:19], v[32:35]\n"
"ds_read_b64_tr_b16 v[120:121], %[vT20] offset:9216\n"
"v_mfma_f32_16x16x32_bf16 v[36:39], v[112:115], a[20:23], v[36:39]\n"
"ds_read_b64_tr_b16 v[122:123], %[vT20] offset:10240\n"
"v_mfma_f32_16x16x32_bf16 v[40:43], v[112:115], a[24:27], v[40:43]\n"
"s_add_u32 m0, %[m0A2], 69632\n"
"s_add_u32 s62, %[soffRm], 10240\n"
"s_mov_b32 exec_hi, 0\n"
"buffer_load_dwordx4 %[vAh], %[srdRm], s62 offen lds\n"
"s_mov_b32 exec_hi, -1\n"
"v_mfma_f32_16x16x32_bf16 v[44:47], v[112:115], a[28:31], v[44:47]\n"
"s_add_u32 s60, %[soffB], 10240\n"
"s_add_u32 m0, %[m0B], 69632\n"
"s_nop 0\n"
"buffer_load_dwordx4 %[vB0], %[srdB], s60 offen offset:0 sc1 lds\n"
"s_waitcnt lgkmcnt(2)\n"
"v_mfma_f32_16x16x32_bf16 v[48:51], v[116:119], a[16:19], v[48:51]\n"
"ds_read_b64_tr_b16 v[124:125], %[vT30] offset:9216\n"
"v_mfma_f32_16x16x32_bf16 v[52:55], v[116:119], a[20:23], v[52:55]\n"
"ds_read_b64_tr_b16 v[126:127], %[vT30] offset:10240\n"
"v_mfma_f32_16x16x32_bf16 v[56:59], v[116:119], a[24:27], v[56:59]\n"
"buffer_load_dwordx4 %[vB0], %[srdB], s60 offen offset:1024 sc1 lds\n"
"v_mfma_f32_16x16x32_bf16 v[60:63], v[116:119], a[28:31], v[60:63]\n"
"buffer_load_dwordx4 %[vB1], %[srdB], s60 offen offset:2048 sc1 lds\n"
"s_waitcnt lgkmcnt(2)\n"
"v_mfma_f32_16x16x32_bf16 v[64:67], v[120:123], a[16:19], v[64:67]\n"
"ds_read_b64_tr_b16 v[128:129], %[vS00] offset:9216\n"
"v_mfma_f32_16x16x32_bf16 v[68:71], v[120:123], a[20:23], v[68:71]\n"
"buffer_load_dwordx4 %[vB1], %[srdB], s60 offen offset:3072 sc1 lds\n"
"s_waitcnt vmcnt(9)\n"
"s_barrier\n"
"ds_read_b64_tr_b16 v[130:131], %[vS10] offset:9216\n"
"v_mfma_f32_16x16x32_bf16 v[72:75], v[120:123], a[24:27], v[72:75]\n"
"ds_read_b128 a[0:3], %[vTBa0] offset:34816\n"
"v_mfma_f32_16x16x32_bf16 v[76:79], v[120:123], a[28:31], v[76:79]\n"
"ds_read_b128 a[4:7], %[vTBa0] offset:36864\n"
"s_waitcnt lgkmcnt(4)\n"
"v_mfma_f32_16x16x32_bf16 v[80:83], v[124:127], a[16:19], v[80:83]\n"
"ds_read_b128 a[8:11], %[vTBb0] offset:38912\n"
"v_mfma_f32_16x16x32_bf16 v[84:87], v[124:127], a[20:23], v[84:87]\n"
"ds_read_b128 a[12:15], %[vTBb0] offset:40960\n"
"v_mfma_f32_16x16x32_bf16 v[88:91], v[124:127], a[24:27], v[88:91]\n"
"ds_read_b64_tr_b16 v[112:113], %[vT00] offset:34816\n"
"v_mfma_f32_16x16x32_bf16 v[92:95], v[124:127], a[28:31], v[92:95]\n"
"ds_read_b64_tr_b16 v[114:115], %[vT00] offset:35840\n"
"s_waitcnt lgkmcnt(6)\n"
"v_mfma_f32_16x16x32_bf16 v[96:99], v[128:131], a[16:19], v[96:99]\n"
"ds_read_b64_tr_b16 v[116:117], %[vT10] offset:34816\n"
"v_mfma_f32_16x16x32_bf16 v[100:103], v[128:131], a[20:23], v[100:103]\n"
"ds_read_b64_tr_b16 v[118:119], %[vT10] offset:35840\n"
"s_waitcnt lgkmcnt(2)\n"
"v_mfma_f32_16x16x32_bf16 v[32:35], v[112:115], a[0:3], v[32:35]\n"
"ds_read_b64_tr_b16 v[120:121], %[vT20] offset:34816\n"
"v_mfma_f32_16x16x32_bf16 v[36:39], v[112:115], a[4:7], v[36:39]\n"
"ds_read_b64_tr_b16 v[122:123], %[vT20] offset:35840\n"
"v_mfma_f32_16x16x32_bf16 v[40:43], v[112:115], a[8:11], v[40:43]\n"
"ds_read_b128 a[16:19], %[vTBa0] offset:35840\n"
"v_mfma_f32_16x16x32_bf16 v[44:47], v[112:115], a[12:15], v[44:47]\n"
"ds_read_b128 a[20:23], %[vTBa0] offset:37888\n"
"s_waitcnt lgkmcnt(4)\n"
"v_mfma_f32_16x16x32_bf16 v[48:51], v[116:119], a[0:3], v[48:51]\n"
"ds_read_b128 a[24:27], %[vTBb0] offset:39936\n"
"v_mfma_f32_16x16x32_bf16 v[52:55], v[116:119], a[4:7], v[52:55]\n"
"ds_read_b128 a[28:31], %[vTBb0] offset:41984\n"
"v_mfma_f32_16x16x32_bf16 v[56:59], v[116:119], a[8:11], v[56:59]\n"
"ds_read_b64_tr_b16 v[124:125], %[vT30] offset:34816\n"
"v_mfma_f32_16x16x32_bf16 v[60:63], v[116:119], a[12:15], v[60:63]\n"
"ds_read_b64_tr_b16 v[126:127], %[vT30] offset:35840\n"
"s_waitcnt lgkmcnt(6)\n"
"v_mfma_f32_16x16x32_bf16 v[64:67], v[120:123], a[0:3], v[64:67]\n"
"ds_read_b64_tr_b16 v[128:129], %[vS00] offset:34816\n"
"v_mfma_f32_16x16x32_bf16 v[68:71], v[120:123], a[4:7], v[68:71]\n"
"ds_read_b64_tr_b16 v[130:131], %[vS10] offset:34816\n"
"v_mfma_f32_16x16x32_bf16 v[72:75], v[120:123], a[8:11], v[72:75]\n"
"s_add_u32 s61, %[soffA], 516096\n"
"s_add_u32 m0, %[m0A], 104448\n"
"s_nop 0\n"
"buffer_load_dwordx4 %[vA0], %[srdA], s61 offen offset:0 lds\n"
"v_mfma_f32_16x16x32_bf16 v[76:79], v[120:123], a[12:15], v[76:79]\n"
"buffer_load_dwordx4 %[vA1], %[srdA], s61 offen offset:1024 lds\n"
"s_waitcnt lgkmcnt(2)\n"
"v_mfma_f32_16x16x32_bf16 v[80:83], v[124:127], a[0:3], v[80:83]\n"
"ds_read_b64_tr_b16 v[112:113], %[vT00] offset:44032\n"
"v_mfma_f32_16x16x32_bf16 v[84:87], v[124:127], a[4:7], v[84:87]\n"
"ds_read_b64_tr_b16 v[114:115], %[vT00] offset:45056\n"
"v_mfma_f32_16x16x32_bf16 v[88:91], v[124:127], a[8:11], v[88:91]\n"
"buffer_load_dwordx4 %[vA2], %[srdA], s61 offen offset:2048 lds\n"
"v_mfma_f32_16x16x32_bf16 v[92:95], v[124:127], a[12:15], v[92:95]\n"
"buffer_load_dwordx4 %[vA3], %[srdA], s61 offen offset:3072 lds\n"
"s_waitcnt lgkmcnt(2)\n"
"v_mfma_f32_16x16x32_bf16 v[96:99], v[128:131], a[0:3], v[96:99]\n"
"ds_read_b64_tr_b16 v[116:117], %[vT10] offset:44032\n"
"v_mfma_f32_16x16x32_bf16 v[100:103], v[128:131], a[4:7], v[100:103]\n"
"ds_read_b64_tr_b16 v[118:119], %[vT10] offset:45056\n"
"s_waitcnt lgkmcnt(2)\n"
"v_mfma_f32_16x16x32_bf16 v[32:35], v[112:115], a[16:19], v[32:35]\n"
"ds_read_b64_tr_b16 v[120:121], %[vT20] offset:44032\n"
"v_mfma_f32_16x16x32_bf16 v[36:39], v[112:115], a[20:23], v[36:39]\n"
"ds_read_b64_tr_b16 v[122:123], %[vT20] offset:45056\n"
"v_mfma_f32_16x16x32_bf16 v[40:43], v[112:115], a[24:27], v[40:43]\n"
"s_add_u32 m0, %[m0A2], 104448\n"
"s_add_u32 s62, %[soffRm], 14336\n"
"s_mov_b32 exec_hi, 0\n"
"buffer_load_dwordx4 %[vAh], %[srdRm], s62 offen lds\n"
"s_mov_b32 exec_hi, -1\n"
"v_mfma_f32_16x16x32_bf16 v[44:47], v[112:115], a[28:31], v[44:47]\n"
"s_add_u32 s60, %[soffB], 14336\n"
"s_add_u32 m0, %[m0B], 104448\n"
"s_nop 0\n"
"buffer_load_dwordx4 %[vB0], %[srdB], s60 offen offset:0 sc1 lds\n"
"s_waitcnt lgkmcnt(2)\n"
"v_mfma_f32_16x16x32_bf16 v[48:51], v[116:119], a[16:19], v[48:51]\n"
"ds_read_b64_tr_b16 v[124:125], %[vT30] offset:44032\n"
"v_mfma_f32_16x16x32_bf16 v[52:55], v[116:119], a[20:23], v[52:55]\n"
"ds_read_b64_tr_b16 v[126:127], %[vT30] offset:45056\n"
"v_mfma_f32_16x16x32_bf16 v[56:59], v[116:119], a[24:27], v[56:59]\n"
"buffer_load_dwordx4 %[vB0], %[srdB], s60 offen offset:1024 sc1 lds\n"
"v_mfma_f32_16x16x32_bf16 v[60:63], v[116:119], a[28:31], v[60:63]\n"
"buffer_load_dwordx4 %[vB1], %[srdB], s60 offen offset:2048 sc1 lds\n"
"s_waitcnt lgkmcnt(2)\n"
"v_mfma_f32_16x16x32_bf16 v[64:67], v[120:123], a[16:19], v[64:67]\n"
"ds_read_b64_tr_b16 v[128:129], %[vS00] offset:44032\n"
"v_mfma_f32_16x16x32_bf16 v[68:71], v[120:123], a[20:23], v[68:71]\n"
"buffer_load_dwordx4 %[vB1], %[srdB], s60 offen offset:3072 sc1 lds\n"
"s_waitcnt vmcnt(9)\n"
"s_barrier\n"
"ds_read_b64_tr_b16 v[130:131], %[vS10] offset:44032\n"
"v_mfma_f32_16x16x32_bf16 v[72:75], v[120:123], a[24:27], v[72:75]\n"
"ds_read_b128 a[0:3], %[vTBa1] offset:0\n"
"v_mfma_f32_16x16x32_bf16 v[76:79], v[120:123], a[28:31], v[76:79]\n"
"ds_read_b128 a[4:7], %[vTBa1] offset:2048\n"
"s_waitcnt lgkmcnt(4)\n"
"v_mfma_f32_16x16x32_bf16 v[80:83], v[124:127], a[16:19], v[80:83]\n"
"ds_read_b128 a[8:11], %[vTBb1] offset:4096\n"
"v_mfma_f32_16x16x32_bf16 v[84:87], v[124:127], a[20:23], v[84:87]\n"
"ds_read_b128 a[12:15], %[vTBb1] offset:6144\n"
"v_mfma_f32_16x16x32_bf16 v[88:91], v[124:127], a[24:27], v[88:91]\n"
"ds_read_b64_tr_b16 v[112:113], %[vT01] offset:0\n"
"v_mfma_f32_16x16x32_bf16 v[92:95], v[124:127], a[28:31], v[92:95]\n"
"ds_read_b64_tr_b16 v[114:115], %[vT01] offset:1024\n"
"s_waitcnt lgkmcnt(6)\n"
"v_mfma_f32_16x16x32_bf16 v[96:99], v[128:131], a[16:19], v[96:99]\n"
"ds_read_b64_tr_b16 v[116:117], %[vT11] offset:0\n"
"v_mfma_f32_16x16x32_bf16 v[100:103], v[128:131], a[20:23], v[100:103]\n"
"ds_read_b64_tr_b16 v[118:119], %[vT11] offset:1024\n"
"s_waitcnt lgkmcnt(2)\n"
"v_mfma_f32_16x16x32_bf16 v[32:35], v[112:115], a[0:3], v[32:35]\n"
"ds_read_b64_tr_b16 v[120:121], %[vT21] offset:0\n"
"v_mfma_f32_16x16x32_bf16 v[36:39], v[112:115], a[4:7], v[36:39]\n"
"ds_read_b64_tr_b16 v[122:123], %[vT21] offset:1024\n"
"v_mfma_f32_16x16x32_bf16 v[40:43], v[112:115], a[8:11], v[40:43]\n"
"ds_read_b128 a[16:19], %[vTBa1] offset:1024\n"
"v_mfma_f32_16x16x32_bf16 v[44:47], v[112:115], a[12:15], v[44:47]\n"
"ds_read_b128 a[20:23], %[vTBa1] offset:3072\n"
"s_waitcnt lgkmcnt(4)\n"
"v_mfma_f32_16x16x32_bf16 v[48:51], v[116:119], a[0:3], v[48:51]\n"
"ds_read_b128 a[24:27], %[vTBb1] offset:5120\n"
"v_mfma_f32_16x16x32_bf16 v[52:55], v[116:119], a[4:7], v[52:55]\n"
"ds_read_b128 a[28:31], %[vTBb1] offset:7168\n"
"v_mfma_f32_16x16x32_bf16 v[56:59], v[116:119], a[8:11], v[56:59]\n"
"ds_read_b64_tr_b16 v[124:125], %[vT31] offset:0\n"
"v_mfma_f32_16x16x32_bf16 v[60:63], v[116:119], a[12:15], v[60:63]\n"
"ds_read_b64_tr_b16 v[126:127], %[vT31] offset:1024\n"
"s_waitcnt lgkmcnt(6)\n"
"v_mfma_f32_16x16x32_bf16 v[64:67], v[120:123], a[0:3], v[64:67]\n"
"ds_read_b64_tr_b16 v[128:129], %[vS01] offset:0\n"
"v_mfma_f32_16x16x32_bf16 v[68:71], v[120:123], a[4:7], v[68:71]\n"
"ds_read_b64_tr_b16 v[130:131], %[vS11] offset:0\n"
"v_mfma_f32_16x16x32_bf16 v[72:75], v[120:123], a[8:11], v[72:75]\n"
"s_add_u32 s61, %[soffA], 663552\n"
"s_add_u32 m0, %[m0A], 0\n"
"s_nop 0\n"
"buffer_load_dwordx4 %[vA0], %[srdA], s61 offen offset:0 lds\n"
"v_mfma_f32_16x16x32_bf16 v[76:79], v[120:123], a[12:15], v[76:79]\n"
"buffer_load_dwordx4 %[vA1], %[srdA], s61 offen offset:1024 lds\n"
"s_waitcnt lgkmcnt(2)\n"
"v_mfma_f32_16x16x32_bf16 v[80:83], v[124:127], a[0:3], v[80:83]\n"
"ds_read_b64_tr_b16 v[112:113], %[vT01] offset:9216\n"
"v_mfma_f32_16x16x32_bf16 v[84:87], v[124:127], a[4:7], v[84:87]\n"
"ds_read_b64_tr_b16 v[114:115], %[vT01] offset:10240\n"
"v_mfma_f32_16x16x32_bf16 v[88:91], v[124:127], a[8:11], v[88:91]\n"
"buffer_load_dwordx4 %[vA2], %[srdA], s61 offen offset:2048 lds\n"
"v_mfma_f32_16x16x32_bf16 v[92:95], v[124:127], a[12:15], v[92:95]\n"
"buffer_load_dwordx4 %[vA3], %[srdA], s61 offen offset:3072 lds\n"
"s_waitcnt lgkmcnt(2)\n"
"v_mfma_f32_16x16x32_bf16 v[96:99], v[128:131], a[0:3], v[96:99]\n"
"ds_read_b64_tr_b16 v[116:117], %[vT11] offset:9216\n"
"v_mfma_f32_16x16x32_bf16 v[100:103], v[128:131], a[4:7], v[100:103]\n"
"ds_read_b64_tr_b16 v[118:119], %[vT11] offset:10240\n"
"s_waitcnt lgkmcnt(2)\n"
"v_mfma_f32_16x16x32_bf16 v[32:35], v[112:115], a[16:19], v[32:35]\n"
"ds_read_b64_tr_b16 v[120:121], %[vT21] offset:9216\n"
"v_mfma_f32_16x16x32_bf16 v[36:39], v[112:115], a[20:23], v[36:39]\n"
"ds_read_b64_tr_b16 v[122:123], %[vT21] offset:10240\n"
"v_mfma_f32_16x16x32_bf16 v[40:43], v[112:115], a[24:27], v[40:43]\n"
"s_add_u32 m0, %[m0A2], 0\n"
"s_add_u32 s62, %[soffRm], 18432\n"
"s_mov_b32 exec_hi, 0\n"
"buffer_load_dwordx4 %[vAh], %[srdRm], s62 offen lds\n"
"s_mov_b32 exec_hi, -1\n"
"v_mfma_f32_16x16x32_bf16 v[44:47], v[112:115], a[28:31], v[44:47]\n"
"s_add_u32 s60, %[soffB], 18432\n"
"s_add_u32 m0, %[m0B], 0\n"
"s_nop 0\n"
"buffer_load_dwordx4 %[vB0], %[srdB], s60 offen offset:0 sc1 lds\n"
"s_waitcnt lgkmcnt(2)\n"
"v_mfma_f32_16x16x32_bf16 v[48:51], v[116:119], a[16:19], v[48:51]\n"
"ds_read_b64_tr_b16 v[124:125], %[vT31] offset:9216\n"
"v_mfma_f32_16x16x32_bf16 v[52:55], v[116:119], a[20:23], v[52:55]\n"
"ds_read_b64_tr_b16 v[126:127], %[vT31] offset:10240\n"
"v_mfma_f32_16x16x32_bf16 v[56:59], v[116:119], a[24:27], v[56:59]\n"
"buffer_load_dwordx4 %[vB0], %[srdB], s60 offen offset:1024 sc1 lds\n"
"v_mfma_f32_16x16x32_bf16 v[60:63], v[116:119], a[28:31], v[60:63]\n"
"buffer_load_dwordx4 %[vB1], %[srdB], s60 offen offset:2048 sc1 lds\n"
"s_waitcnt lgkmcnt(2)\n"
"v_mfma_f32_16x16x32_bf16 v[64:67], v[120:123], a[16:19], v[64:67]\n"
"ds_read_b64_tr_b16 v[128:129], %[vS01] offset:9216\n"
"v_mfma_f32_16x16x32_bf16 v[68:71], v[120:123], a[20:23], v[68:71]\n"
"buffer_load_dwordx4 %[vB1], %[srdB], s60 offen offset:3072 sc1 lds\n"
"s_waitcnt vmcnt(9)\n"
"s_barrier\n"
"ds_read_b64_tr_b16 v[130:131], %[vS11] offset:9216\n"
"v_mfma_f32_16x16x32_bf16 v[72:75], v[120:123], a[24:27], v[72:75]\n"
"ds_read_b128 a[0:3], %[vTBa1] offset:34816\n"
"v_mfma_f32_16x16x32_bf16 v[76:79], v[120:123], a[28:31], v[76:79]\n"
"ds_read_b128 a[4:7], %[vTBa1] offset:36864\n"
"s_waitcnt lgkmcnt(4)\n"
"v_mfma_f32_16x16x32_bf16 v[80:83], v[124:127], a[16:19], v[80:83]\n"
"ds_read_b128 a[8:11], %[vTBb1] offset:38912\n"
"v_mfma_f32_16x16x32_bf16 v[84:87], v[124:127], a[20:23], v[84:87]\n"
"ds_read_b128 a[12:15], %[vTBb1] offset:40960\n"
"v_mfma_f32_16x16x32_bf16 v[88:91], v[124:127], a[24:27], v[88:91]\n"
"ds_read_b64_tr_b16 v[112:113], %[vT01] offset:34816\n"
"v_mfma_f32_16x16x32_bf16 v[92:95], v[124:127], a[28:31], v[92:95]\n"
"ds_read_b64_tr_b16 v[114:115], %[vT01] offset:35840\n"
"s_waitcnt lgkmcnt(6)\n"
"v_mfma_f32_16x16x32_bf16 v[96:99], v[128:131], a[16:19], v[96:99]\n"
"ds_read_b64_tr_b16 v[116:117], %[vT11] offset:34816\n"
"v_mfma_f32_16x16x32_bf16 v[100:103], v[128:131], a[20:23], v[100:103]\n"
"ds_read_b64_tr_b16 v[118:119], %[vT11] offset:35840\n"
"s_waitcnt lgkmcnt(2)\n"
"v_mfma_f32_16x16x32_bf16 v[32:35], v[112:115], a[0:3], v[32:35]\n"
"ds_read_b64_tr_b16 v[120:121], %[vT21] offset:34816\n"
"v_mfma_f32_16x16x32_bf16 v[36:39], v[112:115], a[4:7], v[36:39]\n"
"ds_read_b64_tr_b16 v[122:123], %[vT21] offset:35840\n"
"v_mfma_f32_16x16x32_bf16 v[40:43], v[112:115], a[8:11], v[40:43]\n"
"ds_read_b128 a[16:19], %[vTBa1] offset:35840\n"
"v_mfma_f32_16x16x32_bf16 v[44:47], v[112:115], a[12:15], v[44:47]\n"
"ds_read_b128 a[20:23], %[vTBa1] offset:37888\n"
"s_waitcnt lgkmcnt(4)\n"
"v_mfma_f32_16x16x32_bf16 v[48:51], v[116:119], a[0:3], v[48:51]\n"
"ds_read_b128 a[24:27], %[vTBb1] offset:39936\n"
"v_mfma_f32_16x16x32_bf16 v[52:55], v[116:119], a[4:7], v[52:55]\n"
"ds_read_b128 a[28:31], %[vTBb1] offset:41984\n"
"v_mfma_f32_16x16x32_bf16 v[56:59], v[116:119], a[8:11], v[56:59]\n"
"ds_read_b64_tr_b16 v[124:125], %[vT31] offset:34816\n"
"v_mfma_f32_16x16x32_bf16 v[60:63], v[116:119], a[12:15], v[60:63]\n"
"ds_read_b64_tr_b16 v[126:127], %[vT31] offset:35840\n"
"s_waitcnt lgkmcnt(6)\n"
"v_mfma_f32_16x16x32_bf16 v[64:67], v[120:123], a[0:3], v[64:67]\n"
"ds_read_b64_tr_b16 v[128:129], %[vS01] offset:34816\n"
"v_mfma_f32_16x16x32_bf16 v[68:71], v[120:123], a[4:7], v[68:71]\n"
"ds_read_b64_tr_b16 v[130:131], %[vS11] offset:34816\n"
"v_mfma_f32_16x16x32_bf16 v[72:75], v[120:123], a[8:11], v[72:75]\n"
"s_add_u32 s61, %[soffA], 811008\n"
"s_add_u32 m0, %[m0A], 34816\n"
"s_nop 0\n"
"buffer_load_dwordx4 %[vA0], %[srdA], s61 offen offset:0 lds\n"
"v_mfma_f32_16x16x32_bf16 v[76:79], v[120:123], a[12:15], v[76:79]\n"
"buffer_load_dwordx4 %[vA1], %[srdA], s61 offen offset:1024 lds\n"
"s_waitcnt lgkmcnt(2)\n"
"v_mfma_f32_16x16x32_bf16 v[80:83], v[124:127], a[0:3], v[80:83]\n"
"ds_read_b64_tr_b16 v[112:113], %[vT01] offset:44032\n"
"v_mfma_f32_16x16x32_bf16 v[84:87], v[124:127], a[4:7], v[84:87]\n"
"ds_read_b64_tr_b16 v[114:115], %[vT01] offset:45056\n"
"v_mfma_f32_16x16x32_bf16 v[88:91], v[124:127], a[8:11], v[88:91]\n"
"buffer_load_dwordx4 %[vA2], %[srdA], s61 offen offset:2048 lds\n"
"v_mfma_f32_16x16x32_bf16 v[92:95], v[124:127], a[12:15], v[92:95]\n"
"buffer_load_dwordx4 %[vA3], %[srdA], s61 offen offset:3072 lds\n"
"s_waitcnt lgkmcnt(2)\n"
"v_mfma_f32_16x16x32_bf16 v[96:99], v[128:131], a[0:3], v[96:99]\n"
"ds_read_b64_tr_b16 v[116:117], %[vT11] offset:44032\n"
"v_mfma_f32_16x16x32_bf16 v[100:103], v[128:131], a[4:7], v[100:103]\n"
"ds_read_b64_tr_b16 v[118:119], %[vT11] offset:45056\n"
"s_waitcnt lgkmcnt(2)\n"
"v_mfma_f32_16x16x32_bf16 v[32:35], v[112:115], a[16:19], v[32:35]\n"
"ds_read_b64_tr_b16 v[120:121], %[vT21] offset:44032\n"
"v_mfma_f32_16x16x32_bf16 v[36:39], v[112:115], a[20:23], v[36:39]\n"
"ds_read_b64_tr_b16 v[122:123], %[vT21] offset:45056\n"
"v_mfma_f32_16x16x32_bf16 v[40:43], v[112:115], a[24:27], v[40:43]\n"
"s_add_u32 m0, %[m0A2], 34816\n"
"s_add_u32 s62, %[soffRm], 22528\n"
"s_mov_b32 exec_hi, 0\n"
"buffer_load_dwordx4 %[vAh], %[srdRm], s62 offen lds\n"
"s_mov_b32 exec_hi, -1\n"
"v_mfma_f32_16x16x32_bf16 v[44:47], v[112:115], a[28:31], v[44:47]\n"
"s_add_u32 s60, %[soffB], 22528\n"
"s_add_u32 m0, %[m0B], 34816\n"
"s_nop 0\n"
"buffer_load_dwordx4 %[vB0], %[srdB], s60 offen offset:0 sc1 lds\n"
"s_waitcnt lgkmcnt(2)\n"
"v_mfma_f32_16x16x32_bf16 v[48:51], v[116:119], a[16:19], v[48:51]\n"
"ds_read_b64_tr_b16 v[124:125], %[vT31] offset:44032\n"
"v_mfma_f32_16x16x32_bf16 v[52:55], v[116:119], a[20:23], v[52:55]\n"
"ds_read_b64_tr_b16 v[126:127], %[vT31] offset:45056\n"
"v_mfma_f32_16x16x32_bf16 v[56:59], v[116:119], a[24:27], v[56:59]\n"
"buffer_load_dwordx4 %[vB0], %[srdB], s60 offen offset:1024 sc1 lds\n"
"v_mfma_f32_16x16x32_bf16 v[60:63], v[116:119], a[28:31], v[60:63]\n"
"buffer_load_dwordx4 %[vB1], %[srdB], s60 offen offset:2048 sc1 lds\n"
"s_waitcnt lgkmcnt(2)\n"
"v_mfma_f32_16x16x32_bf16 v[64:67], v[120:123], a[16:19], v[64:67]\n"
"ds_read_b64_tr_b16 v[128:129], %[vS01] offset:44032\n"
"v_mfma_f32_16x16x32_bf16 v[68:71], v[120:123], a[20:23], v[68:71]\n"
"buffer_load_dwordx4 %[vB1], %[srdB], s60 offen offset:3072 sc1 lds\n"
"s_waitcnt vmcnt(9)\n"
"s_barrier\n"
"ds_read_b64_tr_b16 v[130:131], %[vS11] offset:44032\n"
"v_mfma_f32_16x16x32_bf16 v[72:75], v[120:123], a[24:27], v[72:75]\n"
"ds_read_b128 a[0:3], %[vTBa0] offset:0\n"
"v_mfma_f32_16x16x32_bf16 v[76:79], v[120:123], a[28:31], v[76:79]\n"
"ds_read_b128 a[4:7], %[vTBa0] offset:2048\n"
"s_waitcnt lgkmcnt(4)\n"
"v_mfma_f32_16x16x32_bf16 v[80:83], v[124:127], a[16:19], v[80:83]\n"
"ds_read_b128 a[8:11], %[vTBb0] offset:4096\n"
"v_mfma_f32_16x16x32_bf16 v[84:87], v[124:127], a[20:23], v[84:87]\n"
"ds_read_b128 a[12:15], %[vTBb0] offset:6144\n"
"v_mfma_f32_16x16x32_bf16 v[88:91], v[124:127], a[24:27], v[88:91]\n"
"ds_read_b64_tr_b16 v[112:113], %[vT00] offset:0\n"
"v_mfma_f32_16x16x32_bf16 v[92:95], v[124:127], a[28:31], v[92:95]\n"
"ds_read_b64_tr_b16 v[114:115], %[vT00] offset:1024\n"
"s_waitcnt lgkmcnt(6)\n"
"v_mfma_f32_16x16x32_bf16 v[96:99], v[128:131], a[16:19], v[96:99]\n"
"ds_read_b64_tr_b16 v[116:117], %[vT10] offset:0\n"
"v_mfma_f32_16x16x32_bf16 v[100:103], v[128:131], a[20:23], v[100:103]\n"
"ds_read_b64_tr_b16 v[118:119], %[vT10] offset:1024\n"
"s_waitcnt lgkmcnt(2)\n"
"v_mfma_f32_16x16x32_bf16 v[32:35], v[112:115], a[0:3], v[32:35]\n"
"ds_read_b64_tr_b16 v[120:121], %[vT20] offset:0\n"
"v_mfma_f32_16x16x32_bf16 v[36:39], v[112:115], a[4:7], v[36:39]\n"
"ds_read_b64_tr_b16 v[122:123], %[vT20] offset:1024\n"
"v_mfma_f32_16x16x32_bf16 v[40:43], v[112:115], a[8:11], v[40:43]\n"
"ds_read_b128 a[16:19], %[vTBa0] offset:1024\n"
"v_mfma_f32_16x16x32_bf16 v[44:47], v[112:115], a[12:15], v[44:47]\n"
"ds_read_b128 a[20:23], %[vTBa0] offset:3072\n"
"s_waitcnt lgkmcnt(4)\n"
"v_mfma_f32_16x16x32_bf16 v[48:51], v[116:119], a[0:3], v[48:51]\n"
"ds_read_b128 a[24:27], %[vTBb0] offset:5120\n"
"v_mfma_f32_16x16x32_bf16 v[52:55], v[116:119], a[4:7], v[52:55]\n"
"ds_read_b128 a[28:31], %[vTBb0] offset:7168\n"
"v_mfma_f32_16x16x32_bf16 v[56:59], v[116:119], a[8:11], v[56:59]\n"
"ds_read_b64_tr_b16 v[124:125], %[vT30] offset:0\n"
"v_mfma_f32_16x16x32_bf16 v[60:63], v[116:119], a[12:15], v[60:63]\n"
"ds_read_b64_tr_b16 v[126:127], %[vT30] offset:1024\n"
"s_waitcnt lgkmcnt(6)\n"
"v_mfma_f32_16x16x32_bf16 v[64:67], v[120:123], a[0:3], v[64:67]\n"
"ds_read_b64_tr_b16 v[128:129], %[vS00] offset:0\n"
"v_mfma_f32_16x16x32_bf16 v[68:71], v[120:123], a[4:7], v[68:71]\n"
"ds_read_b64_tr_b16 v[130:131], %[vS10] offset:0\n"
"v_mfma_f32_16x16x32_bf16 v[72:75], v[120:123], a[8:11], v[72:75]\n"
"s_add_u32 s61, %[soffA], 958464\n"
"s_add_u32 m0, %[m0A], 69632\n"
"s_nop 0\n"
"buffer_load_dwordx4 %[vA0], %[srdA], s61 offen offset:0 lds\n"
"v_mfma_f32_16x16x32_bf16 v[76:79], v[120:123], a[12:15], v[76:79]\n"
"buffer_load_dwordx4 %[vA1], %[srdA], s61 offen offset:1024 lds\n"
"s_waitcnt lgkmcnt(2)\n"
"v_mfma_f32_16x16x32_bf16 v[80:83], v[124:127], a[0:3], v[80:83]\n"
"ds_read_b64_tr_b16 v[112:113], %[vT00] offset:9216\n"
"v_mfma_f32_16x16x32_bf16 v[84:87], v[124:127], a[4:7], v[84:87]\n"
"ds_read_b64_tr_b16 v[114:115], %[vT00] offset:10240\n"
"v_mfma_f32_16x16x32_bf16 v[88:91], v[124:127], a[8:11], v[88:91]\n"
"buffer_load_dwordx4 %[vA2], %[srdA], s61 offen offset:2048 lds\n"
"v_mfma_f32_16x16x32_bf16 v[92:95], v[124:127], a[12:15], v[92:95]\n"
"buffer_load_dwordx4 %[vA3], %[srdA], s61 offen offset:3072 lds\n"
"s_waitcnt lgkmcnt(2)\n"
"v_mfma_f32_16x16x32_bf16 v[96:99], v[128:131], a[0:3], v[96:99]\n"
"ds_read_b64_tr_b16 v[116:117], %[vT10] offset:9216\n"
"v_mfma_f32_16x16x32_bf16 v[100:103], v[128:131], a[4:7], v[100:103]\n"
"ds_read_b64_tr_b16 v[118:119], %[vT10] offset:10240\n"
"s_waitcnt lgkmcnt(2)\n"
"v_mfma_f32_16x16x32_bf16 v[32:35], v[112:115], a[16:19], v[32:35]\n"
"ds_read_b64_tr_b16 v[120:121], %[vT20] offset:9216\n"
"v_mfma_f32_16x16x32_bf16 v[36:39], v[112:115], a[20:23], v[36:39]\n"
"ds_read_b64_tr_b16 v[122:123], %[vT20] offset:10240\n"
"v_mfma_f32_16x16x32_bf16 v[40:43], v[112:115], a[24:27], v[40:43]\n"
"s_add_u32 m0, %[m0A2], 69632\n"
"s_add_u32 s62, %[soffRm], 26624\n"
"s_mov_b32 exec_hi, 0\n"
"buffer_load_dwordx4 %[vAh], %[srdRm], s62 offen lds\n"
"s_mov_b32 exec_hi, -1\n"
"v_mfma_f32_16x16x32_bf16 v[44:47], v[112:115], a[28:31], v[44:47]\n"
"s_add_u32 s60, %[soffB], 26624\n"
"s_add_u32 m0, %[m0B], 69632\n"
"s_nop 0\n"
"buffer_load_dwordx4 %[vB0], %[srdB], s60 offen offset:0 sc1 lds\n"
"s_waitcnt lgkmcnt(2)\n"
"v_mfma_f32_16x16x32_bf16 v[48:51], v[116:119], a[16:19], v[48:51]\n"
"ds_read_b64_tr_b16 v[124:125], %[vT30] offset:9216\n"
"v_mfma_f32_16x16x32_bf16 v[52:55], v[116:119], a[20:23], v[52:55]\n"
"ds_read_b64_tr_b16 v[126:127], %[vT30] offset:10240\n"
"v_mfma_f32_16x16x32_bf16 v[56:59], v[116:119], a[24:27], v[56:59]\n"
"buffer_load_dwordx4 %[vB0], %[srdB], s60 offen offset:1024 sc1 lds\n"
"v_mfma_f32_16x16x32_bf16 v[60:63], v[116:119], a[28:31], v[60:63]\n"
"buffer_load_dwordx4 %[vB1], %[srdB], s60 offen offset:2048 sc1 lds\n"
"s_waitcnt lgkmcnt(2)\n"
"v_mfma_f32_16x16x32_bf16 v[64:67], v[120:123], a[16:19], v[64:67]\n"
"ds_read_b64_tr_b16 v[128:129], %[vS00] offset:9216\n"
"v_mfma_f32_16x16x32_bf16 v[68:71], v[120:123], a[20:23], v[68:71]\n"
"buffer_load_dwordx4 %[vB1], %[srdB], s60 offen offset:3072 sc1 lds\n"
"s_waitcnt vmcnt(9)\n"
"s_barrier\n"
"ds_read_b64_tr_b16 v[130:131], %[vS10] offset:9216\n"
"v_mfma_f32_16x16x32_bf16 v[72:75], v[120:123], a[24:27], v[72:75]\n"
"ds_read_b128 a[0:3], %[vTBa0] offset:34816\n"
"v_mfma_f32_16x16x32_bf16 v[76:79], v[120:123], a[28:31], v[76:79]\n"
"ds_read_b128 a[4:7], %[vTBa0] offset:36864\n"
"s_waitcnt lgkmcnt(4)\n"
"v_mfma_f32_16x16x32_bf16 v[80:83], v[124:127], a[16:19], v[80:83]\n"
"ds_read_b128 a[8:11], %[vTBb0] offset:38912\n"
"v_mfma_f32_16x16x32_bf16 v[84:87], v[124:127], a[20:23], v[84:87]\n"
"ds_read_b128 a[12:15], %[vTBb0] offset:40960\n"
"v_mfma_f32_16x16x32_bf16 v[88:91], v[124:127], a[24:27], v[88:91]\n"
"ds_read_b64_tr_b16 v[112:113], %[vT00] offset:34816\n"
"v_mfma_f32_16x16x32_bf16 v[92:95], v[124:127], a[28:31], v[92:95]\n"
"ds_read_b64_tr_b16 v[114:115], %[vT00] offset:35840\n"
"s_waitcnt lgkmcnt(6)\n"
"v_mfma_f32_16x16x32_bf16 v[96:99], v[128:131], a[16:19], v[96:99]\n"
"ds_read_b64_tr_b16 v[116:117], %[vT10] offset:34816\n"
"v_mfma_f32_16x16x32_bf16 v[100:103], v[128:131], a[20:23], v[100:103]\n"
"ds_read_b64_tr_b16 v[118:119], %[vT10] offset:35840\n"
"s_waitcnt lgkmcnt(2)\n"
"v_mfma_f32_16x16x32_bf16 v[32:35], v[112:115], a[0:3], v[32:35]\n"
"ds_read_b64_tr_b16 v[120:121], %[vT20] offset:34816\n"
"v_mfma_f32_16x16x32_bf16 v[36:39], v[112:115], a[4:7], v[36:39]\n"
"ds_read_b64_tr_b16 v[122:123], %[vT20] offset:35840\n"
"v_mfma_f32_16x16x32_bf16 v[40:43], v[112:115], a[8:11], v[40:43]\n"
"ds_read_b128 a[16:19], %[vTBa0] offset:35840\n"
"v_mfma_f32_16x16x32_bf16 v[44:47], v[112:115], a[12:15], v[44:47]\n"
"ds_read_b128 a[20:23], %[vTBa0] offset:37888\n"
"s_waitcnt lgkmcnt(4)\n"
"v_mfma_f32_16x16x32_bf16 v[48:51], v[116:119], a[0:3], v[48:51]\n"
"ds_read_b128 a[24:27], %[vTBb0] offset:39936\n"
"v_mfma_f32_16x16x32_bf16 v[52:55], v[116:119], a[4:7], v[52:55]\n"
"ds_read_b128 a[28:31], %[vTBb0] offset:41984\n"
"v_mfma_f32_16x16x32_bf16 v[56:59], v[116:119], a[8:11], v[56:59]\n"
"ds_read_b64_tr_b16 v[124:125], %[vT30] offset:34816\n"
"v_mfma_f32_16x16x32_bf16 v[60:63], v[116:119], a[12:15], v[60:63]\n"
"ds_read_b64_tr_b16 v[126:127], %[vT30] offset:35840\n"
"s_waitcnt lgkmcnt(6)\n"
"v_mfma_f32_16x16x32_bf16 v[64:67], v[120:123], a[0:3], v[64:67]\n"
"ds_read_b64_tr_b16 v[128:129], %[vS00] offset:34816\n"
"v_mfma_f32_16x16x32_bf16 v[68:71], v[120:123], a[4:7], v[68:71]\n"
"ds_read_b64_tr_b16 v[130:131], %[vS10] offset:34816\n"
"v_mfma_f32_16x16x32_bf16 v[72:75], v[120:123], a[8:11], v[72:75]\n"
"s_add_u32 s61, %[soffA], 1105920\n"
"s_add_u32 m0, %[m0A], 104448\n"
"s_nop 0\n"
"buffer_load_dwordx4 %[vA0], %[srdA], s61 offen offset:0 lds\n"
"v_mfma_f32_16x16x32_bf16 v[76:79], v[120:123], a[12:15], v[76:79]\n"
"buffer_load_dwordx4 %[vA1], %[srdA], s61 offen offset:1024 lds\n"
"s_waitcnt lgkmcnt(2)\n"
"v_mfma_f32_16x16x32_bf16 v[80:83], v[124:127], a[0:3], v[80:83]\n"
"ds_read_b64_tr_b16 v[112:113], %[vT00] offset:44032\n"
"v_mfma_f32_16x16x32_bf16 v[84:87], v[124:127], a[4:7], v[84:87]\n"
"ds_read_b64_tr_b16 v[114:115], %[vT00] offset:45056\n"
"v_mfma_f32_16x16x32_bf16 v[88:91], v[124:127], a[8:11], v[88:91]\n"
"buffer_load_dwordx4 %[vA2], %[srdA], s61 offen offset:2048 lds\n"
"v_mfma_f32_16x16x32_bf16 v[92:95], v[124:127], a[12:15], v[92:95]\n"
"buffer_load_dwordx4 %[vA3], %[srdA], s61 offen offset:3072 lds\n"
"s_waitcnt lgkmcnt(2)\n"
"v_mfma_f32_16x16x32_bf16 v[96:99], v[128:131], a[0:3], v[96:99]\n"
"ds_read_b64_tr_b16 v[116:117], %[vT10] offset:44032\n"
"v_mfma_f32_16x16x32_bf16 v[100:103], v[128:131], a[4:7], v[100:103]\n"
"ds_read_b64_tr_b16 v[118:119], %[vT10] offset:45056\n"
"s_waitcnt lgkmcnt(2)\n"
"v_mfma_f32_16x16x32_bf16 v[32:35], v[112:115], a[16:19], v[32:35]\n"
"ds_read_b64_tr_b16 v[120:121], %[vT20] offset:44032\n"
"v_mfma_f32_16x16x32_bf16 v[36:39], v[112:115], a[20:23], v[36:39]\n"
"ds_read_b64_tr_b16 v[122:123], %[vT20] offset:45056\n"
"v_mfma_f32_16x16x32_bf16 v[40:43], v[112:115], a[24:27], v[40:43]\n"
"s_add_u32 m0, %[m0A2], 104448\n"
"s_add_u32 s62, %[soffRm], 30720\n"
"s_mov_b32 exec_hi, 0\n"
"buffer_load_dwordx4 %[vAh], %[srdRm], s62 offen lds\n"
"s_mov_b32 exec_hi, -1\n"
"v_mfma_f32_16x16x32_bf16 v[44:47], v[112:115], a[28:31], v[44:47]\n"
"s_add_u32 s60, %[soffB], 30720\n"
"s_add_u32 m0, %[m0B], 104448\n"
"s_nop 0\n"
"buffer_load_dwordx4 %[vB0], %[srdB], s60 offen offset:0 sc1 lds\n"
"s_waitcnt lgkmcnt(2)\n"
"v_mfma_f32_16x16x32_bf16 v[48:51], v[116:119], a[16:19], v[48:51]\n"
"ds_read_b64_tr_b16 v[124:125], %[vT30] offset:44032\n"
"v_mfma_f32_16x16x32_bf16 v[52:55], v[116:119], a[20:23], v[52:55]\n"
"ds_read_b64_tr_b16 v[126:127], %[vT30] offset:45056\n"
"v_mfma_f32_16x16x32_bf16 v[56:59], v[116:119], a[24:27], v[56:59]\n"
"buffer_load_dwordx4 %[vB0], %[srdB], s60 offen offset:1024 sc1 lds\n"
"v_mfma_f32_16x16x32_bf16 v[60:63], v[116:119], a[28:31], v[60:63]\n"
"buffer_load_dwordx4 %[vB1], %[srdB], s60 offen offset:2048 sc1 lds\n"
"s_waitcnt lgkmcnt(2)\n"
"v_mfma_f32_16x16x32_bf16 v[64:67], v[120:123], a[16:19], v[64:67]\n"
"ds_read_b64_tr_b16 v[128:129], %[vS00] offset:44032\n"
"v_mfma_f32_16x16x32_bf16 v[68:71], v[120:123], a[20:23], v[68:71]\n"
"buffer_load_dwordx4 %[vB1], %[srdB], s60 offen offset:3072 sc1 lds\n"
"s_waitcnt vmcnt(9)\n"
"s_barrier\n"
"ds_read_b64_tr_b16 v[130:131], %[vS10] offset:44032\n"
"v_mfma_f32_16x16x32_bf16 v[72:75], v[120:123], a[24:27], v[72:75]\n"
"ds_read_b128 a[0:3], %[vTBa1] offset:0\n"
"v_mfma_f32_16x16x32_bf16 v[76:79], v[120:123], a[28:31], v[76:79]\n"
"ds_read_b128 a[4:7], %[vTBa1] offset:2048\n"
"s_waitcnt lgkmcnt(4)\n"
"v_mfma_f32_16x16x32_bf16 v[80:83], v[124:127], a[16:19], v[80:83]\n"
"ds_read_b128 a[8:11], %[vTBb1] offset:4096\n"
"v_mfma_f32_16x16x32_bf16 v[84:87], v[124:127], a[20:23], v[84:87]\n"
"ds_read_b128 a[12:15], %[vTBb1] offset:6144\n"
"v_mfma_f32_16x16x32_bf16 v[88:91], v[124:127], a[24:27], v[88:91]\n"
"ds_read_b64_tr_b16 v[112:113], %[vT01] offset:0\n"
"v_mfma_f32_16x16x32_bf16 v[92:95], v[124:127], a[28:31], v[92:95]\n"
"ds_read_b64_tr_b16 v[114:115], %[vT01] offset:1024\n"
"s_waitcnt lgkmcnt(6)\n"
"v_mfma_f32_16x16x32_bf16 v[96:99], v[128:131], a[16:19], v[96:99]\n"
"ds_read_b64_tr_b16 v[116:117], %[vT11] offset:0\n"
"v_mfma_f32_16x16x32_bf16 v[100:103], v[128:131], a[20:23], v[100:103]\n"
"ds_read_b64_tr_b16 v[118:119], %[vT11] offset:1024\n"
"s_waitcnt lgkmcnt(2)\n"
"v_mfma_f32_16x16x32_bf16 v[32:35], v[112:115], a[0:3], v[32:35]\n"
"ds_read_b64_tr_b16 v[120:121], %[vT21] offset:0\n"
"v_mfma_f32_16x16x32_bf16 v[36:39], v[112:115], a[4:7], v[36:39]\n"
"ds_read_b64_tr_b16 v[122:123], %[vT21] offset:1024\n"
"v_mfma_f32_16x16x32_bf16 v[40:43], v[112:115], a[8:11], v[40:43]\n"
"ds_read_b128 a[16:19], %[vTBa1] offset:1024\n"
"v_mfma_f32_16x16x32_bf16 v[44:47], v[112:115], a[12:15], v[44:47]\n"
"ds_read_b128 a[20:23], %[vTBa1] offset:3072\n"
"s_waitcnt lgkmcnt(4)\n"
"v_mfma_f32_16x16x32_bf16 v[48:51], v[116:119], a[0:3], v[48:51]\n"
"ds_read_b128 a[24:27], %[vTBb1] offset:5120\n"
"v_mfma_f32_16x16x32_bf16 v[52:55], v[116:119], a[4:7], v[52:55]\n"
"ds_read_b128 a[28:31], %[vTBb1] offset:7168\n"
"v_mfma_f32_16x16x32_bf16 v[56:59], v[116:119], a[8:11], v[56:59]\n"
"ds_read_b64_tr_b16 v[124:125], %[vT31] offset:0\n"
"v_mfma_f32_16x16x32_bf16 v[60:63], v[116:119], a[12:15], v[60:63]\n"
"ds_read_b64_tr_b16 v[126:127], %[vT31] offset:1024\n"
"s_waitcnt lgkmcnt(6)\n"
"v_mfma_f32_16x16x32_bf16 v[64:67], v[120:123], a[0:3], v[64:67]\n"
"ds_read_b64_tr_b16 v[128:129], %[vS01] offset:0\n"
"v_mfma_f32_16x16x32_bf16 v[68:71], v[120:123], a[4:7], v[68:71]\n"
"ds_read_b64_tr_b16 v[130:131], %[vS11] offset:0\n"
"v_mfma_f32_16x16x32_bf16 v[72:75], v[120:123], a[8:11], v[72:75]\n"
"s_add_u32 s61, %[soffA2], 0\n"
"s_add_u32 m0, %[m0A], 0\n"
"s_nop 0\n"
"buffer_load_dwordx4 %[vA0], %[srdA], s61 offen offset:0 lds\n"
"v_mfma_f32_16x16x32_bf16 v[76:79], v[120:123], a[12:15], v[76:79]\n"
"buffer_load_dwordx4 %[vA1], %[srdA], s61 offen offset:1024 lds\n"
"s_waitcnt lgkmcnt(2)\n"
"v_mfma_f32_16x16x32_bf16 v[80:83], v[124:127], a[0:3], v[80:83]\n"
"ds_read_b64_tr_b16 v[112:113], %[vT01] offset:9216\n"
"v_mfma_f32_16x16x32_bf16 v[84:87], v[124:127], a[4:7], v[84:87]\n"
"ds_read_b64_tr_b16 v[114:115], %[vT01] offset:10240\n"
"v_mfma_f32_16x16x32_bf16 v[88:91], v[124:127], a[8:11], v[88:91]\n"
"buffer_load_dwordx4 %[vA2], %[srdA], s61 offen offset:2048 lds\n"
"v_mfma_f32_16x16x32_bf16 v[92:95], v[124:127], a[12:15], v[92:95]\n"
"buffer_load_dwordx4 %[vA3], %[srdA], s61 offen offset:3072 lds\n"
"s_waitcnt lgkmcnt(2)\n"
"v_mfma_f32_16x16x32_bf16 v[96:99], v[128:131], a[0:3], v[96:99]\n"
"ds_read_b64_tr_b16 v[116:117], %[vT11] offset:9216\n"
"v_mfma_f32_16x16x32_bf16 v[100:103], v[128:131], a[4:7], v[100:103]\n"
"ds_read_b64_tr_b16 v[118:119], %[vT11] offset:10240\n"
"s_waitcnt lgkmcnt(2)\n"
"v_mfma_f32_16x16x32_bf16 v[32:35], v[112:115], a[16:19], v[32:35]\n"
"ds_read_b64_tr_b16 v[120:121], %[vT21] offset:9216\n"
"v_mfma_f32_16x16x32_bf16 v[36:39], v[112:115], a[20:23], v[36:39]\n"
"ds_read_b64_tr_b16 v[122:123], %[vT21] offset:10240\n"
"v_mfma_f32_16x16x32_bf16 v[40:43], v[112:115], a[24:27], v[40:43]\n"
"s_add_u32 m0, %[m0A2], 0\n"
"s_add_u32 s62, %[soffRm], 32768\n"
"s_mov_b32 exec_hi, 0\n"
"buffer_load_dwordx4 %[vAh], %[srdRm], s62 offen lds\n"
"s_mov_b32 exec_hi, -1\n"
"v_mfma_f32_16x16x32_bf16 v[44:47], v[112:115], a[28:31], v[44:47]\n"
"s_add_u32 s60, %[soffB], 32768\n"
"s_add_u32 m0, %[m0B], 0\n"
"s_nop 0\n"
"buffer_load_dwordx4 %[vB0], %[srdB], s60 offen offset:0 sc1 lds\n"
"s_waitcnt lgkmcnt(2)\n"
"v_mfma_f32_16x16x32_bf16 v[48:51], v[116:119], a[16:19], v[48:51]\n"
"ds_read_b64_tr_b16 v[124:125], %[vT31] offset:9216\n"
"v_mfma_f32_16x16x32_bf16 v[52:55], v[116:119], a[20:23], v[52:55]\n"
"ds_read_b64_tr_b16 v[126:127], %[vT31] offset:10240\n"
"v_mfma_f32_16x16x32_bf16 v[56:59], v[116:119], a[24:27], v[56:59]\n"
"buffer_load_dwordx4 %[vB0], %[srdB], s60 offen offset:1024 sc1 lds\n"
"v_mfma_f32_16x16x32_bf16 v[60:63], v[116:119], a[28:31], v[60:63]\n"
"buffer_load_dwordx4 %[vB1], %[srdB], s60 offen offset:2048 sc1 lds\n"
"s_waitcnt lgkmcnt(2)\n"
"v_mfma_f32_16x16x32_bf16 v[64:67], v[120:123], a[16:19], v[64:67]\n"
"ds_read_b64_tr_b16 v[128:129], %[vS01] offset:9216\n"
"v_mfma_f32_16x16x32_bf16 v[68:71], v[120:123], a[20:23], v[68:71]\n"
"buffer_load_dwordx4 %[vB1], %[srdB], s60 offen offset:3072 sc1 lds\n"
"s_waitcnt vmcnt(9)\n"
"s_barrier\n"
"ds_read_b64_tr_b16 v[130:131], %[vS11] offset:9216\n"
"v_mfma_f32_16x16x32_bf16 v[72:75], v[120:123], a[24:27], v[72:75]\n"
"ds_read_b128 a[0:3], %[vTBa1] offset:34816\n"
"v_mfma_f32_16x16x32_bf16 v[76:79], v[120:123], a[28:31], v[76:79]\n"
"ds_read_b128 a[4:7], %[vTBa1] offset:36864\n"
"s_waitcnt lgkmcnt(4)\n"
"v_mfma_f32_16x16x32_bf16 v[80:83], v[124:127], a[16:19], v[80:83]\n"
"ds_read_b128 a[8:11], %[vTBb1] offset:38912\n"
"v_mfma_f32_16x16x32_bf16 v[84:87], v[124:127], a[20:23], v[84:87]\n"
"ds_read_b128 a[12:15], %[vTBb1] offset:40960\n"
"v_mfma_f32_16x16x32_bf16 v[88:91], v[124:127], a[24:27], v[88:91]\n"
"ds_read_b64_tr_b16 v[112:113], %[vT01] offset:34816\n"
"v_mfma_f32_16x16x32_bf16 v[92:95], v[124:127], a[28:31], v[92:95]\n"
"ds_read_b64_tr_b16 v[114:115], %[vT01] offset:35840\n"
"s_waitcnt lgkmcnt(6)\n"
"v_mfma_f32_16x16x32_bf16 v[96:99], v[128:131], a[16:19], v[96:99]\n"
"ds_read_b64_tr_b16 v[116:117], %[vT11] offset:34816\n"
"v_mfma_f32_16x16x32_bf16 v[100:103], v[128:131], a[20:23], v[100:103]\n"
"ds_read_b64_tr_b16 v[118:119], %[vT11] offset:35840\n"
"s_waitcnt lgkmcnt(2)\n"
"v_mfma_f32_16x16x32_bf16 v[32:35], v[112:115], a[0:3], v[32:35]\n"
"ds_read_b64_tr_b16 v[120:121], %[vT21] offset:34816\n"
"v_mfma_f32_16x16x32_bf16 v[36:39], v[112:115], a[4:7], v[36:39]\n"
"ds_read_b64_tr_b16 v[122:123], %[vT21] offset:35840\n"
"v_mfma_f32_16x16x32_bf16 v[40:43], v[112:115], a[8:11], v[40:43]\n"
"ds_read_b128 a[16:19], %[vTBa1] offset:35840\n"
"v_mfma_f32_16x16x32_bf16 v[44:47], v[112:115], a[12:15], v[44:47]\n"
"ds_read_b128 a[20:23], %[vTBa1] offset:37888\n"
"s_waitcnt lgkmcnt(4)\n"
"v_mfma_f32_16x16x32_bf16 v[48:51], v[116:119], a[0:3], v[48:51]\n"
"ds_read_b128 a[24:27], %[vTBb1] offset:39936\n"
"v_mfma_f32_16x16x32_bf16 v[52:55], v[116:119], a[4:7], v[52:55]\n"
"ds_read_b128 a[28:31], %[vTBb1] offset:41984\n"
"v_mfma_f32_16x16x32_bf16 v[56:59], v[116:119], a[8:11], v[56:59]\n"
"ds_read_b64_tr_b16 v[124:125], %[vT31] offset:34816\n"
"v_mfma_f32_16x16x32_bf16 v[60:63], v[116:119], a[12:15], v[60:63]\n"
"ds_read_b64_tr_b16 v[126:127], %[vT31] offset:35840\n"
"s_waitcnt lgkmcnt(6)\n"
"v_mfma_f32_16x16x32_bf16 v[64:67], v[120:123], a[0:3], v[64:67]\n"
"ds_read_b64_tr_b16 v[128:129], %[vS01] offset:34816\n"
"v_mfma_f32_16x16x32_bf16 v[68:71], v[120:123], a[4:7], v[68:71]\n"
"ds_read_b64_tr_b16 v[130:131], %[vS11] offset:34816\n"
"v_mfma_f32_16x16x32_bf16 v[72:75], v[120:123], a[8:11], v[72:75]\n"
"s_add_u32 s61, %[soffA2], 73728\n"
"s_add_u32 m0, %[m0A], 34816\n"
"s_nop 0\n"
"buffer_load_dwordx4 %[vA0], %[srdA], s61 offen offset:0 lds\n"
"v_mfma_f32_16x16x32_bf16 v[76:79], v[120:123], a[12:15], v[76:79]\n"
"buffer_load_dwordx4 %[vA1], %[srdA], s61 offen offset:1024 lds\n"
"s_waitcnt lgkmcnt(2)\n"
"v_mfma_f32_16x16x32_bf16 v[80:83], v[124:127], a[0:3], v[80:83]\n"
"ds_read_b64_tr_b16 v[112:113], %[vT01] offset:44032\n"
"v_mfma_f32_16x16x32_bf16 v[84:87], v[124:127], a[4:7], v[84:87]\n"
"ds_read_b64_tr_b16 v[114:115], %[vT01] offset:45056\n"
"v_mfma_f32_16x16x32_bf16 v[88:91], v[124:127], a[8:11], v[88:91]\n"
"buffer_load_dwordx4 %[vA2], %[srdA], s61 offen offset:2048 lds\n"
"v_mfma_f32_16x16x32_bf16 v[92:95], v[124:127], a[12:15], v[92:95]\n"
"buffer_load_dwordx4 %[vA3], %[srdA], s61 offen offset:3072 lds\n"
"s_waitcnt lgkmcnt(2)\n"
"v_mfma_f32_16x16x32_bf16 v[96:99], v[128:131], a[0:3], v[96:99]\n"
"ds_read_b64_tr_b16 v[116:117], %[vT11] offset:44032\n"
"v_mfma_f32_16x16x32_bf16 v[100:103], v[128:131], a[4:7], v[100:103]\n"
"ds_read_b64_tr_b16 v[118:119], %[vT11] offset:45056\n"
"s_waitcnt lgkmcnt(2)\n"
"v_mfma_f32_16x16x32_bf16 v[32:35], v[112:115], a[16:19], v[32:35]\n"
"ds_read_b64_tr_b16 v[120:121], %[vT21] offset:44032\n"
"v_mfma_f32_16x16x32_bf16 v[36:39], v[112:115], a[20:23], v[36:39]\n"
"ds_read_b64_tr_b16 v[122:123], %[vT21] offset:45056\n"
"v_mfma_f32_16x16x32_bf16 v[40:43], v[112:115], a[24:27], v[40:43]\n"
"s_add_u32 m0, %[m0A2], 34816\n"
"s_add_u32 s62, %[soffRm], 34816\n"
"s_mov_b32 exec_hi, 0\n"
"buffer_load_dwordx4 %[vAh], %[srdRm], s62 offen lds\n"
"s_mov_b32 exec_hi, -1\n"
"v_mfma_f32_16x16x32_bf16 v[44:47], v[112:115], a[28:31], v[44:47]\n"
"s_add_u32 s60, %[soffB], 34816\n"
"s_add_u32 m0, %[m0B], 34816\n"
"s_nop 0\n"
"buffer_load_dwordx4 %[vB0], %[srdB], s60 offen offset:0 sc1 lds\n"
"s_waitcnt lgkmcnt(2)\n"
"v_mfma_f32_16x16x32_bf16 v[48:51], v[116:119], a[16:19], v[48:51]\n"
"ds_read_b64_tr_b16 v[124:125], %[vT31] offset:44032\n"
"v_mfma_f32_16x16x32_bf16 v[52:55], v[116:119], a[20:23], v[52:55]\n"
"ds_read_b64_tr_b16 v[126:127], %[vT31] offset:45056\n"
"v_mfma_f32_16x16x32_bf16 v[56:59], v[116:119], a[24:27], v[56:59]\n"
"buffer_load_dwordx4 %[vB0], %[srdB], s60 offen offset:1024 sc1 lds\n"
"v_mfma_f32_16x16x32_bf16 v[60:63], v[116:119], a[28:31], v[60:63]\n"
"buffer_load_dwordx4 %[vB1], %[srdB], s60 offen offset:2048 sc1 lds\n"
"s_waitcnt lgkmcnt(2)\n"
"v_mfma_f32_16x16x32_bf16 v[64:67], v[120:123], a[16:19], v[64:67]\n"
"ds_read_b64_tr_b16 v[128:129], %[vS01] offset:44032\n"
"v_mfma_f32_16x16x32_bf16 v[68:71], v[120:123], a[20:23], v[68:71]\n"
"buffer_load_dwordx4 %[vB1], %[srdB], s60 offen offset:3072 sc1 lds\n"
"s_waitcnt vmcnt(9)\n"
"s_barrier\n"
"ds_read_b64_tr_b16 v[130:131], %[vS11] offset:44032\n"
"v_mfma_f32_16x16x32_bf16 v[72:75], v[120:123], a[24:27], v[72:75]\n"
"ds_read_b128 a[0:3], %[vTBa0] offset:0\n"
"v_mfma_f32_16x16x32_bf16 v[76:79], v[120:123], a[28:31], v[76:79]\n"
"ds_read_b128 a[4:7], %[vTBa0] offset:2048\n"
"s_waitcnt lgkmcnt(4)\n"
"v_mfma_f32_16x16x32_bf16 v[80:83], v[124:127], a[16:19], v[80:83]\n"
"ds_read_b128 a[8:11], %[vTBb0] offset:4096\n"
"v_mfma_f32_16x16x32_bf16 v[84:87], v[124:127], a[20:23], v[84:87]\n"
"ds_read_b128 a[12:15], %[vTBb0] offset:6144\n"
"v_mfma_f32_16x16x32_bf16 v[88:91], v[124:127], a[24:27], v[88:91]\n"
"ds_read_b64_tr_b16 v[112:113], %[vT00] offset:0\n"
"v_mfma_f32_16x16x32_bf16 v[92:95], v[124:127], a[28:31], v[92:95]\n"
"ds_read_b64_tr_b16 v[114:115], %[vT00] offset:1024\n"
"s_waitcnt lgkmcnt(6)\n"
"v_mfma_f32_16x16x32_bf16 v[96:99], v[128:131], a[16:19], v[96:99]\n"
"ds_read_b64_tr_b16 v[116:117], %[vT10] offset:0\n"
"v_mfma_f32_16x16x32_bf16 v[100:103], v[128:131], a[20:23], v[100:103]\n"
"ds_read_b64_tr_b16 v[118:119], %[vT10] offset:1024\n"
"s_waitcnt lgkmcnt(2)\n"
"v_mfma_f32_16x16x32_bf16 v[32:35], v[112:115], a[0:3], v[32:35]\n"
"ds_read_b64_tr_b16 v[120:121], %[vT20] offset:0\n"
"v_mfma_f32_16x16x32_bf16 v[36:39], v[112:115], a[4:7], v[36:39]\n"
"ds_read_b64_tr_b16 v[122:123], %[vT20] offset:1024\n"
"v_mfma_f32_16x16x32_bf16 v[40:43], v[112:115], a[8:11], v[40:43]\n"
"ds_read_b128 a[16:19], %[vTBa0] offset:1024\n"
"v_mfma_f32_16x16x32_bf16 v[44:47], v[112:115], a[12:15], v[44:47]\n"
"ds_read_b128 a[20:23], %[vTBa0] offset:3072\n"
"s_waitcnt lgkmcnt(4)\n"
"v_mfma_f32_16x16x32_bf16 v[48:51], v[116:119], a[0:3], v[48:51]\n"
"ds_read_b128 a[24:27], %[vTBb0] offset:5120\n"
"v_mfma_f32_16x16x32_bf16 v[52:55], v[116:119], a[4:7], v[52:55]\n"
"ds_read_b128 a[28:31], %[vTBb0] offset:7168\n"
"v_mfma_f32_16x16x32_bf16 v[56:59], v[116:119], a[8:11], v[56:59]\n"
"ds_read_b64_tr_b16 v[124:125], %[vT30] offset:0\n"
"v_mfma_f32_16x16x32_bf16 v[60:63], v[116:119], a[12:15], v[60:63]\n"
"ds_read_b64_tr_b16 v[126:127], %[vT30] offset:1024\n"
"s_waitcnt lgkmcnt(6)\n"
"v_mfma_f32_16x16x32_bf16 v[64:67], v[120:123], a[0:3], v[64:67]\n"
"ds_read_b64_tr_b16 v[128:129], %[vS00] offset:0\n"
"v_mfma_f32_16x16x32_bf16 v[68:71], v[120:123], a[4:7], v[68:71]\n"
"ds_read_b64_tr_b16 v[130:131], %[vS10] offset:0\n"
"v_mfma_f32_16x16x32_bf16 v[72:75], v[120:123], a[8:11], v[72:75]\n"
"v_mfma_f32_16x16x32_bf16 v[76:79], v[120:123], a[12:15], v[76:79]\n"
"s_waitcnt lgkmcnt(2)\n"
"v_mfma_f32_16x16x32_bf16 v[80:83], v[124:127], a[0:3], v[80:83]\n"
"ds_read_b64_tr_b16 v[112:113], %[vT00] offset:9216\n"
"v_mfma_f32_16x16x32_bf16 v[84:87], v[124:127], a[4:7], v[84:87]\n"
"ds_read_b64_tr_b16 v[114:115], %[vT00] offset:10240\n"
"v_mfma_f32_16x16x32_bf16 v[88:91], v[124:127], a[8:11], v[88:91]\n"
"v_mfma_f32_16x16x32_bf16 v[92:95], v[124:127], a[12:15], v[92:95]\n"
"s_waitcnt lgkmcnt(2)\n"
"v_mfma_f32_16x16x32_bf16 v[96:99], v[128:131], a[0:3], v[96:99]\n"
"ds_read_b64_tr_b16 v[116:117], %[vT10] offset:9216\n"
"v_mfma_f32_16x16x32_bf16 v[100:103], v[128:131], a[4:7], v[100:103]\n"
"ds_read_b64_tr_b16 v[118:119], %[vT10] offset:10240\n"
"s_waitcnt lgkmcnt(2)\n"
"v_mfma_f32_16x16x32_bf16 v[32:35], v[112:115], a[16:19], v[32:35]\n"
"ds_read_b64_tr_b16 v[120:121], %[vT20] offset:9216\n"
"v_mfma_f32_16x16x32_bf16 v[36:39], v[112:115], a[20:23], v[36:39]\n"
"ds_read_b64_tr_b16 v[122:123], %[vT20] offset:10240\n"
"v_mfma_f32_16x16x32_bf16 v[40:43], v[112:115], a[24:27], v[40:43]\n"
"v_mfma_f32_16x16x32_bf16 v[44:47], v[112:115], a[28:31], v[44:47]\n"
"s_waitcnt lgkmcnt(2)\n"
"v_mfma_f32_16x16x32_bf16 v[48:51], v[116:119], a[16:19], v[48:51]\n"
"ds_read_b64_tr_b16 v[124:125], %[vT30] offset:9216\n"
"v_mfma_f32_16x16x32_bf16 v[52:55], v[116:119], a[20:23], v[52:55]\n"
"ds_read_b64_tr_b16 v[126:127], %[vT30] offset:10240\n"
"v_mfma_f32_16x16x32_bf16 v[56:59], v[116:119], a[24:27], v[56:59]\n"
"v_mfma_f32_16x16x32_bf16 v[60:63], v[116:119], a[28:31], v[60:63]\n"
"s_waitcnt lgkmcnt(2)\n"
"v_mfma_f32_16x16x32_bf16 v[64:67], v[120:123], a[16:19], v[64:67]\n"
"ds_read_b64_tr_b16 v[128:129], %[vS00] offset:9216\n"
"v_mfma_f32_16x16x32_bf16 v[68:71], v[120:123], a[20:23], v[68:71]\n"
"s_waitcnt vmcnt(0)\n"
"s_barrier\n"
"ds_read_b64_tr_b16 v[130:131], %[vS10] offset:9216\n"
"v_mfma_f32_16x16x32_bf16 v[72:75], v[120:123], a[24:27], v[72:75]\n"
"ds_read_b128 a[0:3], %[vTBa0] offset:34816\n"
"v_mfma_f32_16x16x32_bf16 v[76:79], v[120:123], a[28:31], v[76:79]\n"
"ds_read_b128 a[4:7], %[vTBa0] offset:36864\n"
"s_waitcnt lgkmcnt(4)\n"
"v_mfma_f32_16x16x32_bf16 v[80:83], v[124:127], a[16:19], v[80:83]\n"
"ds_read_b128 a[8:11], %[vTBb0] offset:38912\n"
"v_mfma_f32_16x16x32_bf16 v[84:87], v[124:127], a[20:23], v[84:87]\n"
"ds_read_b128 a[12:15], %[vTBb0] offset:40960\n"
"v_mfma_f32_16x16x32_bf16 v[88:91], v[124:127], a[24:27], v[88:91]\n"
"ds_read_b64_tr_b16 v[112:113], %[vT00] offset:34816\n"
"v_mfma_f32_16x16x32_bf16 v[92:95], v[124:127], a[28:31], v[92:95]\n"
"ds_read_b64_tr_b16 v[114:115], %[vT00] offset:35840\n"
"s_waitcnt lgkmcnt(6)\n"
"v_mfma_f32_16x16x32_bf16 v[96:99], v[128:131], a[16:19], v[96:99]\n"
"ds_read_b64_tr_b16 v[116:117], %[vT10] offset:34816\n"
"v_mfma_f32_16x16x32_bf16 v[100:103], v[128:131], a[20:23], v[100:103]\n"
"ds_read_b64_tr_b16 v[118:119], %[vT10] offset:35840\n"
"s_waitcnt lgkmcnt(2)\n"
"v_mfma_f32_16x16x32_bf16 v[32:35], v[112:115], a[0:3], v[32:35]\n"
"ds_read_b64_tr_b16 v[120:121], %[vT20] offset:34816\n"
"v_mfma_f32_16x16x32_bf16 v[36:39], v[112:115], a[4:7], v[36:39]\n"
"ds_read_b64_tr_b16 v[122:123], %[vT20] offset:35840\n"
"v_mfma_f32_16x16x32_bf16 v[40:43], v[112:115], a[8:11], v[40:43]\n"
"ds_read_b128 a[16:19], %[vTBa0] offset:35840\n"
"v_mfma_f32_16x16x32_bf16 v[44:47], v[112:115], a[12:15], v[44:47]\n"
"ds_read_b128 a[20:23], %[vTBa0] offset:37888\n"
"s_waitcnt lgkmcnt(4)\n"
"v_mfma_f32_16x16x32_bf16 v[48:51], v[116:119], a[0:3], v[48:51]\n"
"ds_read_b128 a[24:27], %[vTBb0] offset:39936\n"
"v_mfma_f32_16x16x32_bf16 v[52:55], v[116:119], a[4:7], v[52:55]\n"
"ds_read_b128 a[28:31], %[vTBb0] offset:41984\n"
"v_mfma_f32_16x16x32_bf16 v[56:59], v[116:119], a[8:11], v[56:59]\n"
"ds_read_b64_tr_b16 v[124:125], %[vT30] offset:34816\n"
"v_mfma_f32_16x16x32_bf16 v[60:63], v[116:119], a[12:15], v[60:63]\n"
"ds_read_b64_tr_b16 v[126:127], %[vT30] offset:35840\n"
"s_waitcnt lgkmcnt(6)\n"
"v_mfma_f32_16x16x32_bf16 v[64:67], v[120:123], a[0:3], v[64:67]\n"
"ds_read_b64_tr_b16 v[128:129], %[vS00] offset:34816\n"
"v_mfma_f32_16x16x32_bf16 v[68:71], v[120:123], a[4:7], v[68:71]\n"
"ds_read_b64_tr_b16 v[130:131], %[vS10] offset:34816\n"
"v_mfma_f32_16x16x32_bf16 v[72:75], v[120:123], a[8:11], v[72:75]\n"
"buffer_load_dword v140, %[vC], %[srdC], 0 offen\n"
"v_mfma_f32_16x16x32_bf16 v[76:79], v[120:123], a[12:15], v[76:79]\n"
"s_waitcnt lgkmcnt(2)\n"
"v_mfma_f32_16x16x32_bf16 v[80:83], v[124:127], a[0:3], v[80:83]\n"
"ds_read_b64_tr_b16 v[112:113], %[vT00] offset:44032\n"
"v_mfma_f32_16x16x32_bf16 v[84:87], v[124:127], a[4:7], v[84:87]\n"
"ds_read_b64_tr_b16 v[114:115], %[vT00] offset:45056\n"
"v_mfma_f32_16x16x32_bf16 v[88:91], v[124:127], a[8:11], v[88:91]\n"
"v_mfma_f32_16x16x32_bf16 v[92:95], v[124:127], a[12:15], v[92:95]\n"
"s_waitcnt lgkmcnt(2)\n"
"v_mfma_f32_16x16x32_bf16 v[96:99], v[128:131], a[0:3], v[96:99]\n"
"ds_read_b64_tr_b16 v[116:117], %[vT10] offset:44032\n"
"v_mfma_f32_16x16x32_bf16 v[100:103], v[128:131], a[4:7], v[100:103]\n"
"ds_read_b64_tr_b16 v[118:119], %[vT10] offset:45056\n"
"s_waitcnt lgkmcnt(2)\n"
"v_mfma_f32_16x16x32_bf16 v[32:35], v[112:115], a[16:19], v[32:35]\n"
"ds_read_b64_tr_b16 v[120:121], %[vT20] offset:44032\n"
"v_mfma_f32_16x16x32_bf16 v[36:39], v[112:115], a[20:23], v[36:39]\n"
"ds_read_b64_tr_b16 v[122:123], %[vT20] offset:45056\n"
"v_mfma_f32_16x16x32_bf16 v[40:43], v[112:115], a[24:27], v[40:43]\n"
"v_mfma_f32_16x16x32_bf16 v[44:47], v[112:115], a[28:31], v[44:47]\n"
"s_waitcnt lgkmcnt(2)\n"
"v_mfma_f32_16x16x32_bf16 v[48:51], v[116:119], a[16:19], v[48:51]\n"
"ds_read_b64_tr_b16 v[124:125], %[vT30] offset:44032\n"
"v_mfma_f32_16x16x32_bf16 v[52:55], v[116:119], a[20:23], v[52:55]\n"
"ds_read_b64_tr_b16 v[126:127], %[vT30] offset:45056\n"
"v_mfma_f32_16x16x32_bf16 v[56:59], v[116:119], a[24:27], v[56:59]\n"
"v_mfma_f32_16x16x32_bf16 v[60:63], v[116:119], a[28:31], v[60:63]\n"
"s_waitcnt lgkmcnt(2)\n"
"v_mfma_f32_16x16x32_bf16 v[64:67], v[120:123], a[16:19], v[64:67]\n"
"ds_read_b64_tr_b16 v[128:129], %[vS00] offset:44032\n"
"v_mfma_f32_16x16x32_bf16 v[68:71], v[120:123], a[20:23], v[68:71]\n"
"ds_read_b64_tr_b16 v[130:131], %[vS10] offset:44032\n"
"v_mfma_f32_16x16x32_bf16 v[72:75], v[120:123], a[24:27], v[72:75]\n"
"v_mfma_f32_16x16x32_bf16 v[76:79], v[120:123], a[28:31], v[76:79]\n"
"s_waitcnt lgkmcnt(2)\n"
"v_mfma_f32_16x16x32_bf16 v[80:83], v[124:127], a[16:19], v[80:83]\n"
"v_cvt_pk_bf16_f32 v144, v32, v33\n"
"v_cvt_pk_bf16_f32 v145, v34, v35\n"
"v_mfma_f32_16x16x32_bf16 v[84:87], v[124:127], a[20:23], v[84:87]\n"
"v_cvt_pk_bf16_f32 v146, v48, v49\n"
"v_cvt_pk_bf16_f32 v147, v50, v51\n"
"v_mfma_f32_16x16x32_bf16 v[88:91], v[124:127], a[24:27], v[88:91]\n"
"s_nop 1\n"
"v_permlane16_swap_b32 v144, v146\n"
"v_permlane16_swap_b32 v145, v147\n"
"s_nop 1\n"
"s_cmp_eq_u32 %[js], 0\n"
"s_cbranch_scc1 .Lsk0_%=\n"
"s_add_u32 s60, %[soffP], 0\n"
"buffer_store_dwordx4 v[144:147], %[vP], %[srdP], s60 offen sc1\n"
".Lsk0_%=:\n"
"v_mfma_f32_16x16x32_bf16 v[92:95], v[124:127], a[28:31], v[92:95]\n"
"v_cvt_pk_bf16_f32 v148, v36, v37\n"
"v_cvt_pk_bf16_f32 v149, v38, v39\n"
"s_waitcnt lgkmcnt(0)\n"
"v_mfma_f32_16x16x32_bf16 v[96:99], v[128:131], a[16:19], v[96:99]\n"
"v_cvt_pk_bf16_f32 v150, v52, v53\n"
"v_cvt_pk_bf16_f32 v151, v54, v55\n"
"v_mfma_f32_16x16x32_bf16 v[100:103], v[128:131], a[20:23], v[100:103]\n"
"s_nop 1\n"
"v_permlane16_swap_b32 v148, v150\n"
"v_permlane16_swap_b32 v149, v151\n"
"s_nop 1\n"
"s_cmp_eq_u32 %[js], 1\n"
"s_cbranch_scc1 .Lsk2_%=\n"
"s_add_u32 s60, %[soffP], 2048\n"
"buffer_store_dwordx4 v[148:151], %[vP], %[srdP], s60 offen sc1\n"
".Lsk2_%=:\n"
"v_cvt_pk_bf16_f32 v152, v40, v41\n"
"v_cvt_pk_bf16_f32 v153, v42, v43\n"
"v_cvt_pk_bf16_f32 v154, v56, v57\n"
"v_cvt_pk_bf16_f32 v155, v58, v59\n"
"s_nop 1\n"
"v_permlane16_swap_b32 v152, v154\n"
"v_permlane16_swap_b32 v153, v155\n"
"s_nop 1\n"
"s_cmp_eq_u32 %[js], 2\n"
"s_cbranch_scc1 .Lsk4_%=\n"
"s_add_u32 s60, %[soffP], 4096\n"
"buffer_store_dwordx4 v[152:155], %[vP], %[srdP], s60 offen sc1\n"
".Lsk4_%=:\n"
"v_cvt_pk_bf16_f32 v156, v44, v45\n"
"v_cvt_pk_bf16_f32 v157, v46, v47\n"
"v_cvt_pk_bf16_f32 v158, v60, v61\n"
"v_cvt_pk_bf16_f32 v159, v62, v63\n"
"s_nop 1\n"
"v_permlane16_swap_b32 v156, v158\n"
"v_permlane16_swap_b32 v157, v159\n"
"s_nop 1\n"
"s_cmp_eq_u32 %[js], 3\n"
"s_cbranch_scc1 .Lsk6_%=\n"
"s_add_u32 s60, %[soffP], 6144\n"
"buffer_store_dwordx4 v[156:159], %[vP], %[srdP], s60 offen sc1\n"
".Lsk6_%=:\n"
"s_nop 7\n"
"v_cvt_pk_bf16_f32 v160, v64, v65\n"
"v_cvt_pk_bf16_f32 v161, v66, v67\n"
"v_cvt_pk_bf16_f32 v162, v80, v81\n"
"v_cvt_pk_bf16_f32 v163, v82, v83\n"
"s_nop 1\n"
"v_permlane16_swap_b32 v160, v162\n"
"v_permlane16_swap_b32 v161, v163\n"
"s_nop 1\n"
"s_cmp_eq_u32 %[js], 0\n"
"s_cbranch_scc1 .Lsk1_%=\n"
"s_add_u32 s60, %[soffP], 1024\n"
"buffer_store_dwordx4 v[160:163], %[vP], %[srdP], s60 offen sc1\n"
".Lsk1_%=:\n"
"v_cvt_pk_bf16_f32 v164, v68, v69\n"
"v_cvt_pk_bf16_f32 v165, v70, v71\n"
"v_cvt_pk_bf16_f32 v166, v84, v85\n"
"v_cvt_pk_bf16_f32 v167, v86, v87\n"
"s_nop 1\n"
"v_permlane16_swap_b32 v164, v166\n"
"v_permlane16_swap_b32 v165, v167\n"
"s_nop 1\n"
"s_cmp_eq_u32 %[js], 1\n"
"s_cbranch_scc1 .Lsk3_%=\n"
"s_add_u32 s60, %[soffP], 3072\n"
"buffer_store_dwordx4 v[164:167], %[vP], %[srdP], s60 offen sc1\n"
".Lsk3_%=:\n"
"v_cvt_pk_bf16_f32 v168, v72, v73\n"
"v_cvt_pk_bf16_f32 v169, v74, v75\n"
"v_cvt_pk_bf16_f32 v170, v88, v89\n"
"v_cvt_pk_bf16_f32 v171, v90, v91\n"
"s_nop 1\n"
"v_permlane16_swap_b32 v168, v170\n"
"v_permlane16_swap_b32 v169, v171\n"
"s_nop 1\n"
"s_cmp_eq_u32 %[js], 2\n"
"s_cbranch_scc1 .Lsk5_%=\n"
"s_add_u32 s60, %[soffP], 5120\n"
"buffer_store_dwordx4 v[168:171], %[vP], %[srdP], s60 offen sc1\n"
".Lsk5_%=:\n"
"v_cvt_pk_bf16_f32 v172, v76, v77\n"
"v_cvt_pk_bf16_f32 v173, v78, v79\n"
"v_cvt_pk_bf16_f32 v174, v92, v93\n"
"v_cvt_pk_bf16_f32 v175, v94, v95\n"
"s_nop 1\n"
"v_permlane16_swap_b32 v172, v174\n"
"v_permlane16_swap_b32 v173, v175\n"
"s_nop 1\n"
"s_cmp_eq_u32 %[js], 3\n"
"s_cbranch_scc1 .Lsk7_%=\n"
"s_add_u32 s60, %[soffP], 7168\n"
"buffer_store_dwordx4 v[172:175], %[vP], %[srdP], s60 offen sc1\n"
".Lsk7_%=:\n"
"v_cvt_pk_bf16_f32 v184, v96, v97\n"
"v_cvt_pk_bf16_f32 v185, v98, v99\n"
"v_cvt_pk_bf16_f32 v186, v100, v101\n"
"v_cvt_pk_bf16_f32 v187, v102, v103\n"
"s_nop 1\n"
"v_permlane16_swap_b32 v184, v186\n"
"v_permlane16_swap_b32 v185, v187\n"
"s_nop 1\n"
"s_add_u32 s60, %[soffP], 8192\n"
"buffer_store_dwordx4 v[184:187], %[vP8], %[srdP], s60 offen sc1\n"
"s_cmp_lg_u32 %[js], 0\n"
"s_cbranch_scc1 .Lkp0_%=\n"
"v_mov_b32 v176, v144\n"
"v_mov_b32 v177, v145\n"
"v_mov_b32 v178, v146\n"
"v_mov_b32 v179, v147\n"
"v_mov_b32 v180, v160\n"
"v_mov_b32 v181, v161\n"
"v_mov_b32 v182, v162\n"
"v_mov_b32 v183, v163\n"
".Lkp0_%=:\n"
"s_cmp_lg_u32 %[js], 1\n"
"s_cbranch_scc1 .Lkp1_%=\n"
"v_mov_b32 v176, v148\n"
"v_mov_b32 v177, v149\n"
"v_mov_b32 v178, v150\n"
"v_mov_b32 v179, v151\n"
"v_mov_b32 v180, v164\n"
"v_mov_b32 v181, v165\n"
"v_mov_b32 v182, v166\n"
"v_mov_b32 v183, v167\n"
".Lkp1_%=:\n"
"s_cmp_lg_u32 %[js], 2\n"
"s_cbranch_scc1 .Lkp2_%=\n"
"v_mov_b32 v176, v152\n"
"v_mov_b32 v177, v153\n"
"v_mov_b32 v178, v154\n"
"v_mov_b32 v179, v155\n"
"v_mov_b32 v180, v168\n"
"v_mov_b32 v181, v169\n"
"v_mov_b32 v182, v170\n"
"v_mov_b32 v183, v171\n"
".Lkp2_%=:\n"
"s_cmp_lg_u32 %[js], 3\n"
"s_cbranch_scc1 .Lkp3_%=\n"
"v_mov_b32 v176, v156\n"
"v_mov_b32 v177, v157\n"
"v_mov_b32 v178, v158\n"
"v_mov_b32 v179, v159\n"
"v_mov_b32 v180, v172\n"
"v_mov_b32 v181, v173\n"
"v_mov_b32 v182, v174\n"
"v_mov_b32 v183, v175\n"
".Lkp3_%=:\n"
    : "=&{v[176:179]}"(xo0), "=&{v[180:183]}"(xo1), "=&{v[184:187]}"(xo2) : [srdA]"s"(srdA), [srdB]"s"(srdB), [srdP]"s"(srdP), [srdRm]"s"(srdRm), [soffRm]"s"(soffRm), [soffA]"s"(soffA), [soffA2]"s"(soffA2), [m0A]"s"(m0A), [m0A2]"s"(m0A2), [m0B]"s"(m0B), [soffB]"s"(soffB), [soffP]"s"(soffP),
       [vA0]"v"(vA0), [vA1]"v"(vA1), [vA2]"v"(vA2), [vA3]"v"(vA3), [vAh]"v"(vAh), [vB0]"v"(vB0), [vB1]"v"(vB1),
       [vT00]"v"(vT00), [vT10]"v"(vT10), [vT20]"v"(vT20), [vT30]"v"(vT30), [vT01]"v"(vT01), [vT11]"v"(vT11), [vT21]"v"(vT21), [vT31]"v"(vT31),
       [vS00]"v"(vS00), [vS10]"v"(vS10), [vS01]"v"(vS01), [vS11]"v"(vS11),
       [vTBa0]"v"(vTBa0), [vTBb0]"v"(vTBb0), [vTBa1]"v"(vTBa1), [vTBb1]"v"(vTBb1), [vP]"v"(vP), [srdF4]"s"(srdF4), [vFB]"v"(vFB), [vFBp]"v"(vFBp), [js]"s"(js), [vP8]"v"(vP8), [srdC]"s"(srdC), [vC]"v"(vC)
    : "memory", "s60", "s61", "s62", "s63", "vcc",  "scc", "v32","v33","v34","v35","v36","v37","v38","v39","v40","v41","v42","v43","v44","v45","v46","v47","v48","v49","v50","v51","v52","v53","v54","v55","v56","v57","v58","v59","v60","v61","v62","v63","v64","v65","v66","v67","v68","v69","v70","v71","v72","v73","v74","v75","v76","v77","v78","v79","v80","v81","v82","v83","v84","v85","v86","v87","v88","v89","v90","v91","v92","v93","v94","v95","v96","v97","v98","v99","v100","v101","v102","v103","v104","v105","v106","v107","v108","v109","v110","v111","v112","v113","v114","v115","v116","v117","v118","v119","v120","v121","v122","v123","v124","v125","v126","v127","v128","v129","v130","v131","v132","v133","v134","v135","v136","v137","v138","v139","v140","v141","v142","v143","v144","v145","v146","v147","v148","v149","v150","v151","v152","v153","v154","v155","v156","v157","v158","v159","v160","v161","v162","v163","v164","v165","v166","v167","v168","v169","v170","v171","v172","v173","v174","v175", "a0","a1","a2","a3","a4","a5","a6","a7","a8","a9","a10","a11","a12","a13","a14","a15","a16","a17","a18","a19","a20","a21","a22","a23","a24","a25","a26","a27","a28","a29","a30","a31");
  asm volatile("kc5e:");
  CODE_LABEL("kc6b");
  {
    unsigned b_ = b, w_ = w, lt = lane_id();
    asm volatile("" : "+s"(b_), "+s"(w_), "+v"(lt));
    const unsigned w = w_, rt = 8u * (b_ & 1u) + ((b_ >> 3) & 7u), kq = (b_ & 7u) >> 1, ct = b_ >> 6, ch = w >> 1;
    const unsigned js = (kq + 2u * ch) & 3u, blk = rt * 16u + ct * 4u + kq;
    const unsigned long long pb = ((unsigned long long)kb[1] << 32) | kb[0];
    const unsigned long long hb = pb + (9u << 20), fb = pb + 0xB80000u;
    const u32x4 srdH = srd_of((unsigned)hb, (unsigned)(hb >> 32), 2048u * 576u * 2u);
    const u32x4 srdF = srd_of((unsigned)fb, (unsigned)(fb >> 32), 256u * 64u);
    const u32x4 srdQ = srd_of(kb[0], kb[1], 4u * 2048u * 576u * 2u);
    const unsigned vL = lt * 16u;
    const bool own8 = ch == (kq >> 1) && ((lt >> 4) & 1u) == (kq & 1u);
    const unsigned v8 = own8 ? vL : 0x7fffffffu;
    const unsigned sT = ((rt * 4u + ct) * 4u + w) * 9216u, sH = sT + js * 2048u, sH8 = sT + 8192u;
    u32x4 h0, h1, h2;
    asm volatile("buffer_load_dwordx4 %0, %1, %2, %3 offen" : "=&v"(h0) : "v"(vL), "s"(srdH), "s"(sH) : "memory");
    asm volatile("buffer_load_dwordx4 %0, %1, %2, %3 offen offset:1024" : "=&v"(h1) : "v"(vL), "s"(srdH), "s"(sH) : "memory");
    asm volatile("buffer_load_dwordx4 %0, %1, %2, %3 offen" : "=&v"(h2) : "v"(v8), "s"(srdH), "s"(sH8) : "memory");
    unsigned sQ[3], sQ8[3];
#pragma unroll
    for (unsigned k = 0; k < 3; ++k) { const unsigned o = ((kq + 1u + k) & 3u) * 2359296u; sQ[k] = o + sH; sQ8[k] = o + sH8; }
    const unsigned fo = (lt & 3u) == 0u ? 1u : (lt & 3u);
    const unsigned vFs = lt == 0u ? blk * 64u : 0x7fffffffu, vFp = (rt * 16u + ct * 4u + ((kq + fo) & 3u)) * 64u;
    const unsigned tb = lds + 69632u + w * 2816u;
    const unsigned tw = tb + (lt & 15u) * 176u + ((lt >> 4) & 1u) * 32u + (lt >> 5) * 16u;
    const unsigned tw8 = tb + (lt & 15u) * 176u + (own8 ? 128u + (lt >> 5) * 16u : 160u);
    const unsigned tr = tb + (lt >> 3) * 176u + (lt & 7u) * 16u, tr8 = tb + ((lt >> 1) & 15u) * 176u + 128u + (lt & 1u) * 16u;
    const unsigned vO = (lt >> 3) * 1152u + 256u * ct + 128u * ch + (lt & 7u) * 16u;
    const unsigned vO8 = (ch == (kq >> 1) && lt < 32u) ? (lt >> 1) * 1152u + 1024u + 32u * ct + (lt & 1u) * 16u : 0x7fffffffu;
    const u32x4 srdO = srd_of(kb[2], kb[3], 2048u * 576u * 2u);
    const unsigned sO = (128u * rt + 64u * (w & 1u) + 16u * kq) * 1152u, sO2 = sO + 8u * 1152u;
    asm volatile("s_waitcnt vmcnt(0)\n s_barrier" : "+v"(h0), "+v"(h1), "+v"(h2) : "v"(vL), "v"(v8), "v"(vFs), "v"(vFp), "v"(tw), "v"(tw8), "v"(tr), "v"(tr8), "v"(vO), "v"(vO8),
"s"(sQ[0]), "s"(sQ[1]), "s"(sQ[2]), "s"(sQ8[0]), "s"(sQ8[1]), "s"(sQ8[2]), "s"(sO), "s"(sO2), "s"(srdF), "s"(srdO) : "memory");
    if (w == 3u) {
      asm volatile("buffer_store_dword %0, %1, %2, 0 offen sc1" :: "v"(1u), "v"(vFs), "s"(srdF) : "memory");
      for (unsigned it = 0; it < 8192u; ++it) {
        unsigned fv;
        asm volatile("buffer_load_dword %0, %1, %2, 0 offen sc1\n s_waitcnt vmcnt(0)" : "=&v"(fv) : "v"(vFp), "s"(srdF) : "memory");
        if (__builtin_amdgcn_ballot_w64(fv != 1u) == 0ull) break;
      }
    }
    asm volatile("s_barrier" ::: "memory");
    {
      u32x4 p[3][3];
#pragma unroll
      for (unsigned k = 0; k < 3; ++k) asm volatile("buffer_load_dwordx4 %0, %1, %2, %3 offen sc1" : "=&v"(p[0][k]) : "v"(vL), "s"(srdQ), "s"(sQ[k]) : "memory");
#pragma unroll
      for (unsigned k = 0; k < 3; ++k) asm volatile("buffer_load_dwordx4 %0, %1, %2, %3 offen offset:1024 sc1" : "=&v"(p[1][k]) : "v"(vL), "s"(srdQ), "s"(sQ[k]) : "memory");
#pragma unroll
      for (unsigned k = 0; k < 3; ++k) asm volatile("buffer_load_dwordx4 %0, %1, %2, %3 offen sc1" : "=&v"(p[2][k]) : "v"(v8), "s"(srdQ), "s"(sQ8[k]) : "memory");
#pragma unroll
      for (unsigned j = 0; j < 3; ++j) {
        if (j == 0) asm volatile("s_waitcnt vmcnt(6)" : "+v"(p[0][0]), "+v"(p[0][1]), "+v"(p[0][2]) :: "memory");
        if (j == 1) asm volatile("s_waitcnt vmcnt(3)" : "+v"(p[1][0]), "+v"(p[1][1]), "+v"(p[1][2]) :: "memory");
        if (j == 2) asm volatile("s_waitcnt vmcnt(0)" : "+v"(p[2][0]), "+v"(p[2][1]), "+v"(p[2][2]) :: "memory");
        if (j == 2) { }
        const u32x4 hh = j == 0 ? h0 : (j == 1 ? h1 : h2), ow = j == 0 ? xo0 : (j == 1 ? xo1 : xo2);
        u32x4 o;
#pragma unroll
        for (int i = 0; i < 4; ++i) {
          const float a = bf_lo(hh[i]) + ((bf_lo(ow[i]) + bf_lo(p[j][0][i])) + (bf_lo(p[j][1][i]) + bf_lo(p[j][2][i])));
          const float c = bf_hi(hh[i]) + ((bf_hi(ow[i]) + bf_hi(p[j][0][i])) + (bf_hi(p[j][1][i]) + bf_hi(p[j][2][i])));
          o[i] = pk_bf16(a, c);
        }
        if (j == 0) asm volatile("ds_write_b128 %0, %1" :: "v"(tw), "v"(o) : "memory");
        if (j == 1) asm volatile("ds_write_b128 %0, %1 offset:64" :: "v"(tw), "v"(o) : "memory");
        if (j == 2) asm volatile("ds_write_b128 %0, %1" :: "v"(tw8), "v"(o) : "memory");
        __builtin_amdgcn_sched_barrier(0);
      }
      u32x4 q0, q1, q2;
      asm volatile("ds_read_b128 %0, %3\n ds_read_b128 %1, %3 offset:1408\n ds_read_b128 %2, %4\n s_waitcnt lgkmcnt(0)" : "=&v"(q0), "=&v"(q1), "=&v"(q2) : "v"(tr), "v"(tr8) : "memory");
      asm volatile("buffer_store_dwordx4 %0, %1, %2, %3 offen\n s_nop 1" :: "v"(q0), "v"(vO), "s"(srdO), "s"(sO) : "memory");
      asm volatile("buffer_store_dwordx4 %0, %1, %2, %3 offen\n s_nop 1" :: "v"(q1), "v"(vO), "s"(srdO), "s"(sO2) : "memory");
      asm volatile("buffer_store_dwordx4 %0, %1, %2, %3 offen\n s_nop 1" :: "v"(q2), "v"(vO8), "s"(srdO), "s"(sO) : "memory");
    }
  }
  asm volatile("kc6e:");
  }
}

extern "C" void kernel_launch(void* const* d_in, const int* in_sizes, int n_in, void* d_out, int out_size, void* d_ws, size_t ws_size, hipStream_t stream) {
  const u16* x = (const u16*)d_in[0];
  const u16* cr = (const u16*)d_in[1];
  const u16* ci = (const u16*)d_in[2];
  const u16* wre = (const u16*)d_in[3];
  const u16* wim = (const u16*)d_in[4];
  const u16* wg = (const u16*)d_in[6];
  const u16* wu = (const u16*)d_in[7];
  const u16* wd = (const u16*)d_in[8];
  char* ws = (char*)d_ws;
  u16* abF = (u16*)ws;
  u16* part = (u16*)(ws + (5u << 20));
  u16* hT = (u16*)(ws + (14u << 20));
  u16* hnF = (u16*)(ws + (17u << 20));
  u16* actF = (u16*)(ws + (20u << 20));
  u16* wdF = (u16*)(ws + (38u << 20));
  k12_spectral<<<256, 320, 0, stream>>>(x, wre, wim, abF, cr, ci, part, wg, wu, wd, x);

  k45_ffn<<<256, 256, 0, stream>>>(hnF, wg, wu, actF, wd, part, (u16*)d_out);


}
```
